```python
import torch

def _softmax(x, axis=-1):
    return torch.softmax(x, dim=axis)

def _gelu(x):
    return torch.nn.functional.gelu(x, approximate='tanh')

def _silu(x):
    return torch.nn.functional.silu(x)

def _sigmoid(x):
    return torch.sigmoid(x)

def _layernorm(x, axis=-1, eps=1e-05):
    m = x.mean(dim=axis, keepdim=True)
    v = x.var(dim=axis, keepdim=True, unbiased=False)
    return (x - m) / torch.sqrt(v + eps)

def _scatter_add(tgt, idx, src, axis=0):
    return tgt.clone().index_add_(axis, idx, src)

def _segment_sum(src, seg, n):
    out = torch.zeros((n,) + src.shape[1:], dtype=src.dtype)
    return out.index_add_(0, seg, src)

def _pad(x, pw):
    flat = tuple((v for pair in reversed(pw) for v in pair))
    return torch.nn.functional.pad(x, flat)
kernel_name = 'sequential_preLN__memory_aug_recurrent_mem_tr__mixer_s_mlp__prompt'

def setup_inputs():

    _w = lambda a: (lambda t: t.to(torch.bfloat16) if t.is_floating_point() else t)(torch.as_tensor(a))

    def _A_setup():
        B, S, Dm = (1, 2048, 640)
        H, Dh = (32, 192)
        M, seg = (8, 32)
        seg = min(seg, S)
        S = S // seg * seg
        return {'x': _w((lambda *s: torch.randn(*s, dtype=torch.bfloat16) * 0.05)(B, S, Dm)), 'mem0': _w((lambda *s: torch.randn(*s, dtype=torch.bfloat16) * 0.2)(B, M, Dm)), 'Wq': _w((lambda *s: torch.randn(*s, dtype=torch.bfloat16) * 1.6)(Dm, H, Dh) * Dm ** (-0.5)), 'Wk': _w((lambda *s: torch.randn(*s, dtype=torch.bfloat16) * 1.6)(Dm, H, Dh) * Dm ** (-0.5)), 'Wv': _w((lambda *s: torch.randn(*s, dtype=torch.bfloat16) * 0.2)(Dm, H, Dh) * Dm ** (-0.5)), 'Wo': _w((lambda *s: torch.randn(*s, dtype=torch.bfloat16) * 0.2)(H, Dh, Dm) * (H * Dh) ** (-0.5)), 'seg_len': seg}
    _A_in = _A_setup()

    def _B_setup():
        B, S, D, F = (1, 2048, 640, 1536)
        return {'x': _w((lambda *s: torch.randn(*s, dtype=torch.bfloat16) * 0.05)(B, S, D)), 'w1': _w((lambda *s: torch.randn(*s, dtype=torch.bfloat16) * 0.8)(D, D) * D ** (-0.5)), 'w2': _w((lambda *s: torch.randn(*s, dtype=torch.bfloat16) * 0.8)(D, D) * D ** (-0.5)), 'w3': _w((lambda *s: torch.randn(*s, dtype=torch.bfloat16) * 0.8)(D, F) * D ** (-0.5)), 'w4': _w((lambda *s: torch.randn(*s, dtype=torch.bfloat16) * 0.8)(F, D) * F ** (-0.5))}
    _B_in = _B_setup()
    return {'A_x': _A_in['x'], 'A_mem0': _A_in['mem0'], 'A_Wq': _A_in['Wq'], 'A_Wk': _A_in['Wk'], 'A_Wv': _A_in['Wv'], 'A_Wo': _A_in['Wo'], 'A_seg_len': _A_in['seg_len'], 'B_x': _B_in['x'], 'B_w1': _B_in['w1'], 'B_w2': _B_in['w2'], 'B_w3': _B_in['w3'], 'B_w4': _B_in['w4']}

def reference(A_x, A_mem0, A_Wq, A_Wk, A_Wv, A_Wo, A_seg_len, B_x, B_w1, B_w2, B_w3, B_w4):

    def _A_ref(x, mem0, Wq, Wk, Wv, Wo, seg_len):
        B, S, Dm = x.shape
        H, Dh = (Wq.shape[1], Wq.shape[2])
        M = mem0.shape[1]
        scale = Dh ** (-0.5)
        n_seg = S // seg_len
        L = M + seg_len
        idx = torch.arange(L)
        mask = (idx[:, None] < M) | (idx[:, None] >= idx[None, :])
        mem = mem0
        outs = []
        for s in range(n_seg):
            xs = x[:, s * seg_len:(s + 1) * seg_len, :].clone()
            h = torch.concatenate([mem, xs], axis=1)
            q = torch.einsum('btd,dhe->bhte', h, Wq)
            k = torch.einsum('btd,dhe->bhte', h, Wk)
            v = torch.einsum('btd,dhe->bhte', h, Wv)
            scores = torch.einsum('bhqe,bhke->bhqk', q, k) * scale
            scores = torch.where(mask[None, None], scores, -1000000000.0)
            p = _softmax(scores, axis=-1)
            o = torch.einsum('bhqk,bhke->bhqe', p, v)
            o = torch.einsum('bhte,hed->btd', o, Wo)
            mem = o[:, :M, :].clone()
            outs.append(o[:, M:, :])
        return torch.concatenate(outs, axis=1)

    def _B_ref(x, w1, w2, w3, w4):
        B, S, D = x.shape
        h = _gelu(_layernorm(x, axis=-1) @ w1)
        c = D // 4
        g0 = _pad(h[:, :, 0 * c:1 * c], ((0, 0), (1, 0), (0, 0)))[:, :S, :]
        g1 = _pad(h[:, :, 1 * c:2 * c], ((0, 0), (0, 1), (0, 0)))[:, 1:, :]
        g2 = _pad(h[:, :, 2 * c:3 * c], ((0, 0), (2, 0), (0, 0)))[:, :S, :]
        g3 = _pad(h[:, :, 3 * c:], ((0, 0), (0, 2), (0, 0)))[:, 2:, :]
        hs = torch.concatenate([g0, g1, g2, g3], axis=-1) @ w2
        x1 = x + hs
        h2 = _gelu(_layernorm(x1, axis=-1) @ w3) @ w4
        return x1 + h2
    x = A_x
    h = x + _A_ref(_layernorm(x, axis=-1), A_mem0, A_Wq, A_Wk, A_Wv, A_Wo, A_seg_len)
    h2 = h + _B_ref(_layernorm(h, axis=-1), B_w1, B_w2, B_w3, B_w4)
    return h2
```

```cpp
#include <hip/hip_runtime.h>
#include <stdint.h>

#define GAS __attribute__((address_space(1)))
#define LAS __attribute__((address_space(3)))
#define RLX_AGENT __ATOMIC_RELAXED, __HIP_MEMORY_SCOPE_AGENT
#define DEV __device__ __forceinline__
typedef __bf16 bf16x2 __attribute__((ext_vector_type(2)));
typedef __bf16 bf16x8 __attribute__((ext_vector_type(8)));
typedef __bf16 bf16x16 __attribute__((ext_vector_type(16)));
typedef short s16x4 __attribute__((ext_vector_type(4)));
typedef float f32x2 __attribute__((ext_vector_type(2)));
typedef float f32x4 __attribute__((ext_vector_type(4)));
typedef unsigned u32x2 __attribute__((ext_vector_type(2)));
typedef unsigned u32x4 __attribute__((ext_vector_type(4)));
typedef unsigned u32x8 __attribute__((ext_vector_type(8)));
typedef GAS unsigned gu32;
typedef __amdgpu_buffer_rsrc_t rsrc_t;

constexpr int S_ = 2048, D_ = 640, H_ = 32, E_ = 192, F_ = 1536, NSEG = 64, NUNIT = 128;
constexpr unsigned SENT = 0xFFFFFFFFu;
constexpr unsigned SPINCAP = 1u << 16;

constexpr unsigned REC_B = 1u << 20;
constexpr unsigned R_MEM = 0;
constexpr unsigned R_P = 2 * 8192;
constexpr unsigned R_UB = 5 * 8192;
constexpr unsigned R_PX = 45 * 8192;
constexpr unsigned PX_B = 40960;
constexpr unsigned R_NF = 55 * 8192;
constexpr unsigned R_NS = 56 * 8192;
constexpr int REC_BLOCKS = 57;
#define DELTA_A 20
#define DELTA_B 25
#define VIS 4
#define SLOW 120
#define REDO 150
#define PACE 10
constexpr unsigned amap(unsigned off) { return ((off >> 13) << 14) | (off & 8191u); }
constexpr int KARM = 4;
constexpr size_t O_CTRL = (size_t)NSEG * REC_B, CTRL_BYTES = 65536;
constexpr int W_TMO = 0, W_TICKET = 64  , W_QKPROG = 1024  , W_VCPROG = 4096  ;
constexpr size_t O_PAR = O_CTRL + CTRL_BYTES;
constexpr size_t O_ST = O_PAR + 4096;
constexpr size_t O_HF = O_ST + S_ * 8, XF_B = 20480;
constexpr size_t O_GF = O_HF + NUNIT * XF_B;
constexpr size_t O_X1F = O_GF + NUNIT * XF_B;
constexpr size_t O_G3F = O_X1F + NUNIT * XF_B, G3_B = 49152;
constexpr size_t O_XF = O_G3F + NUNIT * G3_B;
constexpr size_t O_QX = O_XF + NUNIT * XF_B, QX_B = 12288;
constexpr size_t O_KX = O_QX + (size_t)NSEG * H_ * QX_B;
constexpr size_t O_CB = O_KX + (size_t)NSEG * H_ * QX_B, CB_B = 40 * 32768;
constexpr size_t WS_END = O_CB + (size_t)NSEG * CB_B;
static_assert(WS_END < (238u << 20), "workspace");
constexpr int LDS_BYTES = 160 * 1024;

struct Params {
    const unsigned short *x, *mem0, *wq, *wk, *wv, *wo, *w1, *w2, *w3, *w4;
    unsigned short* out;
    unsigned char* ws;
};

DEV unsigned xcc_id() { return (unsigned)__builtin_amdgcn_s_getreg(20 | (3 << 11)) & 0xfu; }
DEV rsrc_t mk_rsrc(const void* p, unsigned bytes) { return __builtin_amdgcn_make_buffer_rsrc((void*)p, 0, bytes, 0x00020000); }
DEV u32x4 ld128(rsrc_t r, unsigned v, unsigned s) { return __builtin_amdgcn_raw_buffer_load_b128(r, v, s, 16); }
DEV u32x2 ld64(rsrc_t r, unsigned v, unsigned s) { return __builtin_amdgcn_raw_buffer_load_b64(r, v, s, 16); }
DEV unsigned ld32(rsrc_t r, unsigned v, unsigned s) { return __builtin_amdgcn_raw_buffer_load_b32(r, v, s, 16); }
DEV void st128(u32x4 d, rsrc_t r, unsigned v, unsigned s) { __builtin_amdgcn_raw_buffer_store_b128(d, r, v, s, 16); }
DEV void st64(u32x2 d, rsrc_t r, unsigned v, unsigned s) { __builtin_amdgcn_raw_buffer_store_b64(d, r, v, s, 16); }
DEV void st32(unsigned d, rsrc_t r, unsigned v, unsigned s) { __builtin_amdgcn_raw_buffer_store_b32(d, r, v, s, 16); }
DEV void st16(unsigned short d, rsrc_t r, unsigned v, unsigned s) { __builtin_amdgcn_raw_buffer_store_b16(d, r, v, s, 16); }
DEV u32x2 trd(LAS const char* p) { return __builtin_bit_cast(u32x2, __builtin_amdgcn_ds_read_tr16_b64_v4i16((LAS s16x4*)p)); }
DEV u32x4 cat2(u32x2 a, u32x2 b) { return __builtin_shufflevector(a, b, 0, 1, 2, 3); }
DEV u32x8 cat4(u32x4 a, u32x4 b) { return __builtin_shufflevector(a, b, 0, 1, 2, 3, 4, 5, 6, 7); }
DEV u32x4 lo4(u32x8 a) { return __builtin_shufflevector(a, a, 0, 1, 2, 3); }
DEV u32x4 hi4(u32x8 a) { return __builtin_shufflevector(a, a, 4, 5, 6, 7); }
DEV u32x4 lds128(LAS const char* p) { return *(LAS const u32x4*)p; }
DEV unsigned pk(float a, float b) { bf16x2 v = {(__bf16)a, (__bf16)b}; return __builtin_bit_cast(unsigned, v); }
DEV unsigned nosent(unsigned u) { return min(u, SENT - 1u); }
DEV unsigned pks(float a, float b) { return min(pk(a, b), SENT - 1u); }
DEV unsigned pka(float a, float b) { unsigned r; asm("v_cvt_pk_bf16_f32 %0, |%1|, |%2|" : "=v"(r) : "v"(a), "v"(b)); return r; }
DEV float bflo(unsigned u) { return __builtin_bit_cast(float, u << 16); }
DEV float bfhi(unsigned u) { return __builtin_bit_cast(float, u & 0xFFFF0000u); }
DEV unsigned f2u(float f) { return __builtin_bit_cast(unsigned, f); }
DEV float u2f(unsigned u) { return __builtin_bit_cast(float, u); }
DEV unsigned mx4(u32x4 v) { return max(max(v.x, v.y), max(v.z, v.w)); }
DEV bool any_lane(bool c) { return __builtin_amdgcn_ballot_w64(c) != 0; }
DEV void bar() { asm volatile("s_waitcnt lgkmcnt(0)" ::: "memory"); __builtin_amdgcn_s_barrier(); asm volatile("" ::: "memory"); }
DEV void fold32(float& a, float& b) {
    auto r = __builtin_amdgcn_permlane32_swap(f2u(a), f2u(b), false, false); a = u2f(r[0]); b = u2f(r[1]);
}
DEV float x32(float v, bool mx) { auto r = __builtin_amdgcn_permlane32_swap(f2u(v), f2u(v), false, false); return mx ? fmaxf(u2f(r[0]), u2f(r[1])) : u2f(r[0]) + u2f(r[1]); }
DEV float x16(float v, bool mx) { auto r = __builtin_amdgcn_permlane16_swap(f2u(v), f2u(v), false, false); return mx ? fmaxf(u2f(r[0]), u2f(r[1])) : u2f(r[0]) + u2f(r[1]); }
DEV float fmax2(float a, float b) { float r; asm("v_max_f32 %0, %1, %2" : "=v"(r) : "v"(a), "v"(b)); return r; }
DEV float fmax3(float a, float b, float c) { float r; asm("v_max3_f32 %0, %1, %2, %3" : "=v"(r) : "v"(a), "v"(b), "v"(c)); return r; }
#define ROWOP2(op) asm volatile("s_nop 1\n\t" \
    op " %0, %0, %0 row_ror:8 row_mask:0xf bank_mask:0xf\n\t" op " %1, %1, %1 row_ror:8 row_mask:0xf bank_mask:0xf\n\ts_nop 0\n\t" \
    op " %0, %0, %0 row_ror:4 row_mask:0xf bank_mask:0xf\n\t" op " %1, %1, %1 row_ror:4 row_mask:0xf bank_mask:0xf\n\ts_nop 0\n\t" \
    op " %0, %0, %0 row_ror:2 row_mask:0xf bank_mask:0xf\n\t" op " %1, %1, %1 row_ror:2 row_mask:0xf bank_mask:0xf\n\ts_nop 0\n\t" \
    op " %0, %0, %0 row_ror:1 row_mask:0xf bank_mask:0xf\n\t" op " %1, %1, %1 row_ror:1 row_mask:0xf bank_mask:0xf" : "+v"(a), "+v"(b))
DEV void row_max2(float& a, float& b) { ROWOP2("v_max_f32_dpp"); }
DEV void row_sum2(float& a, float& b) { ROWOP2("v_add_f32_dpp"); }
DEV unsigned pair_cols(unsigned p, unsigned psel) { return __builtin_amdgcn_perm(p, __builtin_amdgcn_update_dpp(0u, p, 0xB1, 0xf, 0xf, true), psel); }
DEV float ror8(float v) { return u2f(__builtin_amdgcn_update_dpp(0u, f2u(v), 0x128, 0xf, 0xf, true)); }
DEV void glds(unsigned voff, const void* sbase, unsigned lds_dst) {
    unsigned keep;
    asm volatile("s_mov_b32 %0, m0\n\ts_mov_b32 m0, %3\n\ts_nop 0\n\tglobal_load_lds_dwordx4 %1, %2\n\ts_mov_b32 m0, %0" : "=&s"(keep) : "v"(voff), "s"(sbase), "s"(lds_dst) : "memory");
}
template <int OFF> DEV void glds_dev(unsigned voff, const void* sbase, unsigned lds_dst) {
    unsigned keep;
    asm volatile("s_mov_b32 %0, m0\n\ts_mov_b32 m0, %3\n\ts_nop 0\n\tglobal_load_lds_dwordx4 %1, %2 offset:%4 sc1\n\ts_mov_b32 m0, %0" : "=&s"(keep) : "v"(voff), "s"(sbase), "s"(lds_dst), "n"(OFF) : "memory");
}
template <int N> DEV void vmwait() { asm volatile("s_waitcnt vmcnt(%0)" :: "n"(N) : "memory"); }
template <class T> DEV void pin_a(T& v) { asm volatile("" : "+a"(v)); }
template <class T> DEV void pin_v(T& v) { asm volatile("" : "+v"(v)); }
#define MF_ "v_mfma_f32_16x16x32_bf16 "
DEV void smf_a(f32x4& c, u32x4 a, u32x8 b, int idx) { asm volatile("v_smfmac_f32_16x16x64_bf16 %0, %1, %2, %3" : "+v"(c) : "v"(a), "a"(b), "v"(idx)); }
DEV void smf_v(f32x4& c, u32x4 a, u32x8 b, int idx) { asm volatile("v_smfmac_f32_16x16x64_bf16 %0, %1, %2, %3" : "+v"(c) : "v"(a), "v"(b), "v"(idx)); }
DEV void mf_vv(f32x4& c, u32x4 a, u32x4 b, bool first) { if (first) asm volatile(MF_ "%0, %1, %2, 0" : "=&v"(c) : "v"(a), "v"(b)); else asm volatile(MF_ "%0, %1, %2, %0" : "+v"(c) : "v"(a), "v"(b)); }
DEV void mf_av(f32x4& c, u32x4 a, u32x4 b, bool first) { if (first) asm volatile(MF_ "%0, %1, %2, 0" : "=&v"(c) : "a"(a), "v"(b)); else asm volatile(MF_ "%0, %1, %2, %0" : "+v"(c) : "a"(a), "v"(b)); }
DEV void mf_va(f32x4& c, u32x4 a, u32x4 b, bool first) { if (first) asm volatile(MF_ "%0, %1, %2, 0" : "=&v"(c) : "v"(a), "a"(b)); else asm volatile(MF_ "%0, %1, %2, %0" : "+v"(c) : "v"(a), "a"(b)); }
DEV void mf_done(f32x4& a) { asm volatile("s_nop 7" : "+v"(a)); }
DEV void mf_done(f32x4& a, f32x4& b) { asm volatile("s_nop 7" : "+v"(a), "+v"(b)); }
DEV void mf_done(f32x4& a, f32x4& b, f32x4& c) { asm volatile("s_nop 7" : "+v"(a), "+v"(b), "+v"(c)); }
DEV void mf_done(f32x4& a, f32x4& b, f32x4& c, f32x4& d) { asm volatile("s_nop 7" : "+v"(a), "+v"(b), "+v"(c), "+v"(d)); }
DEV float zero1() { float v; asm volatile("v_mov_b32 %0, 0" : "=v"(v)); return v; }
DEV void zero_acc(f32x4& a) { a = f32x4{zero1(), zero1(), zero1(), zero1()}; asm volatile("s_nop 1" : "+v"(a)); }
DEV LAS char* opq(LAS char* p) { unsigned u = (unsigned)(uintptr_t)p; asm volatile("" : "+v"(u)); return (LAS char*)(uintptr_t)u; }
template <int V> struct IC { static constexpr int value = V; constexpr operator int() const { return V; } };
template <int I, int N, class F> DEV void sfor(F&& f) { if constexpr (I < N) { f(IC<I>{}); sfor<I + 1, N>(f); } }
DEV void lgkm0() { asm volatile("s_waitcnt lgkmcnt(0)" ::: "memory"); __builtin_amdgcn_sched_barrier(0); }

#define MARK(i)
#define MARK2(i)
#define SETV(i, v)
#define MARKC(i)
#define FLUSH(step)
struct Spin {
    gu32* tmo; bool dead;
    DEV bool fail(unsigned& n, unsigned code) {
        asm volatile("" ::: "memory");
        if (dead) return true;
        ++n;
        if (n > SPINCAP) { __hip_atomic_store(tmo, code, RLX_AGENT); dead = true; return true; }
        if ((n & 63u) == 0u && __builtin_amdgcn_readfirstlane(__hip_atomic_load(tmo, RLX_AGENT)) != 0) { dead = true; return true; }
        return false;
    }
};

template <int NT> DEV int img_rot(int rho) { return (NT & 1) ? 0 : (NT & 2) ? ((rho >> 2) & 1) : (NT & 4) ? ((rho >> 1) & 3) : (rho & 7); }
DEV int swap23(int r) { return (r & 3) | ((r >> 1) & 4) | ((r << 1) & 8) | (r & 16); }
template <int NT> struct WSrc {
    unsigned voff[NT];
    DEV void init(int lane, unsigned stride, int trot = 0) {
#pragma unroll
        for (int i = 0; i < NT; ++i) {
            const int P = 64 * i + lane, rho = P / (2 * NT), cs = P % (2 * NT);
            const int tile = (((cs >> 1) ^ img_rot<NT>(rho)) + trot) % NT;
            voff[i] = (unsigned)swap23(rho) * stride + (unsigned)(2 * tile + (cs & 1)) * 16u;
        }
    }
    DEV void issue(const char* src, unsigned lds_dst) {
#pragma unroll
        for (int i = 0; i < NT; ++i) glds(voff[i], src, lds_dst + i * 1024);
    }
};
template <int NT, bool PAIR> DEV u32x4 wfrag(LAS const char* slot, int lane, int tile) {
    const int g = lane >> 4, q = (lane >> 2) & 3, p = lane & 3;
    u32x2 r[2];
#pragma unroll
    for (int u = 0; u < 2; ++u) {
        const int rho = q | ((g & 1) << 2) | (u << 3) | ((g >> 1) << 4);
        const int sl = PAIR ? ((tile & ~1) + (p >> 1)) : tile;
        const int in = PAIR ? (16 * (p & 1) + 8 * (tile & 1)) : 8 * p;
        r[u] = trd(slot + rho * (NT * 32) + ((sl ^ img_rot<NT>(rho)) * 32) + in);
    }
    return cat2(r[0], r[1]);
}
DEV void vmwait_n(int n) {
    switch (n) {
#define VW(n) case n: vmwait<n>(); break;
        VW(0) VW(3) VW(4) VW(6) VW(8) VW(9) VW(10) VW(12) VW(15) VW(16) VW(18) VW(20) VW(24) VW(28) VW(30) VW(32) VW(35) VW(36) VW(40)
#undef VW
        default: vmwait<0>(); break;
    }
}
template <int NT, int NK, int NKT, int RING, bool PAIR, class SlotOff, class Sink>
DEV void stream_w(const char* src, unsigned stride, LAS char* lds0, int lane, SlotOff&& so, Sink&& sink, int rot = 0, int trot = 0) {
    auto krow = [&](int k) { int kk = k + 2 * rot; if (kk >= NKT) kk -= NKT; return (size_t)kk * 32 * stride; };
    WSrc<NT> ws; ws.init(lane, stride, trot);
    const unsigned lbase = (unsigned)(uintptr_t)lds0;
#pragma unroll
    for (int k = 0; k < RING && k < NKT; ++k) ws.issue(src + krow(k), lbase + so(k % RING));
#pragma unroll
    for (int k = 0; k < NK; ++k) {
        const int issued = (k + RING < NKT) ? k + RING : NKT;
        vmwait_n((issued - k - 1) * NT);
        u32x4 fr[NT];
#pragma unroll
        for (int t = 0; t < NT; ++t) fr[t] = wfrag<NT, PAIR>(lds0 + so(k % RING), lane, t);
#pragma unroll
        for (int t = 0; t < NT; ++t) sink(t, k, fr[t]);
        if (k + RING < NKT) { lgkm0(); ws.issue(src + krow(k + RING), lbase + so(k % RING)); }
    }
}

DEV unsigned rec(unsigned par, int s) { return (unsigned)s * REC_B + (((par >> (s >> 1)) & 1u) << 13); }
DEV unsigned load_par(rsrc_t rw, int lane) { return (unsigned)__builtin_amdgcn_ballot_w64(lane < 32 && __builtin_amdgcn_raw_buffer_load_b32(rw, (lane & 31) * 4, (unsigned)O_PAR, 0) == 1u); }
DEV unsigned sent1() { unsigned v; asm volatile("v_mov_b32 %0, -1" : "=v"(v)); return v; }
#define SENT4 (u32x4{sent1(), sent1(), sent1(), sent1()})
#define SENT2 (u32x2{sent1(), sent1()})
DEV unsigned rawnow() { unsigned long long v; asm volatile("s_memrealtime %0\n\ts_waitcnt lgkmcnt(0)" : "=s"(v) :: "memory"); return (unsigned)v; }
struct Clock {
    unsigned org;
    DEV void init(unsigned char* w) { org = __builtin_amdgcn_readfirstlane(((const unsigned*)(w + O_PAR))[32]); }
    DEV unsigned now() const { return rawnow() - org; }
    DEV unsigned wait_until(unsigned T) const { unsigned t = now(); for (int g = 0; g < 300 && (int)(t - T) < 0; ++g) t = now(); return t; }
};
DEV unsigned umax_dpp(unsigned v, int k) {
    switch (k) { case 1: return max(v, (unsigned)__builtin_amdgcn_update_dpp(0u, v, 0x121, 0xf, 0xf, true)); case 2: return max(v, (unsigned)__builtin_amdgcn_update_dpp(0u, v, 0x122, 0xf, 0xf, true));
                 case 4: return max(v, (unsigned)__builtin_amdgcn_update_dpp(0u, v, 0x124, 0xf, 0xf, true)); default: return max(v, (unsigned)__builtin_amdgcn_update_dpp(0u, v, 0x128, 0xf, 0xf, true)); }
}
DEV unsigned half_max(unsigned v) {
    v = umax_dpp(umax_dpp(umax_dpp(umax_dpp(v, 1), 2), 4), 8);
    auto r = __builtin_amdgcn_permlane16_swap(v, v, false, false);
    return max((unsigned)r[0], (unsigned)r[1]);
}
DEV unsigned wave_max(unsigned v) { const unsigned m = half_max(v); return max((unsigned)__builtin_amdgcn_readlane(m, 0), (unsigned)__builtin_amdgcn_readlane(m, 32)); }
DEV u32x2 read_notes(u32x2 nf) {
    const unsigned tp = wave_max(nf.x == SENT ? 0u : nf.x), df = wave_max(nf.y == SENT ? 0u : min(nf.y, 400u));
    return u32x2{tp, tp + df + DELTA_B};
}
DEV unsigned mem_time(unsigned tp, unsigned plan, unsigned tn) { return tn + VIS > tp ? max(plan, tn + SLOW) : plan; }
struct MemStream {
    rsrc_t rw; unsigned l16, vn, ko[10];
    DEV void init(rsrc_t r, int lane, int rot, bool reader) {
        rw = r; l16 = lane * 16; vn = reader ? min(lane, 39) * 128 : (lane & 15) * 8;
#pragma unroll
        for (int k = 0; k < 10; ++k) { int kk = k + rot; if (kk >= 10) kk -= 10; ko[k] = kk * 1024 + (kk >= 8 ? 8192 : 0); }
    }
    DEV u32x4 ld(unsigned rb, int kb) const { return ld128(rw, l16, rb + ko[kb]); }
    DEV void lda(u32x4& a, unsigned so) const { asm volatile("buffer_load_dwordx4 %0, %1, %2, %3 offen sc1" : "=v"(a) : "v"(l16), "s"(rw), "s"(so)); }
    DEV void ldn(u32x2& a, unsigned so) const { asm volatile("buffer_load_dwordx2 %0, %1, %2, %3 offen sc1" : "=v"(a) : "v"(vn), "s"(rw), "s"(so)); }
    DEV void poll(u32x4 (&a)[10], u32x2& nf, unsigned rb, Spin& sp, int s) {
        for (unsigned n = 0;;) {
            unsigned m = 0;
#pragma unroll
            for (int k = 0; k < 10; ++k) a[k] = ld(rb, k);
#pragma unroll
            for (int k = 0; k < 10; ++k) m = max(m, mx4(a[k]));
            if (!any_lane(m == SENT) || sp.fail(n, 0x100 + s)) break;
        }
        nf = ld64(rw, vn, rb + amap(R_NF));
    }
};
template <int N> DEV void vmw(u32x4& a) { asm volatile("s_waitcnt vmcnt(%1)" : "+v"(a) : "n"(N)); }
DEV void pace() { asm volatile("s_nop %0" :: "n"(PACE)); }
DEV unsigned track(unsigned est, unsigned m, int s) { return m > est ? min(m, est + (s < 8 ? 30u : 3u)) : est - 1u - ((est - m) >> 3); }
DEV unsigned long long nan_mask(f32x4 c) { unsigned long long b = __builtin_amdgcn_ballot_w64(__builtin_isunordered(c[0], c[1]) | __builtin_isunordered(c[2], c[3])); asm volatile("" : "+s"(b)); return b; }
DEV unsigned long long sent_mask(u32x4 a) { unsigned long long b = __builtin_amdgcn_ballot_w64(mx4(a) == SENT); asm volatile("" : "+s"(b)); return b; }
struct AFrag {
    LAS char *p0, *p1;
    DEV void init(LAS char* img, int lane) {
        const int ga = lane >> 4, base = ga * 256 + (lane & 15) * 8, sw = (ga & 1) * 128;
        p0 = opq(img + base + sw); p1 = opq(img + base + (128 ^ sw));
    }
    DEV u32x4 get(int off) const { return cat2(trd(p0 + off), trd(p1 + off)); }
    static DEV int wofs(int lane, int odd) { const int g = lane >> 4, n = lane & 15; return (((2 * (n >> 2) + (n & 1)) * 32) ^ (odd * 128)) + g * 8 + ((n >> 1) & 1) * 2; }
};
DEV unsigned fold_pk(f32x4 c, float& f0, float& f1) {
    unsigned r;
    asm volatile("v_add_f32 %1, %3, %4\n\tv_add_f32 %2, %5, %6\n\tv_cvt_pk_bf16_f32 %0, %1, %2" : "=v"(r), "=&v"(f0), "=&v"(f1) : "v"(c[0]), "v"(c[1]), "v"(c[2]), "v"(c[3]));
    return r;
}
DEV void st2x16(LAS char* p0, LAS char* p1, int off, unsigned v) { *(LAS unsigned short*)(p0 + off) = (unsigned short)v; *(LAS unsigned short*)(p1 + off) = (unsigned short)(v >> 16); }
template <int NT> struct WRes {
    LAS char *pe, *po;
    DEV void init(LAS char* slot0, int lane) {
        const int g = lane >> 4, q = (lane >> 2) & 3, p = lane & 3, rho = q | ((g & 1) << 2) | ((g >> 1) << 4);
        pe = opq(slot0 + rho * (NT * 32) + (g & 1) * 32 + 8 * p); po = opq(slot0 + rho * (NT * 32) + 32 - (g & 1) * 32 + 8 * p);
    }
    DEV u32x4 get(int slotoff, int tile) const { LAS char* b = ((tile & 1) ? po : pe) + slotoff + (tile & ~1) * 32; return cat2(trd(b), trd(b + 8 * NT * 32)); }
};

constexpr float SCALE_L2E = 0.07216878364870322f * 1.4426950408889634f;

DEV void role_S(const Params& P, unsigned char* ws, int h, LAS char* lds, int wave, int lane, Spin& sp) {
    constexpr int L_QK = 0, L_KX = 12288, L_QX = 36864, L_W = 65536;
    const rsrc_t rw = mk_rsrc(ws, (unsigned)WS_END);
    const int rot = (h * 4 + wave) % 10;
    u32x8 w[6][8];
    {
        const char* src = (const char*)(wave < 2 ? P.wq : P.wk) + h * 384 + (wave & 1) * 192;
        stream_w<6, 16, 20, 6, false>(src, 12288u, lds, lane,
            [&](int i) { const int j = (i + 2) % 6; return (unsigned)(j < 4 ? L_W + wave * 24576 + j * 6144 : 16384 + wave * 12288 + (j - 4) * 6144); },
            [&](int t, int k, u32x4 f) {
                if (k & 1) { w[t][k >> 1] = cat4(lo4(w[t][k >> 1]), f); if ((k >> 1) * 6 + t < 32) pin_a(w[t][k >> 1]); } else w[t][k >> 1] = cat4(f, f); }, rot);
        vmwait<0>();
    }
    bar();
    const unsigned par = load_par(rw, lane);
    const int idx = (lane & 1) ? 0xEEEE : 0x4444;
    const int g = lane >> 4, n = lane & 15;
    const unsigned cq = (unsigned)(O_CTRL + 4 * (W_QKPROG + 32 * (((wave < 2 ? 384 : 0) + h * 12 + (lane & 1) * 11) >> 4)));
    unsigned have = 0;
    auto wait_qk = [&](int s) {
        if (have > (unsigned)s) return;
        for (unsigned c = 0;;) {
            const unsigned v = ld32(rw, cq, 0);
            have = min((unsigned)__builtin_amdgcn_readlane(v, 0), (unsigned)__builtin_amdgcn_readlane(v, 1));
            if (have > (unsigned)s || sp.fail(c, 0x200 + s)) break;
            __builtin_amdgcn_s_sleep(2);
        }
    };
    Clock ck; ck.init(ws);
    MemStream ms; ms.init(rw, lane, rot, wave == 1);
    volatile LAS unsigned* lfail = (volatile LAS unsigned*)(lds + 61440);
    if (wave == 0) *lfail = ~0u;
    WRes<6> wr; wr.init(lds + L_W + wave * 24576, lane);
    AFrag af; af.init(lds + L_QK, lane);
    LAS char* we = opq(lds + L_QK + wave * 1536 + AFrag::wofs(lane, 0));
    LAS char* wo = opq(lds + L_QK + wave * 1536 + AFrag::wofs(lane, 1));
    const unsigned psel = (n & 1) ? 0x07060302u : 0x01000504u, psel2 = (n & 2) ? 0x07060302u : 0x01000504u;
    unsigned so1, so2;
    if (wave == 0) {
        const int i = 2 * g + (n & 1), half = (n >> 1) & 1;
        so1 = amap(R_P) + (h >> 4) * 16384 + (h & 15) * 512 + ((n >> 3) * 16 + 2 * i + half) * 16 + ((n >> 2) & 1) * 8;
        so2 = amap(R_P + 16384) + h * 128 + (2 * (2 * g + half) + ((n >> 2) & 1)) * 8 + (n >> 3) * 4;
    } else {
        const unsigned b = amap(R_PX) + (wave & 1) * amap(PX_B);
        so1 = (wave & 1) ? b + (h >> 3) * 16384 + (h & 7) * 1024 + lane * 16 : b + (h >> 4) * 16384 + ((h >> 1) & 7) * 1024 + lane * 16 + (h & 1) * 8;
        so2 = b + 65536 + h * 256 + n * 16 + g * 4;
    }
    const char* xsrc = (const char*)ws + (wave < 2 ? O_KX : O_QX + (wave & 1) * 1024) + (size_t)h * QX_B;
    auto stage_k = [&](int s) __attribute__((always_inline)) {
        wait_qk(s);
        sfor<0, 12>([&](auto I) __attribute__((always_inline)) { constexpr int i = decltype(I)::value;
            glds_dev<(i & 3) * 1024>(ms.l16, xsrc + (size_t)s * (H_ * QX_B) + (i >> 2) * 4096, (unsigned)(uintptr_t)lds + L_KX + (s & 1) * 12288 + (i >> 2) * 4096); });
    };
    auto stage_q = [&](int s) __attribute__((always_inline)) {
        wait_qk(s);
        sfor<0, 6>([&](auto I) __attribute__((always_inline)) { constexpr int i = decltype(I)::value;
            glds_dev<(i & 1) * 2048>(ms.l16, xsrc + (size_t)s * (H_ * QX_B) + (i >> 1) * 4096, (unsigned)(uintptr_t)lds + L_QX + (wave & 1) * 1024 + (i >> 1) * 4096); });
    };
    auto lwq = [&](int j, int q) {
        const int t = j % 6, kb = j / 6;
        return trd(((t & 1) ? wr.po : wr.pe) + (2 * kb + (q >> 1)) * 6144 + (t & ~1) * 32 + (q & 1) * 1536);
    };
    auto ord = [](int p) { return p == 0 ? 0 : p == 1 ? 8 : p < 5 ? p - 1 : p == 5 ? 9 : p - 2; };
    unsigned dur = 130, tn = 0;
    if (wave == 0) __builtin_amdgcn_s_setprio(3);
    if (wave == 1) stage_k(0); else if (wave >= 2) stage_q(0);
    vmwait<0>();
    bar();
    if (wave == 1) stage_k(1);
    for (int s = 0; s < NSEG; ++s) {
        const unsigned rb = rec(par, s);
        f32x4 acc[6];
        u32x2 st[12][4], nf; u32x4 a[10], kxf[12];
        LAS const char* kx = opq(lds + L_KX + (s & 1) * 12288 + lane * 16);
        unsigned long long bad = 0;
        auto prep = [&]() __attribute__((always_inline)) {
#pragma unroll
            for (int t = 0; t < 6; ++t) zero_acc(acc[t]);
#pragma unroll
            for (int i = 0; i < 12; ++i) st[i >> 2][i & 3] = lwq(i >> 2, i & 3);
        };
        float f0, f1;
        auto fin = [&](int t) __attribute__((always_inline)) {
            const unsigned v = fold_pk(acc[t], f0, f1);
            if (t & 1) st2x16(wo, wo + 4, t * 256, v); else st2x16(we, we + 4, t * 256, v);
        };
        auto slot = [&](auto I) __attribute__((always_inline)) {
            constexpr int i = decltype(I)::value % 64;
            constexpr bool fast = decltype(I)::value < 64;
            constexpr int nd = i <= 12 ? 0 : 1;
            if constexpr (fast) {
                if constexpr (i == 0) vmw<9>(a[0]);
                if constexpr (i == 6) vmw<8>(a[8]);
                if constexpr (i == 7) vmw<7>(a[1]);
                if constexpr (i == 15) vmw<6 + nd>(a[2]);
                if constexpr (i == 23) vmw<5 + nd>(a[3]);
                if constexpr (i == 30) vmw<4 + nd>(a[9]);
                if constexpr (i == 31) vmw<3 + nd>(a[4]);
                if constexpr (i == 39) vmw<2 + nd>(a[5]);
                if constexpr (i == 47) vmw<1 + nd>(a[6]);
                if constexpr (i == 49) vmw<0 + nd>(a[7]);
                if constexpr (i == 59) asm volatile("s_waitcnt vmcnt(0)" : "+v"(nf));
            }
            constexpr bool isl = i >= 6 && i <= 50 && (i & 3) == 2;
            constexpr int nl = i < 6 ? 0 : i > 50 ? 12 : (i - 6 + 3) / 4;
            if constexpr (isl) {
                smf_v(acc[nl % 6], a[8 + nl / 6], cat4(cat2(st[nl][0], st[nl][1]), cat2(st[nl][2], st[nl][3])), idx);
            } else {
                constexpr int r = i - nl;
                constexpr int kb = r < 36 ? r / 6 : 6 + (((r - 36) >> 1) & 1), t = r < 36 ? r % 6 : 2 * ((r - 36) >> 2) + (r & 1);
                if constexpr (kb * 6 + t < 32) smf_a(acc[t], a[kb], w[t][kb], idx); else smf_v(acc[t], a[kb], w[t][kb], idx);
            }
            if constexpr (i == 0) { MARK2(2); }
            if constexpr (i == 1) bad = sent_mask(a[0]);
            if constexpr (i == 12 && fast) ms.ldn(nf, rb + amap(R_NF));
            if constexpr (i >= 7 && i < 43) st[(i - 7) / 4 + 3][(i - 7) & 3] = lwq((i - 7) / 4 + 3, (i - 7) & 3);
            if constexpr (i >= 43 && i < 55) kxf[i - 43] = lds128(kx + (i - 43) * 1024);
            if constexpr (i == 56) { fin(0); bad = __builtin_amdgcn_ballot_w64(__builtin_isunordered(f0, f1)); asm volatile("" : "+s"(bad)); }
            if constexpr (i == 57) { fin(1); }
            if constexpr (i == 58) { fin(2); }
            if constexpr (i == 59) { fin(3); }
        };
        prep();
        unsigned so[10];
#pragma unroll
        for (int p = 0; p < 10; ++p) { so[p] = rb + ms.ko[p]; asm volatile("" : "+s"(so[p])); }
        MARK(0); SETV(7, 0);
        unsigned tg = ck.wait_until(tn);
        if (tg - tn < 10u) tg = tn;
#pragma unroll
        for (int p = 0; p < 10; ++p) { ms.lda(a[ord(p)], so[ord(p)]); if (p < 9) pace(); }
        SETV(1, tg + ck.org);
        sfor<0, 5>(slot);
        if (__builtin_expect(bad != 0, 0)) goto slow;
        sfor<5, 60>(slot);
        if (__builtin_expect(bad != 0, 0)) {
        slow:
            asm volatile("s_nop 7\n\ts_waitcnt vmcnt(0)" ::: "memory");
            *lfail = (unsigned)s; SETV(7, 1);
            ms.poll(a, nf, rb, sp, s);
            prep();
            sfor<64, 124>(slot);
        }
        asm volatile("s_nop 0");
        fin(4); fin(5);
        f32x4 am, a0, a1;
        MARK2(3);
        bar();
        MARK2(4);
        u32x4 qa[3], ka[3];
#pragma unroll
        for (int kb = 0; kb < 3; ++kb) { qa[kb] = af.get(kb * 1024); __builtin_amdgcn_sched_barrier(0); }
#pragma unroll
        for (int kb = 0; kb < 3; ++kb) { ka[kb] = af.get(3072 + kb * 1024); __builtin_amdgcn_sched_barrier(0); }
        asm volatile("" ::: "memory");
        if (wave == 0) {
            if (s < NSEG - 1) {
                a0 = f32x4{zero1(), zero1(), zero1(), zero1()}; a1 = f32x4{zero1(), zero1(), zero1(), zero1()};
                asm volatile("s_nop 0" : "+v"(a0), "+v"(a1));
#pragma unroll
                for (int kb = 0; kb < 3; ++kb) {
                    smf_v(a0, qa[kb], cat4(kxf[4 * kb], kxf[4 * kb + 2]), idx);
                    smf_v(a1, qa[kb], cat4(kxf[4 * kb + 1], kxf[4 * kb + 3]), idx);
                }
#pragma unroll
                for (int kb = 0; kb < 3; ++kb) mf_vv(am, qa[kb], ka[kb], kb == 0);
                asm volatile("s_nop 4" : "+v"(a0), "+v"(a1));
                float v[6];
                v[0] = a0[0] + a0[1]; v[3] = a0[2] + a0[3]; v[1] = a1[0] + a1[1]; v[4] = a1[2] + a1[3];
                asm volatile("" : "+v"(am), "+v"(v[0]), "+v"(v[1]), "+v"(v[3]), "+v"(v[4]));
                asm volatile("v_add_f32_dpp %0, %3, %2 quad_perm:[1,1,3,3] row_mask:0xf bank_mask:0xf\n\tv_add_f32_dpp %1, %5, %4 quad_perm:[1,1,3,3] row_mask:0xf bank_mask:0xf\n\ts_nop 0\n\t"
                             "v_mov_b32_dpp %0, %0 quad_perm:[0,0,2,2] row_mask:0xf bank_mask:0xf\n\tv_mov_b32_dpp %1, %1 quad_perm:[0,0,2,2] row_mask:0xf bank_mask:0xf"
                             : "=&v"(v[2]), "=&v"(v[5]) : "v"(am[0]), "v"(am[1]), "v"(am[2]), "v"(am[3]));
                float ma = fmax3(v[0], v[1], v[2]), mb = fmax3(v[3], v[4], v[5]);
                row_max2(ma, mb);
                ma *= SCALE_L2E; mb *= SCALE_L2E;
#pragma unroll
                for (int r = 0; r < 6; ++r) v[r] = __builtin_amdgcn_exp2f(__builtin_fmaf(v[r], SCALE_L2E, r < 3 ? -ma : -mb));
                float sa = __builtin_fmaf(v[2], 0.5f, v[0] + v[1]), sb = __builtin_fmaf(v[5], 0.5f, v[3] + v[4]);
                row_sum2(sa, sb);
                sa = __builtin_amdgcn_rcpf(sa); sb = __builtin_amdgcn_rcpf(sb);
                unsigned p0 = pka(v[0] * sa, v[3] * sb), p1 = pka(v[1] * sa, v[4] * sb), p2 = pka(v[2] * sa, v[5] * sb);
                asm volatile("s_nop 0" : "+v"(p0), "+v"(p1), "+v"(p2));
                st64(u32x2{pair_cols(p0, psel), pair_cols(p1, psel)}, rw, so1, rb);
                st32(__builtin_amdgcn_perm(p2, __builtin_amdgcn_update_dpp(0u, p2, 0x4E, 0xf, 0xf, true), psel2), rw, so2, rb);
                if (lane == 0) st32(dur, rw, h * 128, rb + amap(R_NS));
                tn = ck.now();
                lfail[1] = tn;
                asm volatile("s_waitcnt lgkmcnt(0)\n\ts_barrier" ::: "memory");
                if (s > 0 && __builtin_amdgcn_readfirstlane(*lfail) != s) dur = track(dur, tn - tg, s);
                SETV(5, tn + ck.org); SETV(6, tg + dur + ck.org);
                if (s + KARM < NSEG) { const unsigned ra = rec(par, s + KARM); st64(SENT2, rw, so1, ra); st32(sent1(), rw, so2, ra); if (lane == 0) st32(sent1(), rw, h * 128, ra + amap(R_NS)); }
            } else asm volatile("s_barrier" ::: "memory");
        } else if (wave == 1) {
            if (s + 2 < NSEG) stage_k(s + 2);
            { const u32x2 pl = read_notes(nf); lfail[2] = pl.x; lfail[3] = pl.y; }
            asm volatile("s_waitcnt lgkmcnt(0)\n\ts_barrier" ::: "memory");
        }
        else {
            const int Tq = wave & 1;
            u32x4 qx[6];
#pragma unroll
            for (int ep = 0; ep < 2; ++ep) qx[ep] = lds128(lds + L_QX + lane * 16 + Tq * 1024 + ep * 2048);
            zero_acc(am);
#pragma unroll
            for (int ep = 0; ep < 6; ++ep) {
                if (ep < 4) qx[ep + 2] = lds128(lds + L_QX + lane * 16 + Tq * 1024 + (ep + 2) * 2048);
                mf_vv(a0, kxf[2 * ep], qx[ep], ep == 0); mf_vv(a1, kxf[2 * ep + 1], qx[ep], ep == 0);
                if (ep & 1) smf_v(am, ka[ep >> 1], cat4(qx[ep - 1], qx[ep]), idx);
            }
            mf_done(am, a0, a1);
            float v[10];
            constexpr float SC = 0.07216878364870322f, L2E = 1.4426950408889634f;
#pragma unroll
            for (int r = 0; r < 4; ++r) {
                v[r] = (4 * g + r <= 16 * Tq + n) ? a0[r] * SC : -1e9f;
                v[4 + r] = (16 + 4 * g + r <= 16 * Tq + n) ? a1[r] * SC : -1e9f;
            }
            v[8] = (am[0] + am[1]) * SC; v[9] = (am[2] + am[3]) * SC;
            float mx = v[0];
#pragma unroll
            for (int r = 1; r < 10; ++r) mx = fmaxf(mx, v[r]);
            mx = x16(mx, true); mx = x32(mx, true);
            float sum = 0.f;
#pragma unroll
            for (int r = 0; r < 10; ++r) { v[r] = __builtin_amdgcn_exp2f((v[r] - mx) * L2E); sum += v[r]; }
            sum = x16(sum, false); sum = x32(sum, false);
            const float inv = __builtin_amdgcn_rcpf(sum);
            u32x4 ox = {pka(v[0] * inv, v[1] * inv), pka(v[2] * inv, v[3] * inv), pka(v[4] * inv, v[5] * inv), pka(v[6] * inv, v[7] * inv)};
            unsigned om = pka(v[8] * inv, v[9] * inv);
            asm volatile("s_barrier" : "+v"(ox), "+v"(om) :: "memory");
            if (Tq) st128(ox, rw, so1, rb); else st64(u32x2{ox.x, ox.y}, rw, so1, rb);
            st32(om, rw, so2, rb);
            if (s + 1 < NSEG) stage_q(s + 1);
            if (s + KARM < NSEG) { const unsigned ra = rec(par, s + KARM); if (Tq) st128(SENT4, rw, so1, ra); else st64(SENT2, rw, so1, ra); st32(sent1(), rw, so2, ra); }
        }
        tn = mem_time(__builtin_amdgcn_readfirstlane(lfail[2]), __builtin_amdgcn_readfirstlane(lfail[3]), __builtin_amdgcn_readfirstlane(lfail[1]));
        FLUSH(s);
    }
    vmwait<0>();
}

constexpr int L_WO = 81920;
template <class SV, class SO>
DEV void load_vo(const Params& P, int h, LAS char* lds, int wave, int lane, SV&& sv, SO&& so_, int rot = 0) {
    auto krow = [&](int k) { int kk = k + 2 * rot; if (kk >= 20) kk -= 20; return (size_t)kk * 32 * 12288; };
    const char* so = (const char*)P.wo + (size_t)h * E_ * 1280 + wave * 320;
    const char* sr = (const char*)P.wv + h * 384 + wave * 96;
    WSrc<10> wo; wo.init(lane, 1280u);
    WSrc<3> wv; wv.init(lane, 12288u);
    const unsigned lo = (unsigned)(uintptr_t)lds + L_WO + wave * 20480, lv = (unsigned)(uintptr_t)lds + wave * 20480;
    wo.issue(so, lo); wo.issue(so + 32 * 1280, lo + 10240);
#pragma unroll
    for (int k = 0; k < 6; ++k) wv.issue(sr + krow(k), lv + k * 3072);
#pragma unroll
    for (int k = 0; k < 20; ++k) {
        if (k == 0 || k == 6) {
            const int ko = k / 3;
            vmwait<18>();
#pragma unroll
            for (int j = 0; j < 2; ++j) {
                u32x4 fo[10];
#pragma unroll
                for (int t = 0; t < 10; ++t) fo[t] = wfrag<10, false>(lds + L_WO + wave * 20480 + j * 10240, lane, t);
#pragma unroll
                for (int t = 0; t < 10; ++t) so_(t, ko + j, fo[t]);
            }
            lgkm0();
            wo.issue(so + (ko + 2) * 32 * 1280, lo); wo.issue(so + (ko + 3) * 32 * 1280, lo + 10240);
        }
        vmwait_n(k < 12 ? 35 : k < 15 ? 15 : (19 - k) * 3);
        u32x4 fr[3];
#pragma unroll
        for (int t = 0; t < 3; ++t) fr[t] = wfrag<3, false>(lds + wave * 20480 + (k % 6) * 3072, lane, t);
#pragma unroll
        for (int t = 0; t < 3; ++t) sv(t, k, fr[t]);
        if (k + 6 < 20) { lgkm0(); wv.issue(sr + krow(k + 6), lv + (k % 6) * 3072); }
    }
    vmwait<0>();
    bar();
}

DEV void role_UV(const Params& P, unsigned char* ws, int h, LAS char* lds, int wave, int lane, Spin& sp) {
    const rsrc_t rw = mk_rsrc(ws, (unsigned)WS_END);
    const int rot = (h * 4 + wave + 5) % 10;
    u32x8 wv[3][10], wo[10][2];
    load_vo(P, h, lds, wave, lane,
        [&](int t, int k, u32x4 f) { if (k & 1) { wv[t][k >> 1] = cat4(lo4(wv[t][k >> 1]), f); pin_a(wv[t][k >> 1]); } else wv[t][k >> 1] = cat4(f, f); },
        [&](int t, int k, u32x4 f) { if (k & 1) { wo[t][k >> 1] = cat4(lo4(wo[t][k >> 1]), f); if (k == 1 && t < 2) pin_a(wo[t][k >> 1]); } else wo[t][k >> 1] = cat4(f, f); }, rot);
    const unsigned par = load_par(rw, lane);
    const int idx = (lane & 1) ? 0xEEEE : 0x4444;
    const int g = lane >> 4, n = lane & 15;
    MemStream ms; ms.init(rw, lane, rot, wave == 0);
    volatile LAS unsigned* lfail = (volatile LAS unsigned*)(lds + 4096);
    if (wave == 0) *lfail = ~0u;
    bar();
    AFrag af; af.init(lds, lane);
    WRes<10> wr; wr.init(lds + L_WO + wave * 20480, lane);
    const unsigned uo = amap(R_UB) + (wave * 10 + g) * 16384 + h * 256 + n * 16;
    const unsigned uo2 = amap(R_UB) + (wave * 10 + 8 + (g & 1)) * 16384 + h * 256 + n * 16 + (g >> 1) * 8;
    LAS char* vw0 = opq(lds + wave * 768 + AFrag::wofs(lane, wave & 1));
    LAS char* vw1 = opq(lds + wave * 768 + AFrag::wofs(lane, ~wave & 1));
    auto lwq = [&](int t, int q) { return trd(((t & 1) ? wr.po : wr.pe) + (q >> 1) * 10240 + (t & ~1) * 32 + (q & 1) * 2560); };
    Clock ck; ck.init(ws);
    unsigned dur = 110, tn = 0;
    for (int s = 0; s < NSEG; ++s) {
        const unsigned rb = rec(par, s);
        f32x4 acc[3], c[10];
        u32x2 st[10][4], nf; u32x4 a[10];
#pragma unroll
        for (int i = 0; i < 12; ++i) st[i >> 2][i & 3] = lwq(i >> 2, i & 3);
        MARK(0); SETV(7, 0);
        unsigned tg = 0;
        for (bool slow = false;; slow = true) {
#pragma unroll
            for (int t = 0; t < 3; ++t) zero_acc(acc[t]);
            if (!slow) {
                tg = ck.wait_until(tn);
                if (tg - tn < 10u) tg = tn;
#pragma unroll
                for (int p = 0; p < 10; ++p) { a[p] = ms.ld(rb, p); if (p < 9) pace(); }
                SETV(1, tg + ck.org);
            } else { *lfail = (unsigned)s; SETV(7, 1); ms.poll(a, nf, rb, sp, s); }
            sfor<0, 30>([&](auto I) __attribute__((always_inline)) {
                constexpr int i = decltype(I)::value, kb = i / 3, t = i % 3;
                smf_a(acc[t], a[kb], wv[t][kb], idx);
                if constexpr (i == 0) { MARK2(2); }
                if constexpr (t == 2 && kb >= 3) { c[kb - 3] = f32x4{zero1(), zero1(), zero1(), zero1()}; pin_v(c[kb - 3]); }
            });
            mf_done(acc[0], acc[1], acc[2]);
            if (__builtin_expect(nan_mask(acc[0]) == 0 || slow, 1)) break;
        }
#pragma unroll
        for (int t = 0; t < 3; ++t) { LAS char* p = t == 1 ? vw1 : vw0; float f0, f1; st2x16(p, p + 4, t * 256, fold_pk(acc[t], f0, f1)); }
#pragma unroll
        for (int t = 7; t < 10; ++t) { c[t] = f32x4{zero1(), zero1(), zero1(), zero1()}; pin_v(c[t]); }
        MARK2(3);
        bar();
        MARK2(4);
        u32x4 va[3];
#pragma unroll
        for (int kb = 0; kb < 3; ++kb) va[kb] = af.get(kb * 1024);
        unsigned o[10];
        sfor<0, 36>([&](auto I) __attribute__((always_inline)) {
            constexpr int i = decltype(I)::value, tp = i / 6, k = (i % 6) >> 1, t = 2 * tp + (i & 1);
            if constexpr (i < 30) {
                if constexpr (k == 0) { if constexpr (t < 2) smf_a(c[t], va[0], wo[t][0], idx); else smf_v(c[t], va[0], wo[t][0], idx); }
                else if constexpr (k == 1) smf_v(c[t], va[1], wo[t][1], idx);
                else smf_v(c[t], va[2], cat4(cat2(st[t][0], st[t][1]), cat2(st[t][2], st[t][3])), idx);
                constexpr int q0 = i < 3 ? 0 : ((i - 3) * 4) / 3, q1 = i < 3 ? 0 : ((i - 2) * 4) / 3;
                sfor<q0, q1>([&](auto Q) __attribute__((always_inline)) { constexpr int q = 12 + decltype(Q)::value; if constexpr (q < 40) st[q >> 2][q & 3] = lwq(q >> 2, q & 3); });
            }
            if constexpr (i == 30) asm volatile("s_nop 3" : "+v"(c[8]), "+v"(c[9]));
            if constexpr (i >= 10 && (i - 10) % 6 < 2) {
                constexpr int tf = 2 * ((i - 10) / 6) + (i - 10) % 6;
                float f0, f1;
                o[tf] = nosent(fold_pk(c[tf], f0, f1)); pin_v(o[tf]);
            }
            if constexpr (i == 19 || i == 31) {
                constexpr int q = i == 19 ? 0 : 4;
                auto r02 = __builtin_amdgcn_permlane32_swap(o[q], o[q + 2], false, false);
                auto r13 = __builtin_amdgcn_permlane32_swap(o[q + 1], o[q + 3], false, false);
                o[q] = r02[0]; o[q + 2] = r02[1]; o[q + 1] = r13[0]; o[q + 3] = r13[1];
                asm volatile("" : "+v"(o[q]), "+v"(o[q + 1]), "+v"(o[q + 2]), "+v"(o[q + 3]));
            }
            if constexpr (i == 20 || i == 32) {
                constexpr int q = i == 20 ? 0 : 4;
                auto r01 = __builtin_amdgcn_permlane16_swap(o[q], o[q + 1], false, false);
                auto r23 = __builtin_amdgcn_permlane16_swap(o[q + 2], o[q + 3], false, false);
                st128(u32x4{r01[0], r01[1], r23[0], r23[1]}, rw, uo, rb + q * 16384);
            }
        });
        { auto r = __builtin_amdgcn_permlane16_swap(o[8], o[9], false, false); st64(u32x2{r[0], r[1]}, rw, uo2, rb); }
        if (wave == 0 && lane == 0) st32(dur, rw, (32 + h) * 128, rb + amap(R_NS));
        tn = ck.now();
        if (s > 0 && __builtin_amdgcn_readfirstlane(*lfail) != s) dur = track(dur, tn - tg, s);
        SETV(5, tn + ck.org);
        if (wave == 0) { const u32x2 pl = read_notes(ld64(rw, ms.vn, rb + amap(R_NF))); lfail[1] = mem_time(pl.x, pl.y, tn); }
        bar();
        tn = __builtin_amdgcn_readfirstlane(lfail[1]);
        MARKC(6);
        if (s + KARM < NSEG) {
            const unsigned ra = rec(par, s + KARM);
            st128(SENT4, rw, uo, ra); st128(SENT4, rw, uo, ra + 65536); st64(SENT2, rw, uo2, ra);
            if (wave == 0 && lane == 0) st32(sent1(), rw, (32 + h) * 128, ra + amap(R_NS));
        }
        FLUSH(s);
    }
}

DEV void role_VC(const Params& P, unsigned char* ws, int h, int par, LAS char* lds, int wave, int lane, Spin& sp) {
    constexpr int L_V = 40960;
    const rsrc_t rw = mk_rsrc(ws, (unsigned)WS_END);
    u32x4 wv[3][20], wo[10][4];
    load_vo(P, h, lds, wave, lane, [&](int t, int k, u32x4 f) { wv[t][k] = f; pin_a(wv[t][k]); }, [&](int t, int k, u32x4 f) { wo[t][k] = f; if (t < 2 && k < 2) pin_a(wo[t][k]); });
    const int g = lane >> 4, n = lane & 15;
    const char* xf = (const char*)ws + O_XF;
    WRes<10> wr; wr.init(lds + L_WO + wave * 20480, lane);
    LAS char* l16 = opq(lds + lane * 16);
    auto issue_x = [&](int s) {
#pragma unroll
        for (int i = 0; i < 10; ++i) glds(lane * 16, xf + (size_t)s * 40960 + (wave * 10 + i) * 1024, (unsigned)(uintptr_t)lds + (wave * 10 + i) * 1024);
    };
    issue_x(par);
    for (int s = par; s < NSEG; s += 2) {
        MARK(0);
        if (s < 6) vmwait<0>(); else vmwait<4>();
        bar();
        MARK(1);
        if (s >= 2 && s < 6 && wave == 0 && lane == 0) st32((unsigned)(s - 1), rw, (unsigned)(O_CTRL + 4 * (W_VCPROG + 32 * par + h)), 0);
        f32x4 acc[3][2];
        u32x4 xb[40];
#pragma unroll
        for (int i = 0; i < 4; ++i) xb[i] = lds128(l16 + (i & 1) * 20480 + (i >> 1) * 1024);
#pragma unroll
        for (int i = 0; i < 40; ++i) {
#pragma unroll
            for (int t = 0; t < 3; ++t) {
                mf_av(acc[t][i & 1], wv[t][i >> 1], xb[i], i < 2);
                if (t == 0 && i + 4 < 40) xb[i + 4] = lds128(l16 + (i & 1) * 20480 + ((i + 4) >> 1) * 1024);
            }
        }
        mf_done(acc[0][0], acc[1][0], acc[2][0]); mf_done(acc[0][1], acc[1][1], acc[2][1]);
#pragma unroll
        for (int t = 0; t < 3; ++t)
#pragma unroll
            for (int T = 0; T < 2; ++T) {
                const int et = wave * 3 + t;
                const int off = (et >> 1) * 2048 + T * 1024 + ((2 * (et & 1) + (g >> 1)) * 16 + n) * 16 + (g & 1) * 8;
                *(LAS u32x2*)(lds + L_V + off) = u32x2{pk(acc[t][T][0], acc[t][T][1]), pk(acc[t][T][2], acc[t][T][3])};
            }
        MARK(2);
        vmwait<0>();
        bar();
        if (s >= 6 && wave == 0 && lane == 0) st32((unsigned)(s - 1), rw, (unsigned)(O_CTRL + 4 * (W_VCPROG + 32 * par + h)), 0);
        MARK(3);
        u32x4 va[6][2], st[10][2];
        f32x4 c[10][2];
#pragma unroll
        for (int k = 0; k < 6; ++k)
#pragma unroll
            for (int T = 0; T < 2; ++T) va[k][T] = lds128(l16 + L_V + k * 2048 + T * 1024);
        st[0][0] = wr.get(0, 0); st[0][1] = wr.get(10240, 0);
        const char* xn = xf + (size_t)(s + 2 < NSEG ? s + 2 : s) * 40960 + wave * 10240;
        const unsigned xd = (unsigned)(uintptr_t)lds + wave * 10240;
        auto fin = [&](int t) {
            st128(u32x4{pk(c[t][0][0], c[t][0][1]), pk(c[t][0][2], c[t][0][3]), pk(c[t][1][0], c[t][1][1]), pk(c[t][1][2], c[t][1][3])}, rw,
                  h * 1024 + lane * 16, (unsigned)(O_CB + (size_t)s * CB_B) + (wave * 10 + t) * 32768);
        };
#pragma unroll
        for (int t = 0; t < 10; ++t) {
#pragma unroll
            for (int k = 0; k < 6; ++k) {
                const u32x4 b = k < 4 ? wo[t][k & 3] : st[t][k & 1];
                if (t < 2 && k < 2) { mf_va(c[t][0], va[k][0], b, k == 0); mf_va(c[t][1], va[k][1], b, k == 0); }
                else { mf_vv(c[t][0], va[k][0], b, k == 0); mf_vv(c[t][1], va[k][1], b, k == 0); }
                if (k < 2 && t + 1 < 10) st[t + 1][k] = wr.get(k * 10240, t + 1);
                if (k == 5 && t < 5) { glds(lane * 16 + 2 * t * 1024, xn, xd + 2 * t * 1024); }
                if (k == 2 && t < 5) { glds(lane * 16 + (2 * t + 1) * 1024, xn, xd + (2 * t + 1) * 1024); }
                if (k == 3 && t >= 1) { pin_v(c[t - 1][0]); pin_v(c[t - 1][1]); fin(t - 1); }
            }
        }
        mf_done(c[9][0], c[9][1]);
        fin(9);
        MARK(4); FLUSH(s);
    }
    vmwait<0>();
    bar();
    if (wave == 0 && lane == 0) st32((unsigned)NSEG, rw, (unsigned)(O_CTRL + 4 * (W_VCPROG + 32 * par + h)), 0);
}

DEV void role_QK(const Params& P, unsigned char* ws, int cu, LAS char* lds, int wave, int lane, Spin& sp) {
    const rsrc_t rw = mk_rsrc(ws, (unsigned)WS_END);
    u32x4 w[4][20];
    const int sel = cu >= 24, c0 = (cu - 24 * sel) * 256 + wave * 64;
    stream_w<4, 20, 20, 8, true>((const char*)(sel ? P.wk : P.wq) + c0 * 2, 12288u, lds, lane, [&](int i) { return (unsigned)(wave * 32768 + i * 4096); },
                                 [&](int t, int k, u32x4 f) { w[t][k] = f; if (k < 16) pin_a(w[t][k]); });
    vmwait<0>();
    bar();
    const char* xf = (const char*)ws + O_XF;
    auto issue_x = [&](int s) {
#pragma unroll
        for (int i = 0; i < 10; ++i) glds(lane * 16, xf + (size_t)s * 40960 + (wave * 10 + i) * 1024, (unsigned)(uintptr_t)lds + (s & 1) * 40960 + (wave * 10 + i) * 1024);
    };
    issue_x(0);
    const unsigned ob = (unsigned)(sel ? O_KX : O_QX) + (c0 >> 5) * 2048;
    for (int s = 0; s < NSEG; ++s) {
        MARK(0);
        if (s < 4) vmwait<0>(); else vmwait<4>();
        bar();
        MARK(1);
        if (s >= 1 && wave == 0 && lane == 0) st32((unsigned)(s < 4 ? s : s - 1), rw, (unsigned)(O_CTRL + 4 * (W_QKPROG + 32 * cu)), 0);
        LAS char* l16 = opq(lds + (s & 1) * 40960 + lane * 16);
        f32x4 acc[4][2];
        u32x4 xb[40];
#pragma unroll
        for (int i = 0; i < 4; ++i) xb[i] = lds128(l16 + (i & 1) * 20480 + (i >> 1) * 1024);
        const char* xn = xf + (size_t)(s + 1 < NSEG ? s + 1 : s) * 40960 + wave * 10240;
        const unsigned xd = (unsigned)(uintptr_t)lds + (~s & 1) * 40960 + wave * 10240;
#pragma unroll
        for (int i = 0; i < 40; ++i)
#pragma unroll
            for (int t = 0; t < 4; ++t) {
                if (t == 2 && i % 3 == 0 && i < 30) glds(lane * 16 + (i / 3) * 1024, xn, xd + (i / 3) * 1024);
                if (i < 32) mf_av(acc[t][i & 1], w[t][i >> 1], xb[i], i < 2); else mf_vv(acc[t][i & 1], w[t][i >> 1], xb[i], false);
                if (t == 0 && i + 4 < 40) xb[i + 4] = lds128(l16 + (i & 1) * 20480 + ((i + 4) >> 1) * 1024);
            }
        mf_done(acc[0][0], acc[1][0], acc[2][0], acc[3][0]); mf_done(acc[0][1], acc[1][1], acc[2][1], acc[3][1]);
#pragma unroll
        for (int pr = 0; pr < 2; ++pr)
#pragma unroll
            for (int T = 0; T < 2; ++T) {
                const f32x4 a = acc[2 * pr][T], b = acc[2 * pr + 1][T];
                st128(u32x4{pk(a[0], a[1]), pk(a[2], a[3]), pk(b[0], b[1]), pk(b[2], b[3])}, rw, pr * 2048 + T * 1024 + lane * 16, ob + (unsigned)s * (unsigned)(H_ * QX_B));
            }
        MARK(2); MARKC(6); FLUSH(s);
    }
    vmwait<0>();
    bar();
    if (wave == 0 && lane == 0) st32((unsigned)NSEG, rw, (unsigned)(O_CTRL + 4 * (W_QKPROG + 32 * cu)), 0);
}

DEV void role_F(const Params& P, unsigned char* ws, int f, LAS char* lds, int wave, int lane, Spin& sp) {
    constexpr int L_FL = 131072, L_U = L_FL + 1024;
    const rsrc_t rw = mk_rsrc(ws, (unsigned)WS_END);
    volatile LAS unsigned* fl = (volatile LAS unsigned*)(lds + L_FL);
    if (wave == 0 && lane < 8) fl[lane] = 0u;
    bar();
    const unsigned par = load_par(rw, lane);
    const int g = lane >> 4, n = lane & 15;
    const unsigned l16 = lane * 16;
    const unsigned xr = ((f % 10) * 3) & 15;
    auto lwait = [&](int i, int need, unsigned code) {
        for (unsigned c = 0, k = 0; (int)__builtin_amdgcn_readfirstlane(fl[i]) < need;) { if ((++k & 63u) == 0u && sp.fail(c, code)) break; }
        asm volatile("" ::: "memory");
    };
    auto lset = [&](int i, int v) { asm volatile("s_waitcnt lgkmcnt(0)" ::: "memory"); if (lane == 0) fl[i] = (unsigned)v; };
    if (wave == 3) {
        unsigned have0 = 0, have1 = 0;
        for (int s = 0; s < NSEG; ++s) {
            MARK(0);
            lwait(1, s - 1, 0x340); lwait(2, s - 3, 0x341); lwait(3, s - 3, 0x342);
            MARK(1);
            unsigned hv = (s & 1) ? have1 : have0;
            for (unsigned c = 0; hv <= (unsigned)s;) {
                const unsigned v = ld32(rw, (lane & 31) * 4, (unsigned)(O_CTRL + 4 * (W_VCPROG + 32 * (s & 1))));
                if (!any_lane(v <= (unsigned)s)) {
                    hv = ~wave_max(~v);
                    break;
                }
                if (sp.fail(c, 0x300 + s)) break;
                __builtin_amdgcn_s_sleep(2);
            }
            if (s & 1) have1 = hv; else have0 = hv;
            const char* src = (const char*)ws + O_CB + (size_t)s * CB_B + f * 32768;
#pragma unroll
            for (int i = 0; i < 16; ++i) {
                glds_dev<0>(l16, src + (i ^ xr) * 2048, (unsigned)(uintptr_t)lds + (s & 3) * 32768 + i * 2048);
                glds_dev<1024>(l16, src + (i ^ xr) * 2048, (unsigned)(uintptr_t)lds + (s & 3) * 32768 + i * 2048);
            }
            vmwait<0>();
            lset(0, s + 1);
            MARK(2); FLUSH(s);
        }
    } else if (wave == 0) {
        const int idx = (lane & 1) ? 0xEEEE : 0x4444;
        const unsigned pmo = g * 256 + n * 8;
        const unsigned mo = amap(R_MEM + f * 256) + (2 * (2 * g + (n & 1)) + ((n >> 1) & 1)) * 16 + (n >> 2) * 4;
        const unsigned psel = (n & 1) ? 0x07060302u : 0x01000504u;
        unsigned pv[4], ps[4];
#pragma unroll
        for (int j = 0; j < 4; ++j) { pv[j] = l16 + ((j ^ xr) & 3) * 1024; const unsigned k = j ^ (xr >> 2); ps[j] = amap(R_P) + (k & 1) * 4096 + (k >> 1) * 16384; }
        Clock ck; ck.init(ws);
        unsigned dur = 60, tp = 0, plan = 0, dS = 140, dU = 130;
        const unsigned vf = min(lane, 39) * 128;
        unsigned boost = 0;
        u32x2 nf = {0u, 0u};
        auto use_notes = [&](unsigned rb) __attribute__((always_inline)) {
            for (unsigned c = 0; any_lane(max(nf.x, nf.y) == SENT);) { if (sp.fail(c, 0x390)) break; nf = ld64(rw, vf, rb + amap(R_NF)); }
            const u32x2 pl = read_notes(nf); tp = pl.x; plan = pl.y;
        };
        __builtin_amdgcn_s_setprio(3);
        for (int s = 0; s < NSEG - 1; ++s) {
            MARK(0);
            lwait(0, s + 1, 0x343);
            LAS char* cb = opq(lds + (s & 3) * 32768 + l16);
            u32x8 bc[16];
#pragma unroll
            for (int t = 0; t < 16; ++t) bc[t] = cat4(lds128(cb + 2 * t * 1024), lds128(cb + (2 * t + 1) * 1024));
#pragma unroll
            for (int t = 0; t < 16; ++t) pin_a(bc[t]);
            const unsigned rb = rec(par, s), rn = rec(par, s + 1), ub = rb + amap(R_UB) + f * 16384, pb = rb + amap(R_P);
            use_notes(rb);
            const unsigned tu = tp - (max(dS, dU + 8u) - dU);
            unsigned ns;
            float r0, r1;
            u32x4 u[8], px[16]; u32x2 pm[8];
            f32x4 d; zero_acc(d);
            MARK(1);
            ck.wait_until(tu);
#pragma unroll
            for (int i = 0; i < 8; ++i) u[i] = ld128(rw, l16 + (i & 3) * 1024, ub + (i >> 2) * 4096);
            const unsigned tg = ck.wait_until(tp);
            SETV(3, tg + ck.org); SETV(4, tp + ck.org); SETV(5, plan - DELTA_B + ck.org); SETV(6, tu + ck.org);
            bool failed = false;
            for (unsigned c = 0;;) {
#pragma unroll
                for (int i = 0; i < 16; ++i) px[i] = ld128(rw, pv[i & 3], rb + ps[i >> 2]);
#pragma unroll
                for (int i = 0; i < 8; ++i) pm[i] = ld64(rw, pmo + (i >> 1) * 1024 + (i & 1) * 128, pb + 32768);
                ns = ld32(rw, lane * 128, rb + amap(R_NS));
#pragma unroll
                for (int i = 0; i < 16; ++i) smf_a(d, px[i], bc[i], idx);
#pragma unroll
                for (int i = 0; i < 4; ++i) smf_v(d, cat2(pm[2 * i], pm[2 * i + 1]), cat4(u[2 * i], u[2 * i + 1]), idx);
                mf_done(d);
                r0 = d[0] + d[1]; r1 = d[2] + d[3];
                if (__builtin_expect(!any_lane(__builtin_isunordered(r0, r1)), 1)) { SETV(7, c); break; }
                unsigned sg = 0; bool miss = false;
#pragma unroll
                for (int i = 0; i < 8; ++i) miss |= mx4(u[i]) == SENT;
#pragma unroll
                for (int i = 0; i < 16; ++i) sg |= px[i].x | px[i].y | px[i].z | px[i].w;
#pragma unroll
                for (int i = 0; i < 8; ++i) sg |= pm[i].x | pm[i].y;
                if (!any_lane(miss | ((sg & 0x80008000u) != 0u))) break;
                if (sp.fail(c, 0x400 + s)) break;
                failed = true;
                zero_acc(d);
#pragma unroll
                for (int i = 0; i < 8; ++i) u[i] = ld128(rw, l16 + (i & 3) * 1024, ub + (i >> 2) * 4096);
            }
            st32(nosent(pair_cols(pk(r0, r1), psel)), rw, mo, rn);
            const unsigned tpub = ck.now();
            const unsigned prop = (tpub + VIS <= plan ? plan : max(tpub + SLOW, plan + REDO)) + max(dS, dU + 8u) + DELTA_A + (failed ? boost : 0u);
            boost = failed ? min(boost + 100u, 600u) : 0u;
            if (lane == 0) { st64(u32x2{prop, dur}, rw, f * 128, rn + amap(R_NF)); fl[1] = (unsigned)(s + 1); }
            if (!failed && tg - tp < 15u) dur = track(dur, tpub - tp, s);
            SETV(2, tpub + ck.org);
            if (!any_lane(ns == SENT)) { const unsigned m = half_max(min(ns, 400u)); dS = __builtin_amdgcn_readlane(m, 0); dU = __builtin_amdgcn_readlane(m, 32); }
            lwait(2, s - 1, 0x346); lwait(3, s - 1, 0x347);
#pragma unroll
            for (int i = 0; i < 8; ++i) *(LAS u32x4*)(lds + L_U + (s & 1) * 8192 + i * 1024 + l16) = u[i];
            lset(4, s + 1);
            ck.wait_until(max(plan + 10, tpub + 20));
            nf = ld64(rw, vf, rn + amap(R_NF));
            if (s + KARM < NSEG) { const unsigned ra = rec(par, s + KARM); st32(sent1(), rw, mo, ra); if (lane == 0) st64(SENT2, rw, f * 128, ra + amap(R_NF)); }
            FLUSH(s);
        }
        {
            const unsigned ub = rec(par, NSEG - 1) + amap(R_UB) + f * 16384;
            u32x4 u[8];
            use_notes(rec(par, NSEG - 1));
            ck.wait_until(tp);
            lset(1, NSEG);
            for (unsigned c = 0;;) {
                unsigned m = 0;
#pragma unroll
                for (int i = 0; i < 8; ++i) u[i] = ld128(rw, l16 + (i & 3) * 1024, ub + (i >> 2) * 4096);
#pragma unroll
                for (int i = 0; i < 8; ++i) m = max(m, mx4(u[i]));
                if (!any_lane(m == SENT) || sp.fail(c, 0x4FF)) break;
            }
            lwait(2, NSEG - 2, 0x346); lwait(3, NSEG - 2, 0x347);
#pragma unroll
            for (int i = 0; i < 8; ++i) *(LAS u32x4*)(lds + L_U + ((NSEG - 1) & 1) * 8192 + i * 1024 + l16) = u[i];
            lset(4, NSEG);
        }
    } else {
        auto xrows = [&](auto TQ) __attribute__((always_inline)) {
            constexpr int Tq = decltype(TQ)::value, NX = Tq ? 32 : 16;
            const rsrc_t rx = mk_rsrc(P.x, S_ * D_ * 2);
            const unsigned ho = (f >> 1) * 1024 + ((2 * (f & 1) + (g >> 1)) * 16 + n) * 16 + (g & 1) * 8;
            for (int s = 0; s < NSEG; ++s) {
                MARK(0);
                lwait(0, s + 1, 0x345);
                LAS char* cb = opq(lds + (s & 3) * 32768 + l16);
                u32x4 ca[NX];
#pragma unroll
                for (int i = 0; i < NX; ++i) ca[i] = Tq ? lds128(cb + i * 1024) : cat2(*(LAS const u32x2*)(cb + i * 2048), *(LAS const u32x2*)(cb + i * 2048 + 1024));
#pragma unroll
                for (int i = 0; i < NX; ++i) pin_a(ca[i]);
                const unsigned pb = rec(par, s) + amap(R_PX) + Tq * amap(PX_B);
                lwait(1, s + 1, 0x344);
                MARK2(1);
                f32x4 d; u32x2 xv;
                for (unsigned c = 0;;) {
                    u32x4 u[8], px[NX + 8];
#pragma unroll
                    for (int i = 0; i < NX; ++i) {
                        if (Tq) { const unsigned k = (i >> 1) ^ xr; px[i] = ld128(rw, l16 + (i & 1) * 1024, pb + (k & 3) * 2048 + (k >> 2) * 16384); }
                        else { const unsigned k = i ^ xr; px[i] = ld128(rw, l16, pb + (k & 7) * 1024 + (k >> 3) * 16384); }
                    }
#pragma unroll
                    for (int i = 0; i < 8; ++i) px[NX + i] = ld128(rw, l16 + (i & 3) * 1024, pb + 65536 + (i >> 2) * 4096);
                    xv = __builtin_amdgcn_raw_buffer_load_b64(rx, ((32 * s + 16 * Tq + n) * D_ + 16 * f + 4 * g) * 2, 0, 0);
                    lwait(4, s + 1, 0x348);
#pragma unroll
                    for (int i = 0; i < 8; ++i) u[i] = lds128(lds + L_U + (s & 1) * 8192 + i * 1024 + l16);
#pragma unroll
                    for (int i = 0; i < NX; ++i) mf_av(d, ca[i], px[i], i == 0);
#pragma unroll
                    for (int i = 0; i < 8; ++i) mf_vv(d, u[i], px[NX + i], false);
                    mf_done(d);
                    if (!any_lane(__builtin_isunordered(d[0] + d[1], d[2] + d[3]))) { SETV(7, c); break; }
                    bool miss = false;
#pragma unroll
                    for (int i = 0; i < NX + 8; ++i) miss |= mx4(px[i]) == SENT;
                    if (!any_lane(miss)) break;
                    if (sp.fail(c, 0x500 + s)) break;
                }
                MARK(2);
                lset(2 + Tq, s + 1);
                const unsigned a01 = pk(d[0], d[1]), a23 = pk(d[2], d[3]);
                st64(u32x2{pks(bflo(xv.x) + bflo(a01), bfhi(xv.x) + bfhi(a01)), pks(bflo(xv.y) + bflo(a23), bfhi(xv.y) + bfhi(a23))}, rw, ho, (unsigned)(O_HF + (size_t)(2 * s + Tq) * XF_B));
                if (s + KARM < NSEG) st64(SENT2, rw, ho, (unsigned)(O_HF + (size_t)(2 * (s + KARM) + Tq) * XF_B));
                MARK(3); FLUSH(s);
            }
        };
        if (wave == 1) xrows(IC<0>{}); else xrows(IC<1>{});
    }
}

DEV float gelu_t(float x) {
    const float u = x * (0.7978845608028654f + 0.035677408136300125f * x * x);
    return x * __builtin_amdgcn_rcpf(1.f + __builtin_amdgcn_exp2f(-2.885390081777927f * u));
}

template <int NK> DEV bool any_sent(const u32x4 (&x)[NK]) {
    unsigned m = 0;
#pragma unroll
    for (int k = 0; k < NK; ++k) m = max(m, mx4(x[k]));
    return any_lane(m == SENT);
}
template <int MODE>
DEV void role_W(const Params& P, unsigned char* ws, int cb, LAS char* lds, int wave, int lane, Spin& sp) {
    const rsrc_t rw = mk_rsrc(ws, (unsigned)WS_END);
    constexpr unsigned stride = MODE == 3 ? F_ * 2 : D_ * 2;
    const char* wsrc = (const char*)(MODE == 1 ? P.w1 : MODE == 2 ? P.w2 : P.w3) + (size_t)wave * 160 * stride + cb * 256;
    u32x4 w[8][5];
    stream_w<8, 5, 5, 4, true>(wsrc, stride, lds, lane, [&](int i) { return (unsigned)(wave * 32768 + i * 8192); }, [&](int t, int k, u32x4 f) { w[t][k] = f; pin_a(w[t][k]); }, 0, 2 * wave + 2);
    vmwait<0>();
    bar();
    const int g = lane >> 4, n = lane & 15;
    const unsigned l16 = lane * 16;
    constexpr size_t O_IN = MODE == 1 ? O_HF : MODE == 2 ? O_GF : O_X1F;
    LAS char* wr[3];
#pragma unroll
    for (int j = 0; j < 3; ++j) wr[j] = opq(lds + ((wave + 1 + j) & 3) * 6144 + j * 2048 + l16);
    LAS char* rd = opq(lds + wave * 6144 + l16);
    LAS char* sw = opq(lds + 24576 + wave * 512 + lane * 8);
    LAS char* sr = opq(lds + 24576 + ((n >> 2) * 16 + n) * 8);
    u32x4 ones = {0x3F803F80u, 0x3F803F80u, 0x3F803F80u, 0x3F803F80u}; pin_v(ones);
    auto fetch1 = [&](int u, int k) __attribute__((always_inline)) { return ld128(rw, l16, (unsigned)(O_IN + (size_t)u * XF_B) + (wave * 5 + k) * 1024); };
    auto product = [&](const u32x4 (&xb)[5], u32x4 (&nx)[5], int un, int buf, f32x4& o0, f32x4& o1, float& S1, float& S2) __attribute__((always_inline)) {
        f32x4 acc[8], s1, s2;
        if (MODE != 2) {
#pragma unroll
            for (int k = 0; k < 5; ++k) { mf_vv(s1, ones, xb[k], k == 0); mf_vv(s2, xb[k], xb[k], k == 0); }
        }
        sfor<0, 40>([&](auto I) __attribute__((always_inline)) {
            constexpr int i = decltype(I)::value, t = 2 * (i / 10) + (i & 1), k = (i % 10) >> 1;
            mf_av(acc[t], w[t][k], xb[k], k == 0);
            if constexpr (i % 3 == 0 && i < 15) { if (un >= 0) nx[i / 3] = fetch1(un, i / 3); }
            if constexpr (i >= 17 && (i % 10 == 7 || i % 10 == 8)) { constexpr int tw = 2 * (i / 10 - 1) + (i % 10 - 7); pin_v(acc[tw]); *(LAS f32x4*)(wr[tw >> 1] + buf * 28672 + (tw & 1) * 1024) = acc[tw]; }
            if constexpr (MODE != 2 && i == 20) {
                asm volatile("" : "+v"(s1), "+v"(s2));
                const float d = (n & 2) ? ((n & 1) ? s2[3] : s2[2]) : ((n & 1) ? s2[1] : s2[0]);
                *(LAS f32x2*)(sw + buf * 28672) = f32x2{s1[0], d};
            }
        });
        bar();
        f32x4 p[6]; f32x2 q[4];
#pragma unroll
        for (int j = 0; j < 6; ++j) p[j] = *(LAS const f32x4*)(rd + buf * 28672 + j * 1024);
        if (MODE != 2) {
#pragma unroll
            for (int j = 0; j < 4; ++j) q[j] = *(LAS const f32x2*)(sr + buf * 28672 + j * 512);
        }
        mf_done(acc[6], acc[7]);
        o0 = (acc[6] + p[0]) + (p[2] + p[4]); o1 = (acc[7] + p[1]) + (p[3] + p[5]);
        if (MODE != 2) { S1 = (q[0].x + q[1].x) + (q[2].x + q[3].x); S2 = (q[0].y + q[1].y) + (q[2].y + q[3].y); }
    };
    f32x4 cs0 = {}, cs1 = {};
    if (MODE != 2) {
        u32x4 o4[5] = {ones, ones, ones, ones, ones}, dm[5];
        asm volatile("s_nop 1" : "+v"(o4[0]), "+v"(o4[1]), "+v"(o4[2]), "+v"(o4[3]), "+v"(o4[4]));
        float a, b; product(o4, dm, -1, 1, cs0, cs1, a, b);
    }
    const int kso = 4 * cb + wave;
    const int grp = kso / 5, sh = grp == 0 ? 1 : grp == 1 ? -1 : grp == 2 ? 2 : -2;
    if (MODE == 1) {
        const int td = sh > 0 ? n : S_ - 1 - n;
        if (n < (sh > 0 ? sh : -sh)) st128(u32x4{0u, 0u, 0u, 0u}, rw, (td >> 4) * (unsigned)XF_B + kso * 1024 + (g * 16 + (td & 15)) * 16, (unsigned)O_GF);
        vmwait<0>();
    }
    u32x4 xa[5], xc[5];
#pragma unroll
    for (int k = 0; k < 5; ++k) xa[k] = fetch1(0, k);
    bool miss = any_sent(xa);
    auto unit = [&](int u, u32x4 (&xb)[5], u32x4 (&nx)[5]) __attribute__((always_inline)) {
        MARK(0);
        for (unsigned c = 0; miss; miss = any_sent(xb)) {
            if (sp.fail(c, 0x600 + MODE * 0x100 + u)) break;
            if (c > 1) __builtin_amdgcn_s_sleep(3);
#pragma unroll
            for (int k = 0; k < 5; ++k) xb[k] = fetch1(u, k);
        }
        MARK(1);
        asm volatile("s_nop 1" : "+v"(xb[0]), "+v"(xb[1]), "+v"(xb[2]), "+v"(xb[3]), "+v"(xb[4]));
        const int tok = 16 * u + n;
        u32x4 hv; u32x2 stt;
        if (MODE == 2) { hv = ld128(rw, l16, (unsigned)(O_HF + (size_t)u * XF_B) + kso * 1024); stt = ld64(rw, tok * 8, (unsigned)O_ST); }
        f32x4 o0, o1; float S1, S2;
        product(xb, nx, u + 1 < NUNIT ? u + 1 : u, u & 1, o0, o1, S1, S2);
        miss = any_sent(nx);
        MARK(2);
        if (MODE != 2) {
            const float mu = S1 * (1.f / D_), var = fmaxf(S2 * (1.f / D_) - mu * mu, 0.f);
            float al = __builtin_amdgcn_rsqf(var + 1e-5f);
            if (MODE == 1) {
                if (cb == 0 && wave == 0 && g == 0) st64(u32x2{nosent(f2u(mu)), nosent(f2u(al))}, rw, tok * 8, (unsigned)O_ST);
                al *= __builtin_amdgcn_rsqf(var * al * al + 1e-5f);
            }
            float y[8];
#pragma unroll
            for (int r = 0; r < 4; ++r) { y[r] = gelu_t(al * (o0[r] - mu * cs0[r])); y[4 + r] = gelu_t(al * (o1[r] - mu * cs1[r])); }
            const u32x4 o = {pks(y[0], y[1]), pks(y[2], y[3]), pks(y[4], y[5]), pks(y[6], y[7])};
            if (MODE == 1) {
                const int td = tok + sh;
                const unsigned vo = (td >> 4) * (int)XF_B + kso * 1024 + (g * 16 + (td & 15)) * 16;
                if (td >= 0 && td < S_) st128(o, rw, vo, (unsigned)O_GF);
                if (u + 2 * KARM < NUNIT && td + 32 * KARM < S_) st128(SENT4, rw, vo + 2 * KARM * (unsigned)XF_B, (unsigned)O_GF);
            } else {
                st128(o, rw, l16, (unsigned)(O_G3F + (size_t)u * G3_B) + kso * 1024);
                if (u + 2 * KARM < NUNIT) st128(SENT4, rw, l16, (unsigned)(O_G3F + (size_t)(u + 2 * KARM) * G3_B) + kso * 1024);
            }
        } else {
            for (unsigned c = 0; any_lane(max(mx4(hv), max(stt.x, stt.y)) == SENT);) {
                if (sp.fail(c, 0xA00 + u)) break;
                hv = ld128(rw, l16, (unsigned)(O_HF + (size_t)u * XF_B) + kso * 1024); stt = ld64(rw, tok * 8, (unsigned)O_ST);
            }
            const float mu = u2f(stt.x), al = u2f(stt.y);
            const u32x4 o = {pks((bflo(hv.x) - mu) * al + o0[0], (bfhi(hv.x) - mu) * al + o0[1]), pks((bflo(hv.y) - mu) * al + o0[2], (bfhi(hv.y) - mu) * al + o0[3]),
                             pks((bflo(hv.z) - mu) * al + o1[0], (bfhi(hv.z) - mu) * al + o1[1]), pks((bflo(hv.w) - mu) * al + o1[2], (bfhi(hv.w) - mu) * al + o1[3])};
            st128(o, rw, l16, (unsigned)(O_X1F + (size_t)u * XF_B) + kso * 1024);
            if (u + 2 * KARM < NUNIT) st128(SENT4, rw, l16, (unsigned)(O_X1F + (size_t)(u + 2 * KARM) * XF_B) + kso * 1024);
        }
        MARK(3); FLUSH(u);
    };
    for (int u = 0; u < NUNIT; u += 2) { unit(u, xa, xc); unit(u + 1, xc, xa); }
}

DEV void role_W4(const Params& P, unsigned char* ws, int cb, LAS char* lds, int wave, int lane, Spin& sp) {
    const rsrc_t rw = mk_rsrc(ws, (unsigned)WS_END);
    const rsrc_t ro = mk_rsrc(P.out, S_ * D_ * 2);
    u32x4 w[4][12];
    stream_w<4, 12, 12, 8, false>((const char*)P.w4 + (size_t)wave * 384 * 1280 + cb * 128, 1280u, lds, lane, [&](int i) { return (unsigned)(wave * 32768 + i * 4096); },
                                  [&](int t, int k, u32x4 f) { w[t][k] = f; pin_a(w[t][k]); }, 0, wave + 1);
    vmwait<0>();
    bar();
    const int g = lane >> 4, n = lane & 15;
    const unsigned l16 = lane * 16;
    const int ct = 4 * cb + wave;
    const unsigned po = (ct >> 1) * 1024 + ((2 * (ct & 1) + (g >> 1)) * 16 + n) * 16 + (g & 1) * 8;
    LAS char* wr[3];
#pragma unroll
    for (int j = 0; j < 3; ++j) wr[j] = opq(lds + ((wave + 1 + j) & 3) * 3072 + j * 1024 + l16);
    LAS char* rd = opq(lds + wave * 3072 + l16);
    auto fetch = [&](u32x4 (&x)[12], int u) __attribute__((always_inline)) {
#pragma unroll
        for (int k = 0; k < 12; ++k) x[k] = ld128(rw, l16 + (k & 3) * 1024, (unsigned)(O_G3F + (size_t)u * G3_B) + (wave * 12 + (k & ~3)) * 1024);
    };
    u32x4 xa[12], xc[12];
    fetch(xa, 0);
    bool miss = any_sent(xa);
    auto unit = [&](int u, u32x4 (&xb)[12], u32x4 (&nx)[12]) __attribute__((always_inline)) {
        MARK(0);
        for (unsigned c = 0; miss; miss = any_sent(xb)) {
            if (sp.fail(c, 0xB00 + u)) break;
            if (c > 1) __builtin_amdgcn_s_sleep(3);
            fetch(xb, u);
        }
        MARK(1);
#pragma unroll
        for (int k = 0; k < 12; ++k) pin_v(xb[k]);
        asm volatile("s_nop 1");
        u32x2 hv = ld64(rw, po, (unsigned)(O_HF + (size_t)u * XF_B)), xv = ld64(rw, po, (unsigned)(O_X1F + (size_t)u * XF_B));
        const int un = u + 1 < NUNIT ? u + 1 : u;
        f32x4 acc[4];
        sfor<0, 48>([&](auto I) __attribute__((always_inline)) {
            constexpr int i = decltype(I)::value, t = i / 12, k = i % 12;
            mf_av(acc[t], w[t][k], xb[k], k == 0);
            if constexpr (i % 2 == 0 && i < 24) { constexpr int kk = i / 2; nx[kk] = ld128(rw, l16 + (kk & 3) * 1024, (unsigned)(O_G3F + (size_t)un * G3_B) + (wave * 12 + (kk & ~3)) * 1024); }
            if constexpr (t >= 1 && k == 8) { pin_v(acc[t - 1]); *(LAS f32x4*)(wr[t - 1] + (u & 1) * 12288) = acc[t - 1]; }
        });
        bar();
        f32x4 p[3];
#pragma unroll
        for (int j = 0; j < 3; ++j) p[j] = *(LAS const f32x4*)(rd + (u & 1) * 12288 + j * 1024);
        mf_done(acc[3]);
        const f32x4 o = (acc[3] + p[0]) + (p[1] + p[2]);
        MARK(2);
        for (unsigned c = 0; any_lane(max(max(hv.x, hv.y), max(xv.x, xv.y)) == SENT);) {
            if (sp.fail(c, 0xC00 + u)) break;
            hv = ld64(rw, po, (unsigned)(O_HF + (size_t)u * XF_B)); xv = ld64(rw, po, (unsigned)(O_X1F + (size_t)u * XF_B));
        }
        miss = any_sent(nx);
        const u32x2 r = {pk(bflo(hv.x) + (bflo(xv.x) + o[0]), bfhi(hv.x) + (bfhi(xv.x) + o[1])), pk(bflo(hv.y) + (bflo(xv.y) + o[2]), bfhi(hv.y) + (bfhi(xv.y) + o[3]))};
        __builtin_amdgcn_raw_buffer_store_b64(r, ro, ((16 * u + n) * D_ + 16 * ct + 4 * g) * 2, 0, 0);
        MARK(3); FLUSH(u);
    };
    for (int u = 0; u < NUNIT; u += 2) { unit(u, xa, xc); unit(u + 1, xc, xa); }
}

DEV unsigned char* ws_base(const Params& P) { return P.ws + ((0u - (unsigned)(uintptr_t)P.ws) & ((2u << 20) - 1u)); }

__global__ void __launch_bounds__(256, 1) k_main(Params P) {
    extern __shared__ __attribute__((aligned(16))) char smem[];
    LAS char* lds = (LAS char*)smem;
    const int tid = threadIdx.x, lane = tid & 63, wave = __builtin_amdgcn_readfirstlane(tid >> 6);
    unsigned char* ws = ws_base(P);
    gu32* ctrl = (gu32*)(ws + O_CTRL);
    if (tid == 0) {
        unsigned x = xcc_id() & 7u, slot = 0;
        for (int k = 0; k < 8; ++k) {
            const unsigned xx = (x + k) & 7u;
            const unsigned t = __hip_atomic_fetch_add(ctrl + W_TICKET + 32 * xx, 1u, RLX_AGENT);
            if (t < 32u) { slot = xx * 32 + t; break; }
        }
        *(LAS unsigned*)lds = slot;
    }
    __syncthreads();
    const int slot = __builtin_amdgcn_readfirstlane(*(LAS unsigned*)lds);
    __syncthreads();
    Spin sp; sp.tmo = ctrl + W_TMO; sp.dead = false;
    const int xq = slot >> 5, t = slot & 31;
    int mlp = -1;
    if (xq < 4) {
        if (t < 8) role_S(P, ws, xq * 8 + t, lds, wave, lane, sp);
        else if (t < 18) role_F(P, ws, xq * 10 + (t - 8), lds, wave, lane, sp);
        else if (t < 26) role_UV(P, ws, xq * 8 + (t - 18), lds, wave, lane, sp);
        else mlp = xq * 6 + (t - 26);
    } else {
        const int y = xq - 4;
        if (t < 16) role_VC(P, ws, y * 8 + (t >> 1), t & 1, lds, wave, lane, sp);
        else if (t < 28) role_QK(P, ws, y * 12 + (t - 16), lds, wave, lane, sp);
        else if (t < 30) mlp = 24 + y * 2 + (t - 28);
    }
    if (mlp >= 0) {
        if (mlp < 5) role_W<1>(P, ws, mlp, lds, wave, lane, sp);
        else if (mlp < 10) role_W<2>(P, ws, mlp - 5, lds, wave, lane, sp);
        else if (mlp < 22) role_W<3>(P, ws, mlp - 10, lds, wave, lane, sp);
        else role_W4(P, ws, mlp - 22, lds, wave, lane, sp);
    }
    MARK(0); MARKC(1); FLUSH(129);
}

__global__ void __launch_bounds__(256) k_pre(Params P) {
    __shared__ float red[2][4][16];
    const int tid = threadIdx.x, b = blockIdx.x;
    unsigned char* ws = ws_base(P);
    if (b < 128) {
        const int r = tid & 15, pc = tid >> 4, wave = tid >> 6;
        u32x4 v[5]; float s1 = 0.f;
        for (int i = 0; i < 5; ++i) {
            v[i] = *(const u32x4*)(P.x + (size_t)(16 * b + r) * D_ + (pc + 16 * i) * 8);
            for (int j = 0; j < 4; ++j) s1 += bflo(v[i][j]) + bfhi(v[i][j]);
        }
        s1 = x32(x16(s1, false), false);
        if ((tid & 63) < 16) red[0][wave][r] = s1;
        __syncthreads();
        const float mu = (red[0][0][r] + red[0][1][r] + red[0][2][r] + red[0][3][r]) * (1.f / D_);
        float s2 = 0.f;
        for (int i = 0; i < 5; ++i) for (int j = 0; j < 4; ++j) { const float a = bflo(v[i][j]) - mu, c = bfhi(v[i][j]) - mu; s2 += a * a + c * c; }
        s2 = x32(x16(s2, false), false);
        if ((tid & 63) < 16) red[1][wave][r] = s2;
        __syncthreads();
        const float rs = rsqrtf((red[1][0][r] + red[1][1][r] + red[1][2][r] + red[1][3][r]) * (1.f / D_) + 1e-5f);
        for (int i = 0; i < 5; ++i) {
            u32x4 o;
            for (int j = 0; j < 4; ++j) o[j] = pk((bflo(v[i][j]) - mu) * rs, (bfhi(v[i][j]) - mu) * rs);
            *(u32x4*)(ws + O_XF + (size_t)b * XF_B + (size_t)(pc + 16 * i) * 256 + r * 16) = o;
        }
    } else if (b < 160) {
        const int pg = b - 128, wave = tid >> 6;
        const rsrc_t rw = mk_rsrc(ws, (unsigned)WS_END);
        const unsigned o = (unsigned)pg * (2u << 20) + (2u << 20) - 16384u + wave * 2048;
        unsigned lat[2] = {0u, 0u};
        if ((tid & 63) == 0) {
            st32(1u, rw, o, 0); st32(1u, rw, o + 8192, 0); st32(1u, rw, o + 1024, 0); st32(1u, rw, o + 8192 + 1024, 0);
            vmwait<0>();
            lat[0] = lat[1] = ~0u;
            for (int i = 0; i < 4; ++i) {
                unsigned long long t0, t1;
                asm volatile("s_memtime %0\n\ts_waitcnt lgkmcnt(0)" : "=s"(t0) :: "memory");
                unsigned v = ld32(rw, o + (i & 1) * 8192 + (i >> 1) * 1024, 0);
                asm volatile("s_waitcnt vmcnt(0)\n\ts_memtime %0\n\ts_waitcnt lgkmcnt(0)" : "=s"(t1), "+v"(v) :: "memory");
                const unsigned d = (unsigned)(t1 - t0);
                if (i & 1) lat[1] = min(lat[1], d); else lat[0] = min(lat[0], d);
            }
            red[0][wave][0] = (float)lat[0]; red[0][wave][1] = (float)lat[1];
        }
        __syncthreads();
        if (tid == 0) {
            const float l0 = fminf(fminf(red[0][0][0], red[0][1][0]), fminf(red[0][2][0], red[0][3][0]));
            const float l1 = fminf(fminf(red[0][0][1], red[0][1][1]), fminf(red[0][2][1], red[0][3][1]));
            ((unsigned*)(ws + O_PAR))[pg] = (unsigned)((l1 < l0) != ((xcc_id() & 4u) != 0u));
            if (pg == 0) ((unsigned*)(ws + O_PAR))[32] = rawnow() - 1000u;
        }
    } else if (b < 163) {
        const int p = (b - 160) * 256 + tid;
        if (p < 640) {
            const unsigned* src = (const unsigned*)(P.mem0 + ((p >> 1) & 7) * D_ + (p >> 4) * 16 + (p & 1) * 2);
            const u32x4 v = {min(src[0], SENT - 1u), min(src[2], SENT - 1u), min(src[4], SENT - 1u), min(src[6], SENT - 1u)};
            const unsigned o = p * 16, oa = ((o >> 13) << 14) | (o & 8191u);
            *(u32x4*)(ws + oa) = v; *(u32x4*)(ws + oa + 8192) = v;
        }
    } else if (b < 192) {
        const int i = (b - 163) * 256 + tid;
        if (i < (int)(CTRL_BYTES / 16)) ((u32x4*)(ws + O_CTRL))[i] = u32x4{0u, 0u, 0u, 0u};
        else if (i < (int)(CTRL_BYTES / 16) + S_ / 2) ((u32x4*)(ws + O_ST))[i - CTRL_BYTES / 16] = SENT4;
    } else {
        const int nb = gridDim.x - 192, q = (b - 192) * 256 + tid;
        constexpr int RU = REC_BLOCKS * 1024;
        for (int i = q; i < KARM * RU; i += nb * 256) { const int r = i / RU, j = i % RU; if (r > 0 || j >= 2048) ((u32x4*)(ws + (size_t)r * REC_B))[j] = SENT4; }
        constexpr int NA = (2 * KARM + 1) * (int)(XF_B / 16), NG = 2 * KARM * (int)(G3_B / 16);
        for (int i = q; i < NA; i += nb * 256) { ((u32x4*)(ws + O_HF))[i] = SENT4; ((u32x4*)(ws + O_GF))[i] = SENT4; ((u32x4*)(ws + O_X1F))[i] = SENT4; }
        for (int i = q; i < NG; i += nb * 256) ((u32x4*)(ws + O_G3F))[i] = SENT4;
    }
}

extern "C" void kernel_launch(void* const* d_in, const int* in_sizes, int n_in, void* d_out, int out_size, void* d_ws, size_t ws_size, hipStream_t stream) {
    Params P;
    P.x = (const unsigned short*)d_in[0]; P.mem0 = (const unsigned short*)d_in[1];
    P.wq = (const unsigned short*)d_in[2]; P.wk = (const unsigned short*)d_in[3]; P.wv = (const unsigned short*)d_in[4]; P.wo = (const unsigned short*)d_in[5];
    P.w1 = (const unsigned short*)d_in[8]; P.w2 = (const unsigned short*)d_in[9]; P.w3 = (const unsigned short*)d_in[10]; P.w4 = (const unsigned short*)d_in[11];
    P.out = (unsigned short*)d_out; P.ws = (unsigned char*)d_ws;
    (void)hipFuncSetAttribute((const void*)k_main, hipFuncAttributeMaxDynamicSharedMemorySize, LDS_BYTES);
    hipLaunchKernelGGL(k_pre, dim3(512), dim3(256), 0, stream, P);
    hipLaunchKernelGGL(k_main, dim3(256), dim3(256), LDS_BYTES, stream, P);
}
```

```cpp
#include <hip/hip_runtime.h>
#include <stdint.h>

#define GAS __attribute__((address_space(1)))
#define LAS __attribute__((address_space(3)))
#define RLX_AGENT __ATOMIC_RELAXED, __HIP_MEMORY_SCOPE_AGENT
#define DEV __device__ __forceinline__
typedef __bf16 bf16x2 __attribute__((ext_vector_type(2)));
typedef __bf16 bf16x8 __attribute__((ext_vector_type(8)));
typedef __bf16 bf16x16 __attribute__((ext_vector_type(16)));
typedef short s16x4 __attribute__((ext_vector_type(4)));
typedef float f32x2 __attribute__((ext_vector_type(2)));
typedef float f32x4 __attribute__((ext_vector_type(4)));
typedef unsigned u32x2 __attribute__((ext_vector_type(2)));
typedef unsigned u32x4 __attribute__((ext_vector_type(4)));
typedef unsigned u32x8 __attribute__((ext_vector_type(8)));
typedef GAS unsigned gu32;
typedef __amdgpu_buffer_rsrc_t rsrc_t;

constexpr int S_ = 2048, D_ = 640, H_ = 32, E_ = 192, F_ = 1536, NSEG = 64, NUNIT = 128;
constexpr unsigned SENT = 0xFFFFFFFFu;
constexpr unsigned SPINCAP = 1u << 16;

constexpr unsigned PAGE_B = 2u << 20;
constexpr int NSLOT = 8;
constexpr unsigned R_MEM = 0;
constexpr unsigned R_P = 2 * 8192;
constexpr unsigned R_UB = 5 * 8192;
constexpr unsigned R_PX = 45 * 8192;
constexpr unsigned PX_B = 40960;
constexpr unsigned R_NF = 55 * 8192;
constexpr unsigned R_NS = 56 * 8192;
constexpr int REC_BLOCKS = 57;
#define DELTA_A 15
#define DELTA_B 15
#define VIS 4
#define SLOW 105
#define BULK_FAR 1
#define PSLEEP 4
#define PWAIT 3
#define DLY1 0
#define DLY2 1500
#define DLY3 0
#define NTMASK 0
#define ROTB 0
#define LATE 40
#define PACE 10
constexpr unsigned amap(unsigned off) { return ((off >> 13) << 14) | (off & 8191u); }
constexpr int KARM = 4;
constexpr size_t O_CTRL = (size_t)NSLOT * 4 * PAGE_B, CTRL_BYTES = 65536;
constexpr int W_TMO = 0, W_TICKET = 64  , W_QKPROG = 1024  , W_VCPROG = 4096  , W_PROG = 8192  ;
constexpr size_t O_PAR = O_CTRL + CTRL_BYTES;
constexpr size_t O_ST = O_PAR + 4096;
constexpr size_t O_HF = O_ST + S_ * 8, XF_B = 20480;
constexpr size_t O_GF = O_HF + NUNIT * XF_B;
constexpr size_t O_X1F = O_GF + NUNIT * XF_B;
constexpr size_t O_G3F = O_X1F + NUNIT * XF_B, G3_B = 49152;
constexpr size_t O_XF = O_G3F + NUNIT * G3_B;
constexpr unsigned QX_B = 12288, R_QX = 0, R_KX = H_ * QX_B;
constexpr size_t WS_END = O_XF + NUNIT * XF_B;
static_assert(WS_END < (238u << 20), "workspace");
constexpr int LDS_BYTES = 160 * 1024;

struct Params {
    const unsigned short *x, *mem0, *wq, *wk, *wv, *wo, *w1, *w2, *w3, *w4;
    unsigned short* out;
    unsigned char* ws;
};

DEV unsigned xcc_id() { return (unsigned)__builtin_amdgcn_s_getreg(20 | (3 << 11)) & 0xfu; }
DEV rsrc_t mk_rsrc(const void* p, unsigned bytes) { return __builtin_amdgcn_make_buffer_rsrc((void*)p, 0, bytes, 0x00020000); }
DEV u32x4 ld128(rsrc_t r, unsigned v, unsigned s) { return __builtin_amdgcn_raw_buffer_load_b128(r, v, s, 16); }
DEV u32x2 ld64(rsrc_t r, unsigned v, unsigned s) { return __builtin_amdgcn_raw_buffer_load_b64(r, v, s, 16); }
DEV unsigned ld32(rsrc_t r, unsigned v, unsigned s) { return __builtin_amdgcn_raw_buffer_load_b32(r, v, s, 16); }
DEV void st128(u32x4 d, rsrc_t r, unsigned v, unsigned s) { __builtin_amdgcn_raw_buffer_store_b128(d, r, v, s, 16); }
DEV void st64(u32x2 d, rsrc_t r, unsigned v, unsigned s) { __builtin_amdgcn_raw_buffer_store_b64(d, r, v, s, 16); }
DEV void st32(unsigned d, rsrc_t r, unsigned v, unsigned s) { __builtin_amdgcn_raw_buffer_store_b32(d, r, v, s, 16); }
DEV void st16(unsigned short d, rsrc_t r, unsigned v, unsigned s) { __builtin_amdgcn_raw_buffer_store_b16(d, r, v, s, 16); }
DEV u32x2 trd(LAS const char* p) { return __builtin_bit_cast(u32x2, __builtin_amdgcn_ds_read_tr16_b64_v4i16((LAS s16x4*)p)); }
DEV u32x4 cat2(u32x2 a, u32x2 b) { return __builtin_shufflevector(a, b, 0, 1, 2, 3); }
DEV u32x8 cat4(u32x4 a, u32x4 b) { return __builtin_shufflevector(a, b, 0, 1, 2, 3, 4, 5, 6, 7); }
DEV u32x4 lo4(u32x8 a) { return __builtin_shufflevector(a, a, 0, 1, 2, 3); }
DEV u32x4 hi4(u32x8 a) { return __builtin_shufflevector(a, a, 4, 5, 6, 7); }
DEV u32x4 lds128(LAS const char* p) { return *(LAS const u32x4*)p; }
DEV unsigned pk(float a, float b) { bf16x2 v = {(__bf16)a, (__bf16)b}; return __builtin_bit_cast(unsigned, v); }
DEV unsigned nosent(unsigned u) { return min(u, SENT - 1u); }
DEV unsigned pks(float a, float b) { return min(pk(a, b), SENT - 1u); }
DEV unsigned pka(float a, float b) { unsigned r; asm("v_cvt_pk_bf16_f32 %0, |%1|, |%2|" : "=v"(r) : "v"(a), "v"(b)); return r; }
DEV float bflo(unsigned u) { return __builtin_bit_cast(float, u << 16); }
DEV float bfhi(unsigned u) { return __builtin_bit_cast(float, u & 0xFFFF0000u); }
DEV unsigned f2u(float f) { return __builtin_bit_cast(unsigned, f); }
DEV float u2f(unsigned u) { return __builtin_bit_cast(float, u); }
DEV unsigned mx4(u32x4 v) { return max(max(v.x, v.y), max(v.z, v.w)); }
DEV bool any_lane(bool c) { return __builtin_amdgcn_ballot_w64(c) != 0; }
DEV void bar() { asm volatile("s_waitcnt lgkmcnt(0)" ::: "memory"); __builtin_amdgcn_s_barrier(); asm volatile("" ::: "memory"); }
DEV void fold32(float& a, float& b) {
    auto r = __builtin_amdgcn_permlane32_swap(f2u(a), f2u(b), false, false); a = u2f(r[0]); b = u2f(r[1]);
}
DEV float x32(float v, bool mx) { auto r = __builtin_amdgcn_permlane32_swap(f2u(v), f2u(v), false, false); return mx ? fmaxf(u2f(r[0]), u2f(r[1])) : u2f(r[0]) + u2f(r[1]); }
DEV float x16(float v, bool mx) { auto r = __builtin_amdgcn_permlane16_swap(f2u(v), f2u(v), false, false); return mx ? fmaxf(u2f(r[0]), u2f(r[1])) : u2f(r[0]) + u2f(r[1]); }
DEV float fmax2(float a, float b) { float r; asm("v_max_f32 %0, %1, %2" : "=v"(r) : "v"(a), "v"(b)); return r; }
DEV float fmax3(float a, float b, float c) { float r; asm("v_max3_f32 %0, %1, %2, %3" : "=v"(r) : "v"(a), "v"(b), "v"(c)); return r; }
#define ROWOP2(op) asm volatile("s_nop 1\n\t" \
    op " %0, %0, %0 row_ror:8 row_mask:0xf bank_mask:0xf\n\t" op " %1, %1, %1 row_ror:8 row_mask:0xf bank_mask:0xf\n\ts_nop 0\n\t" \
    op " %0, %0, %0 row_ror:4 row_mask:0xf bank_mask:0xf\n\t" op " %1, %1, %1 row_ror:4 row_mask:0xf bank_mask:0xf\n\ts_nop 0\n\t" \
    op " %0, %0, %0 row_ror:2 row_mask:0xf bank_mask:0xf\n\t" op " %1, %1, %1 row_ror:2 row_mask:0xf bank_mask:0xf\n\ts_nop 0\n\t" \
    op " %0, %0, %0 row_ror:1 row_mask:0xf bank_mask:0xf\n\t" op " %1, %1, %1 row_ror:1 row_mask:0xf bank_mask:0xf" : "+v"(a), "+v"(b))
DEV void row_max2(float& a, float& b) { ROWOP2("v_max_f32_dpp"); }
DEV void row_sum2(float& a, float& b) { ROWOP2("v_add_f32_dpp"); }
DEV unsigned pair_cols(unsigned p, unsigned psel) { return __builtin_amdgcn_perm(p, __builtin_amdgcn_update_dpp(0u, p, 0xB1, 0xf, 0xf, true), psel); }
DEV float ror8(float v) { return u2f(__builtin_amdgcn_update_dpp(0u, f2u(v), 0x128, 0xf, 0xf, true)); }
template <int OFF> DEV void glds(unsigned voff, const void* sbase, unsigned lds_dst) {
    asm volatile("s_mov_b32 m0, %2\n\ts_nop 0\n\tglobal_load_lds_dwordx4 %0, %1 offset:%3" :: "v"(voff), "s"(sbase), "s"(lds_dst), "n"(OFF) : "memory");
}
template <int N, int STEP> DEV void glds_dev(unsigned voff, const void* sbase, unsigned lds_dst) {
    static_assert(N == 2 || N == 4, "");
    if (N == 2) asm volatile("s_mov_b32 m0, %2\n\ts_nop 0\n\tglobal_load_lds_dwordx4 %0, %1 sc1\n\tglobal_load_lds_dwordx4 %0, %1 offset:%3 sc1" :: "v"(voff), "s"(sbase), "s"(lds_dst), "n"(STEP) : "memory");
    else asm volatile("s_mov_b32 m0, %2\n\ts_nop 0\n\tglobal_load_lds_dwordx4 %0, %1 sc1\n\tglobal_load_lds_dwordx4 %0, %1 offset:%3 sc1\n\tglobal_load_lds_dwordx4 %0, %1 offset:%4 sc1\n\tglobal_load_lds_dwordx4 %0, %1 offset:%5 sc1"
                      :: "v"(voff), "s"(sbase), "s"(lds_dst), "n"(STEP), "n"(2 * STEP), "n"(3 * STEP) : "memory");
}
template <int N> DEV void vmwait() { asm volatile("s_waitcnt vmcnt(%0)" :: "n"(N) : "memory"); }
template <class T> DEV void pin_a(T& v) { asm volatile("" : "+a"(v)); }
template <class T> DEV void pin_v(T& v) { asm volatile("" : "+v"(v)); }
#define MF_ "v_mfma_f32_16x16x32_bf16 "
DEV void smf_a(f32x4& c, u32x4 a, u32x8 b, int idx) { asm volatile("v_smfmac_f32_16x16x64_bf16 %0, %1, %2, %3" : "+v"(c) : "v"(a), "a"(b), "v"(idx)); }
DEV void smf_v(f32x4& c, u32x4 a, u32x8 b, int idx) { asm volatile("v_smfmac_f32_16x16x64_bf16 %0, %1, %2, %3" : "+v"(c) : "v"(a), "v"(b), "v"(idx)); }
DEV void mf_vv(f32x4& c, u32x4 a, u32x4 b, bool first) { if (first) asm volatile(MF_ "%0, %1, %2, 0" : "=&v"(c) : "v"(a), "v"(b)); else asm volatile(MF_ "%0, %1, %2, %0" : "+v"(c) : "v"(a), "v"(b)); }
DEV void mf_av(f32x4& c, u32x4 a, u32x4 b, bool first) { if (first) asm volatile(MF_ "%0, %1, %2, 0" : "=&v"(c) : "a"(a), "v"(b)); else asm volatile(MF_ "%0, %1, %2, %0" : "+v"(c) : "a"(a), "v"(b)); }
DEV void mf_va(f32x4& c, u32x4 a, u32x4 b, bool first) { if (first) asm volatile(MF_ "%0, %1, %2, 0" : "=&v"(c) : "v"(a), "a"(b)); else asm volatile(MF_ "%0, %1, %2, %0" : "+v"(c) : "v"(a), "a"(b)); }
DEV void mf_done(f32x4& a) { asm volatile("s_nop 7" : "+v"(a)); }
DEV void mf_done(f32x4& a, f32x4& b) { asm volatile("s_nop 7" : "+v"(a), "+v"(b)); }
DEV void mf_done(f32x4& a, f32x4& b, f32x4& c) { asm volatile("s_nop 7" : "+v"(a), "+v"(b), "+v"(c)); }
DEV void mf_done(f32x4& a, f32x4& b, f32x4& c, f32x4& d) { asm volatile("s_nop 7" : "+v"(a), "+v"(b), "+v"(c), "+v"(d)); }
DEV float zero1() { float v; asm volatile("v_mov_b32 %0, 0" : "=v"(v)); return v; }
DEV void zero_acc(f32x4& a) { a = f32x4{zero1(), zero1(), zero1(), zero1()}; asm volatile("s_nop 1" : "+v"(a)); }
DEV LAS char* opq(LAS char* p) { unsigned u = (unsigned)(uintptr_t)p; asm volatile("" : "+v"(u)); return (LAS char*)(uintptr_t)u; }
template <int V> struct IC { static constexpr int value = V; constexpr operator int() const { return V; } };
template <int I, int N, class F> DEV void sfor(F&& f) { if constexpr (I < N) { f(IC<I>{}); sfor<I + 1, N>(f); } }
DEV void lgkm0() { asm volatile("s_waitcnt lgkmcnt(0)" ::: "memory"); __builtin_amdgcn_sched_barrier(0); }

#define MARK(i)
#define MARK2(i)
#define MARK3(i)
#define SETV(i, v)
#define MARKC(i)
#define FLUSH(step)
struct Spin {
    gu32* tmo; bool dead;
    DEV bool fail(unsigned& n, unsigned code) {
        asm volatile("" ::: "memory");
        if (dead) return true;
        ++n;
        if (n > SPINCAP) { __hip_atomic_store(tmo, code, RLX_AGENT); dead = true; return true; }
        if ((n & 63u) == 0u && __builtin_amdgcn_readfirstlane(__hip_atomic_load(tmo, RLX_AGENT)) != 0) { dead = true; return true; }
        return false;
    }
};

template <int NT> DEV int img_rot(int rho) { return (NT & 1) ? 0 : (NT & 2) ? ((rho >> 2) & 1) : (NT & 4) ? ((rho >> 1) & 3) : (rho & 7); }
DEV int swap23(int r) { return (r & 3) | ((r >> 1) & 4) | ((r << 1) & 8) | (r & 16); }
template <int NT> struct WSrc {
    unsigned voff[NT]; rsrc_t r;
    DEV void init(const char* src, int lane, unsigned stride, int trot = 0) {
        r = mk_rsrc(src - 4096, 0xFFFFFFFFu);
#pragma unroll
        for (int i = 0; i < NT; ++i) {
            const int P = 64 * i + lane, rho = P / (2 * NT), cs = P % (2 * NT);
            const int tile = (((cs >> 1) ^ img_rot<NT>(rho)) + trot) % NT;
            voff[i] = (unsigned)swap23(rho) * stride + (unsigned)(2 * tile + (cs & 1)) * 16u + 4096u - (i & 3) * 1024u;
        }
    }
    DEV void issue(unsigned so, unsigned lds_dst) {
        constexpr bool nt = (NTMASK >> (NT == 6 ? 1 : NT == 10 ? 2 : NT == 3 ? 3 : 0)) & 1;
#define BL_(v, o, p) "buffer_load_dwordx4 " v ", %0, %1 offen offset:" o p " lds\n\t"
#define GRP_(p) \
            if (i + 4 <= NT) asm volatile("s_mov_b32 m0, %2\n\ts_nop 0\n\t" BL_("%3", "0", p) BL_("%4", "1024", p) BL_("%5", "2048", p) BL_("%6", "3072", p) :: "s"(r), "s"(so), "s"(d), "v"(voff[i]), "v"(voff[(i + 1) % NT]), "v"(voff[(i + 2) % NT]), "v"(voff[(i + 3) % NT]) : "memory"); \
            else if (i + 3 == NT) asm volatile("s_mov_b32 m0, %2\n\ts_nop 0\n\t" BL_("%3", "0", p) BL_("%4", "1024", p) BL_("%5", "2048", p) :: "s"(r), "s"(so), "s"(d), "v"(voff[i]), "v"(voff[(i + 1) % NT]), "v"(voff[(i + 2) % NT]) : "memory"); \
            else asm volatile("s_mov_b32 m0, %2\n\ts_nop 0\n\t" BL_("%3", "0", p) BL_("%4", "1024", p) :: "s"(r), "s"(so), "s"(d), "v"(voff[i]), "v"(voff[(i + 1) % NT]) : "memory");
#pragma unroll
        for (int i = 0; i < NT; i += 4) {
            const unsigned d = lds_dst + i * 1024;
            if (nt) { GRP_(" nt") } else { GRP_("") }
        }
#undef GRP_
#undef BL_
    }
};
template <int NT, bool PAIR> struct WRd {
    static constexpr int RR = (NT & 1) ? 1 : (NT & 2) ? 2 : (NT & 4) ? 4 : 8, NB = PAIR ? RR / 2 : RR;
    LAS char* b[NB];
    DEV void init(LAS char* base, int lane) {
        const int g = lane >> 4, q = (lane >> 2) & 3, p = lane & 3, rho = q | ((g & 1) << 2) | ((g >> 1) << 4), rot = img_rot<NT>(rho);
#pragma unroll
        for (int j = 0; j < NB; ++j) b[j] = opq(base + rho * (NT * 32) + (PAIR ? ((2 * j + (p >> 1)) ^ rot) * 32 + 16 * (p & 1) : (j ^ rot) * 32 + 8 * p));
    }
    DEV LAS char* at(int off, int tile) const { return PAIR ? b[(tile >> 1) % NB] + off + (tile / RR) * RR * 32 + 8 * (tile & 1) : b[tile % NB] + off + (tile / RR) * RR * 32; }
    DEV u32x4 get(int off, int tile) const { LAS char* a = at(off, tile); return cat2(trd(a), trd(a + 8 * NT * 32)); }
};
DEV void vmwait_n(int n) {
    switch (n) {
#define VW(n) case n: vmwait<n>(); break;
        VW(0) VW(3) VW(4) VW(6) VW(8) VW(9) VW(10) VW(12) VW(15) VW(16) VW(18) VW(20) VW(24) VW(26) VW(28) VW(29) VW(30) VW(32) VW(35) VW(36) VW(40)
#undef VW
        default: vmwait<0>(); break;
    }
}
template <int NT, int NK, int NKT, int RING, int SROT, bool PAIR, class Sink>
DEV void stream_w(const char* src, unsigned stride, LAS char* lds, unsigned base, int lane, Sink&& sink, int rot = 0, int trot = 0) {
    auto krow = [&](int k) { int kk = k + 2 * rot; if (kk >= NKT) kk -= NKT; return (unsigned)kk * 32u * stride; };
    WSrc<NT> ws; ws.init(src, lane, stride, trot);
    const unsigned lbase = (unsigned)(uintptr_t)lds + base;
#pragma unroll
    for (int k = 0; k < RING && k < NKT; ++k) ws.issue(krow(k), lbase + ((k + SROT) % RING) * NT * 1024);
    WRd<NT, PAIR> rd; rd.init(lds + base, lane);
#pragma unroll
    for (int k = 0; k < NK; ++k) {
        const int issued = (k + RING < NKT) ? k + RING : NKT;
        vmwait_n((issued - k - 1) * NT);
        u32x4 fr[NT];
#pragma unroll
        for (int t = 0; t < NT; ++t) fr[t] = rd.get(((k + SROT) % RING) * NT * 1024, t);
#pragma unroll
        for (int t = 0; t < NT; ++t) sink(t, k, fr[t]);
        if (k + RING < NKT) { lgkm0(); ws.issue(krow(k + RING), lbase + ((k + SROT) % RING) * NT * 1024); }
    }
}

DEV unsigned page(unsigned par, int s, int p) { const unsigned i = (unsigned)(s & (NSLOT - 1)) * 4u + (unsigned)p; return i * PAGE_B + (((par >> i) & 1u) << 13); }
DEV unsigned rec(unsigned par, int s) { return page(par, s, 0); }
DEV void wait_slot(rsrc_t rw, int s, unsigned& seen, int rep, Spin& sp) {
    for (unsigned c = 0; (int)seen + 6 < s;) {
        seen = __builtin_amdgcn_readfirstlane(ld32(rw, 0, (unsigned)(O_CTRL + 4 * (W_PROG + 32 * rep))));
        if ((int)seen + 6 >= s || sp.fail(c, 0x280 + s)) break;
        __builtin_amdgcn_s_sleep(8);
    }
}
DEV unsigned load_par(rsrc_t rw, int lane) {
    unsigned m = (unsigned)__builtin_amdgcn_ballot_w64(lane < 32 && __builtin_amdgcn_raw_buffer_load_b32(rw, (lane & 31) * 4, (unsigned)O_PAR, 0) == 1u);
    const int c = __builtin_popcount(m ^ 0xAAAAAAAAu);
    if (c <= 6) m = 0xAAAAAAAAu; else if (c >= 26) m = 0x55555555u;
    return m ^ (BULK_FAR ? 0xEEEEEEEEu : 0u);
}
DEV unsigned sent1() { unsigned v; asm volatile("v_mov_b32 %0, -1" : "=v"(v)); return v; }
#define SENT4 (u32x4{sent1(), sent1(), sent1(), sent1()})
#define SENT2 (u32x2{sent1(), sent1()})
DEV unsigned rawnow() { unsigned long long v; asm volatile("s_memrealtime %0\n\ts_waitcnt lgkmcnt(0)" : "=s"(v) :: "memory"); return (unsigned)v; }
struct Clock {
    unsigned org;
    DEV void init(unsigned char* w) { org = __builtin_amdgcn_readfirstlane(((const unsigned*)(w + O_PAR))[32]); }
    DEV unsigned now() const { return rawnow() - org; }
    DEV unsigned wait_until(unsigned T) const {
        unsigned t = rawnow(); T += org;
        if ((int)(T - t) > 1000) T = t + 1000u;
        while ((int)(t - T) < 0) t = rawnow();
        return t - org;
    }
};
DEV unsigned umax_dpp(unsigned v, int k) {
    switch (k) { case 1: return max(v, (unsigned)__builtin_amdgcn_update_dpp(0u, v, 0x121, 0xf, 0xf, true)); case 2: return max(v, (unsigned)__builtin_amdgcn_update_dpp(0u, v, 0x122, 0xf, 0xf, true));
                 case 4: return max(v, (unsigned)__builtin_amdgcn_update_dpp(0u, v, 0x124, 0xf, 0xf, true)); default: return max(v, (unsigned)__builtin_amdgcn_update_dpp(0u, v, 0x128, 0xf, 0xf, true)); }
}
DEV unsigned half_max(unsigned v) {
    v = umax_dpp(umax_dpp(umax_dpp(umax_dpp(v, 1), 2), 4), 8);
    auto r = __builtin_amdgcn_permlane16_swap(v, v, false, false);
    return max((unsigned)r[0], (unsigned)r[1]);
}
DEV unsigned wave_max(unsigned v) { const unsigned m = half_max(v); return max((unsigned)__builtin_amdgcn_readlane(m, 0), (unsigned)__builtin_amdgcn_readlane(m, 32)); }
DEV u32x2 read_notes(u32x2 nf) {
    const unsigned tp = wave_max(nf.x == SENT ? 0u : nf.x), df = wave_max(nf.y == SENT ? 0u : min(nf.y, 400u));
    return u32x2{tp, tp + df + DELTA_B};
}
DEV unsigned mem_time(unsigned tp, unsigned plan, unsigned tn) { return tn + VIS > tp ? max(plan, tn + SLOW) : plan; }
struct MemStream {
    rsrc_t rw; unsigned l16, vn, lbase, ko[10];
    static DEV int rot_of(int base, int wave) { return (ROTB * base + ((wave * 5 + 1) >> 1)) % 10; }
    DEV void init(rsrc_t r, LAS char* lds, int lane, int rot, bool reader) {
        rw = r; l16 = lane * 16; vn = reader ? min(lane, 39) * 128 : (lane & 15) * 8; lbase = (unsigned)(uintptr_t)lds;
#pragma unroll
        for (int k = 0; k < 10; ++k) { int kk = k + rot; if (kk >= 10) kk -= 10; ko[k] = kk * 1024 + (kk >= 8 ? 8192 : 0); }
    }
    DEV u32x4 ld(unsigned rb, int kb) const { return ld128(rw, l16, rb + ko[kb]); }
    DEV unsigned so(unsigned rb, int kb) const { unsigned v = rb + ko[kb]; asm volatile("" : "+s"(v)); return v; }
    DEV u32x4 lds_(unsigned s) const { u32x4 v = ld128(rw, l16, s); __builtin_amdgcn_sched_barrier(0); return v; }
    DEV LAS char* at(int kb) const { unsigned a; asm volatile("v_add_u32 %0, %1, %2" : "=v"(a) : "s"(ko[kb] + lbase), "v"(l16)); return (LAS char*)(uintptr_t)a; }
    DEV void share(unsigned so, int kb) const {
        asm volatile("s_mov_b32 m0, %3\n\ts_nop 0\n\tbuffer_load_dwordx4 %0, %1, %2 offen sc1 lds" :: "v"(l16), "s"(rw), "s"(so), "s"(ko[kb] + lbase) : "memory");
    }
    DEV void lda(u32x4& a, unsigned so) const { asm volatile("buffer_load_dwordx4 %0, %1, %2, %3 offen sc1" : "=v"(a) : "v"(l16), "s"(rw), "s"(so)); }
    DEV u32x4 get(int kb) const { return lds128(at(kb)); }
    static DEV unsigned long long bad(u32x4 a, u32x4 b, u32x4 c, u32x4 d) {
        unsigned long long m = __builtin_amdgcn_ballot_w64(max(max(mx4(a), mx4(b)), max(mx4(c), mx4(d))) == SENT); asm volatile("" : "+s"(m)); return m;
    }
};
template <int N> DEV void vmw(u32x4& a) { asm volatile("s_waitcnt vmcnt(%1)" : "+v"(a) : "n"(N)); }
DEV unsigned long long nan2(float a, float b) { unsigned long long m; asm volatile("v_cmp_u_f32 %0, %1, %2" : "=s"(m) : "v"(a), "v"(b)); return m; }
DEV float add1(float a, float b) { float r; asm volatile("v_add_f32 %0, %1, %2" : "=v"(r) : "v"(a), "v"(b)); return r; }
DEV unsigned pk1(float a, float b) { unsigned r; asm volatile("v_cvt_pk_bf16_f32 %0, %1, %2" : "=v"(r) : "v"(a), "v"(b)); return r; }
DEV unsigned track(unsigned est, unsigned m, int s) { return m > est ? min(m, est + (s < 8 ? 30u : 3u)) : est - 1u - ((est - m) >> 3); }
struct AFrag {
    LAS char *p0, *p1;
    DEV void init(LAS char* img, int lane) {
        const int ga = lane >> 4, base = ga * 256 + (lane & 15) * 8, sw = (ga & 1) * 128;
        p0 = opq(img + base + sw); p1 = opq(img + base + (128 ^ sw));
    }
    DEV u32x4 get(int off) const { return cat2(trd(p0 + off), trd(p1 + off)); }
    static DEV int wofs(int lane, int odd) { const int g = lane >> 4, n = lane & 15; return (((2 * (n >> 2) + (n & 1)) * 32) ^ (odd * 128)) + g * 8 + ((n >> 1) & 1) * 2; }
};
DEV unsigned fold_pk(f32x4 c, float& f0, float& f1) {
    unsigned r;
    asm volatile("v_add_f32 %1, %3, %4\n\tv_add_f32 %2, %5, %6\n\tv_cvt_pk_bf16_f32 %0, %1, %2" : "=v"(r), "=&v"(f0), "=&v"(f1) : "v"(c[0]), "v"(c[1]), "v"(c[2]), "v"(c[3]));
    return r;
}
DEV void st2x16(LAS char* p0, LAS char* p1, int off, unsigned v) { *(LAS unsigned short*)(p0 + off) = (unsigned short)v; *(LAS unsigned short*)(p1 + off) = (unsigned short)(v >> 16); }

constexpr float SCALE_L2E = 0.07216878364870322f * 1.4426950408889634f;

DEV void role_S(const Params& P, unsigned char* ws, int h, LAS char* lds, int wave, int lane, Spin& sp) {
    constexpr int L_QK = 8192, L_FL = 14336, L_W = 16384, L_WS = 36864, L_KX = L_W + L_WS, L_QX = L_W + 3 * L_WS;
    const rsrc_t rw = mk_rsrc(ws, (unsigned)WS_END);
    const int rot = MemStream::rot_of(h, wave);
    u32x8 w[6][8];
    {
        const char* src = (const char*)(wave < 2 ? P.wq : P.wk) + h * 384 + (wave & 1) * 192;
        stream_w<6, 16, 20, 6, 4, false>(src, 12288u, lds, L_W + wave * L_WS, lane,
            [&](int t, int k, u32x4 f) {
                if (k & 1) { w[t][k >> 1] = cat4(lo4(w[t][k >> 1]), f); if ((k >> 1) * 6 + t < 32) pin_a(w[t][k >> 1]); } else w[t][k >> 1] = cat4(f, f); }, rot);
        vmwait<0>();
    }
    MARK(2); FLUSH(128);
    bar();
    const unsigned par = load_par(rw, lane);
    const int idx = (lane & 1) ? 0xEEEE : 0x4444;
    const int g = lane >> 4, n = lane & 15;
    const unsigned cq = (unsigned)(O_CTRL + 4 * (W_QKPROG + 32 * (((wave < 2 ? 384 : 0) + h * 12 + (lane & 1) * 11) >> 4)));
    unsigned have = 0;
    auto wait_qk = [&](int s) {
        if (have > (unsigned)s) return;
        for (unsigned c = 0;;) {
            const unsigned v = ld32(rw, cq, 0);
            have = min((unsigned)__builtin_amdgcn_readlane(v, 0), (unsigned)__builtin_amdgcn_readlane(v, 1));
            if (have > (unsigned)s || sp.fail(c, 0x200 + s)) break;
            __builtin_amdgcn_s_sleep(2);
        }
    };
    Clock ck; ck.init(ws);
    MemStream ms; ms.init(rw, lds, lane, rot, wave == 1);
    volatile LAS unsigned* lfail = (volatile LAS unsigned*)(lds + L_FL);
    if (wave == 0) *lfail = ~0u;
    WRd<6, false> wr; wr.init(lds + L_W + wave * L_WS + 12288, lane);
    AFrag af; af.init(lds + L_QK, lane);
    LAS char* we = opq(lds + L_QK + wave * 1536 + AFrag::wofs(lane, 0));
    LAS char* wo = opq(lds + L_QK + wave * 1536 + AFrag::wofs(lane, 1));
    const unsigned psel = (n & 1) ? 0x07060302u : 0x01000504u, psel2 = (n & 2) ? 0x07060302u : 0x01000504u;
    unsigned so1, so2;
    if (wave == 0) {
        const int i = 2 * g + (n & 1), half = (n >> 1) & 1;
        so1 = amap(R_P) + (h >> 4) * 16384 + (h & 15) * 512 + ((n >> 3) * 16 + 2 * i + half) * 16 + ((n >> 2) & 1) * 8;
        so2 = amap(R_P + 16384) + h * 128 + (2 * (2 * g + half) + ((n >> 2) & 1)) * 8 + (n >> 3) * 4;
    } else {
        const unsigned b = amap(R_PX) + (wave & 1) * amap(PX_B);
        so1 = (wave & 1) ? b + (h >> 3) * 16384 + (h & 7) * 1024 + lane * 16 : b + (h >> 4) * 16384 + ((h >> 1) & 7) * 1024 + lane * 16 + (h & 1) * 8;
        so2 = b + 65536 + h * 256 + n * 16 + g * 4;
    }
    unsigned xo[3];
#pragma unroll
    for (int j = 0; j < 3; ++j) { const unsigned o = (wave < 2 ? R_KX : R_QX) + h * QX_B + j * 4096; xo[j] = (((o >> 13) << 14) | (o & 8191u)) + (wave < 2 ? 0 : (wave & 1) * 1024); }
    auto stage_k = [&](int s) __attribute__((always_inline)) {
        wait_qk(s);
#pragma unroll
        for (int j = 0; j < 3; ++j) glds_dev<4, 1024>(ms.l16, ws + (page(par, s, 1) + xo[j]), (unsigned)(uintptr_t)lds + L_KX + (s & 1) * L_WS + j * 4096);
    };
    auto stage_q = [&](int s) __attribute__((always_inline)) {
        wait_qk(s);
#pragma unroll
        for (int j = 0; j < 3; ++j) glds_dev<2, 2048>(ms.l16, ws + (page(par, s, 1) + xo[j]), (unsigned)(uintptr_t)lds + L_QX + (wave & 1) * 1024 + j * 4096);
    };
    auto lwq = [&](int j, int q) {
        const int t = j % 6, kb = 1 - j / 6;
        return trd(wr.at((2 * kb + (q >> 1)) * 6144 + (q & 1) * 1536, t));
    };
    unsigned dur = 150, tn = 0;
    if (wave == 0) __builtin_amdgcn_s_setprio(3);
    if (wave == 1) stage_k(0); else if (wave >= 2) stage_q(0);
    vmwait<0>();
    bar();
    if (wave == 1) stage_k(1);
    for (int s = 0; s < NSEG; ++s) {
        const unsigned rb = rec(par, s);
        f32x4 acc[6];
        u32x2 st[12][4], nf; u32x4 a[10], kxf[12];
        LAS const char* kx = opq(lds + L_KX + (s & 1) * L_WS + lane * 16);
        unsigned long long bad = 0;
        auto prep = [&]() __attribute__((always_inline)) {
#pragma unroll
            for (int t = 0; t < 6; ++t) zero_acc(acc[t]);
#pragma unroll
            for (int i = 0; i < 12; ++i) st[i >> 2][i & 3] = lwq(i >> 2, i & 3);
        };
        float fa[6], fb[6]; unsigned fp[6];
        auto fin = [&](int t, int step) __attribute__((always_inline)) {
            if (step == 0) fa[t] = add1(acc[t][0], acc[t][1]);
            if (step == 1) fb[t] = add1(acc[t][2], acc[t][3]);
            if (step == 2) fp[t] = pk1(fa[t], fb[t]);
            if (step == 3) *(LAS unsigned short*)(((t & 1) ? wo : we) + t * 256) = (unsigned short)fp[t];
            if (step == 4) *(LAS unsigned short*)(((t & 1) ? wo : we) + t * 256 + 4) = (unsigned short)(fp[t] >> 16);
        };
        unsigned long long bad2 = 0;
        unsigned qp;
        auto slot = [&](auto I) __attribute__((always_inline)) {
            constexpr int i = decltype(I)::value % 64;
            constexpr bool fast = decltype(I)::value < 64;
            if constexpr (fast) {
                if constexpr (i == 0) vmw<6>(a[0]);
                if constexpr (i == 6) vmw<5>(a[9]);
                if constexpr (i == 7) vmw<4>(a[1]);
                if constexpr (i == 8) vmw<3>(a[2]);
            }
            if constexpr (i == 15) asm volatile("s_waitcnt vmcnt(0)\n\ts_barrier" ::: "memory");
            if constexpr (i == 15) { MARK3(0); }
            if constexpr (i == 36) { MARK3(5); }
            constexpr bool isl = i >= 6 && i <= 50 && (i & 3) == 2;
            constexpr int nl = i < 6 ? 0 : i > 50 ? 12 : (i - 6 + 3) / 4;
            if constexpr (isl) {
                smf_v(acc[nl % 6], a[9 - nl / 6], cat4(cat2(st[nl][0], st[nl][1]), cat2(st[nl][2], st[nl][3])), idx);
            } else {
                constexpr int r = i - nl;
                constexpr int kb = r < 7 ? r / 6 : r == 7 ? 2 : r < 13 ? 1 : r < 30 ? r / 6 : 5 + ((r - 30) % 6) / 2;
                constexpr int t = r < 7 ? r % 6 : r == 7 ? 0 : r < 13 ? r - 7 : r < 30 ? r % 6 : 2 * ((r - 30) / 6) + (r & 1);
                if constexpr (kb * 6 + t < 32) smf_a(acc[t], a[kb], w[t][kb], idx); else smf_v(acc[t], a[kb], w[t][kb], idx);
            }
            if constexpr (i == 0) { MARK2(2); }
            if constexpr (i == 12) bad = nan2(acc[0][0], acc[0][1]);
            if constexpr (i == 13) bad2 = nan2(acc[0][2], acc[0][3]);
            if constexpr (i == 15) a[3] = ms.get(3);
            if constexpr (i == 18) nf = ld64(rw, ms.vn, rb + amap(R_NF));
            if constexpr (i == 25) qp = ld32(rw, cq, 0);
            if constexpr (i == 20) a[8] = ms.get(8);
            if constexpr (i == 22) a[4] = ms.get(4);
            if constexpr (i == 28) a[5] = ms.get(5);
            if constexpr (i == 31) a[6] = ms.get(6);
            if constexpr (i == 34) a[7] = ms.get(7);
            if constexpr (i >= 7 && i < 43) st[(i - 7) / 4 + 3][(i - 7) & 3] = lwq((i - 7) / 4 + 3, (i - 7) & 3);
            if constexpr (i >= 43 && i < 51) kxf[i - 43] = lds128(kx + (i - 43) * 1024);
            if constexpr (i >= 55 && i < 59) kxf[i - 47] = lds128(kx + (i - 47) * 1024);
            if constexpr (i >= 49 && i < 52) { fin(0, i - 49); } if constexpr (i >= 52 && i < 55) { fin(1, i - 52); if constexpr (i < 54) fin(0, i - 49); }
            if constexpr (i >= 55 && i < 57) fin(1, i - 52);
            if constexpr (i >= 57) { fin(2, i - 57); fin(3, i - 57); }
        };
        prep();
        MARK(0); SETV(7, 0);
        const unsigned o0 = ms.so(rb, 0), o9 = ms.so(rb, 9), o1 = ms.so(rb, 1), o2 = ms.so(rb, 2);
        unsigned tg = ck.wait_until(tn);
        ms.lda(a[0], o0); ms.lda(a[9], o9); ms.lda(a[1], o1); ms.lda(a[2], o2);
        ms.share(o0, 0); ms.share(o1, 1); ms.share(o2, 2);
        if (tg - tn < 10u) tg = tn;
        SETV(1, tg + ck.org);
        sfor<0, 15>(slot);
        if (__builtin_expect((bad | bad2) != 0, 0)) {
            asm volatile("s_nop 7\n\ts_waitcnt vmcnt(0)" ::: "memory");
            *lfail = (unsigned)s; SETV(7, 1);
            for (unsigned c = 0;;) {
                a[0] = ms.ld(rb, 0); a[9] = ms.ld(rb, 9); a[1] = ms.ld(rb, 1); a[2] = ms.ld(rb, 2);
                if (MemStream::bad(a[0], a[9], a[1], a[2]) == 0 || sp.fail(c, 0x100 + s)) break;
            }
            ms.share(o0, 0); ms.share(o1, 1); ms.share(o2, 2);
            prep();
            sfor<64, 79>(slot);
            asm volatile("s_nop 7" : "+v"(acc[0]), "+v"(acc[1]), "+v"(acc[2]), "+v"(acc[3]), "+v"(acc[4]), "+v"(acc[5]));
        }
        MARK3(6);
        sfor<15, 60>(slot);
        fin(2, 3); fin(2, 4); fin(3, 3); fin(3, 4);
        asm volatile("s_nop 0");
        fin(4, 0); fin(4, 1); fin(5, 0); fin(5, 1); fin(4, 2); fin(5, 2); fin(4, 3); fin(4, 4); fin(5, 3); fin(5, 4);
        f32x4 am, a0, a1;
        MARK2(3);
        bar();
        MARK2(4);
        u32x4 qa[3], ka[3];
#pragma unroll
        for (int kb = 0; kb < 3; ++kb) { qa[kb] = af.get(kb * 1024); __builtin_amdgcn_sched_barrier(0); }
#pragma unroll
        for (int kb = 0; kb < 3; ++kb) { ka[kb] = af.get(3072 + kb * 1024); __builtin_amdgcn_sched_barrier(0); }
        asm volatile("" ::: "memory");
        if (wave == 0) {
            if (s < NSEG - 1) {
                a0 = f32x4{zero1(), zero1(), zero1(), zero1()}; a1 = f32x4{zero1(), zero1(), zero1(), zero1()};
                asm volatile("s_nop 0" : "+v"(a0), "+v"(a1));
#pragma unroll
                for (int kb = 0; kb < 3; ++kb) {
                    smf_v(a0, qa[kb], cat4(kxf[4 * kb], kxf[4 * kb + 2]), idx);
                    smf_v(a1, qa[kb], cat4(kxf[4 * kb + 1], kxf[4 * kb + 3]), idx);
                }
#pragma unroll
                for (int kb = 0; kb < 3; ++kb) mf_vv(am, qa[kb], ka[kb], kb == 0);
                asm volatile("s_nop 4" : "+v"(a0), "+v"(a1));
                float v[6];
                v[0] = a0[0] + a0[1]; v[3] = a0[2] + a0[3]; v[1] = a1[0] + a1[1]; v[4] = a1[2] + a1[3];
                asm volatile("" : "+v"(am), "+v"(v[0]), "+v"(v[1]), "+v"(v[3]), "+v"(v[4]));
                asm volatile("v_add_f32_dpp %0, %3, %2 quad_perm:[1,1,3,3] row_mask:0xf bank_mask:0xf\n\tv_add_f32_dpp %1, %5, %4 quad_perm:[1,1,3,3] row_mask:0xf bank_mask:0xf\n\ts_nop 0\n\t"
                             "v_mov_b32_dpp %0, %0 quad_perm:[0,0,2,2] row_mask:0xf bank_mask:0xf\n\tv_mov_b32_dpp %1, %1 quad_perm:[0,0,2,2] row_mask:0xf bank_mask:0xf"
                             : "=&v"(v[2]), "=&v"(v[5]) : "v"(am[0]), "v"(am[1]), "v"(am[2]), "v"(am[3]));
                float ma = fmax3(v[0], v[1], v[2]), mb = fmax3(v[3], v[4], v[5]);
                row_max2(ma, mb);
                ma *= SCALE_L2E; mb *= SCALE_L2E;
#pragma unroll
                for (int r = 0; r < 6; ++r) v[r] = __builtin_amdgcn_exp2f(__builtin_fmaf(v[r], SCALE_L2E, r < 3 ? -ma : -mb));
                float sa = __builtin_fmaf(v[2], 0.5f, v[0] + v[1]), sb = __builtin_fmaf(v[5], 0.5f, v[3] + v[4]);
                row_sum2(sa, sb);
                sa = __builtin_amdgcn_rcpf(sa); sb = __builtin_amdgcn_rcpf(sb);
                unsigned p0 = pka(v[0] * sa, v[3] * sb), p1 = pka(v[1] * sa, v[4] * sb), p2 = pka(v[2] * sa, v[5] * sb);
                asm volatile("s_nop 0" : "+v"(p0), "+v"(p1), "+v"(p2));
                st64(u32x2{pair_cols(p0, psel), pair_cols(p1, psel)}, rw, so1, rb);
                st32(__builtin_amdgcn_perm(p2, __builtin_amdgcn_update_dpp(0u, p2, 0x4E, 0xf, 0xf, true), psel2), rw, so2, rb);
                if (lane == 0) st32(dur, rw, h * 128, rb + amap(R_NS));
                tn = ck.now();
                lfail[1] = tn;
                asm volatile("s_waitcnt lgkmcnt(0)\n\ts_barrier" ::: "memory");
                if (s > 0 && __builtin_amdgcn_readfirstlane(*lfail) != s) dur = track(dur, tn - tg, s);
                SETV(5, tn + ck.org); SETV(6, tg + dur + ck.org);
                if (s + KARM < NSEG) { const unsigned ra = rec(par, s + KARM); st64(SENT2, rw, so1, ra); st32(sent1(), rw, so2, ra); if (lane == 0) st32(sent1(), rw, h * 128, ra + amap(R_NS)); }
            } else asm volatile("s_barrier" ::: "memory");
        } else if (wave == 1) {
            have = min((unsigned)__builtin_amdgcn_readlane(qp, 0), (unsigned)__builtin_amdgcn_readlane(qp, 1));
            if (s + 2 < NSEG) stage_k(s + 2);
            { const u32x2 pl = read_notes(nf); lfail[2] = pl.x; lfail[3] = pl.y; }
            asm volatile("s_waitcnt lgkmcnt(0)\n\ts_barrier" ::: "memory");
        }
        else {
            const int Tq = wave & 1;
            have = min((unsigned)__builtin_amdgcn_readlane(qp, 0), (unsigned)__builtin_amdgcn_readlane(qp, 1));
            u32x4 qx[6];
#pragma unroll
            for (int ep = 0; ep < 2; ++ep) qx[ep] = lds128(lds + L_QX + lane * 16 + Tq * 1024 + ep * 2048);
            zero_acc(am);
#pragma unroll
            for (int ep = 0; ep < 6; ++ep) {
                if (ep < 4) qx[ep + 2] = lds128(lds + L_QX + lane * 16 + Tq * 1024 + (ep + 2) * 2048);
                mf_vv(a0, kxf[2 * ep], qx[ep], ep == 0); mf_vv(a1, kxf[2 * ep + 1], qx[ep], ep == 0);
                if (ep & 1) smf_v(am, ka[ep >> 1], cat4(qx[ep - 1], qx[ep]), idx);
            }
            mf_done(am, a0, a1);
            float v[10];
            constexpr float SC = 0.07216878364870322f, L2E = 1.4426950408889634f;
#pragma unroll
            for (int r = 0; r < 4; ++r) {
                v[r] = (4 * g + r <= 16 * Tq + n) ? a0[r] * SC : -1e9f;
                v[4 + r] = (16 + 4 * g + r <= 16 * Tq + n) ? a1[r] * SC : -1e9f;
            }
            v[8] = (am[0] + am[1]) * SC; v[9] = (am[2] + am[3]) * SC;
            float mx = v[0];
#pragma unroll
            for (int r = 1; r < 10; ++r) mx = fmaxf(mx, v[r]);
            mx = x16(mx, true); mx = x32(mx, true);
            float sum = 0.f;
#pragma unroll
            for (int r = 0; r < 10; ++r) { v[r] = __builtin_amdgcn_exp2f((v[r] - mx) * L2E); sum += v[r]; }
            sum = x16(sum, false); sum = x32(sum, false);
            const float inv = __builtin_amdgcn_rcpf(sum);
            u32x4 ox = {pka(v[0] * inv, v[1] * inv), pka(v[2] * inv, v[3] * inv), pka(v[4] * inv, v[5] * inv), pka(v[6] * inv, v[7] * inv)};
            unsigned om = pka(v[8] * inv, v[9] * inv);
            asm volatile("s_barrier" : "+v"(ox), "+v"(om) :: "memory");
            if (Tq) st128(ox, rw, so1, rb); else st64(u32x2{ox.x, ox.y}, rw, so1, rb);
            st32(om, rw, so2, rb);
            if (s + 1 < NSEG) stage_q(s + 1);
            if (s + KARM < NSEG) { const unsigned ra = rec(par, s + KARM); if (Tq) st128(SENT4, rw, so1, ra); else st64(SENT2, rw, so1, ra); st32(sent1(), rw, so2, ra); }
        }
        tn = mem_time(__builtin_amdgcn_readfirstlane(lfail[2]), __builtin_amdgcn_readfirstlane(lfail[3]), __builtin_amdgcn_readfirstlane(lfail[1]));
        FLUSH(s);
    }
    vmwait<0>();
}

constexpr int L_WO = 81920;
template <class SV, class SO>
DEV void load_vo(const Params& P, int h, LAS char* lds, int wave, int lane, Spin& sp, SV&& sv, SO&& so_, int rot = 0) {
    auto krow = [&](int k) { int kk = k + 2 * rot; if (kk >= 20) kk -= 20; return (unsigned)kk * 32u * 12288u; };
    WSrc<3> wv; wv.init((const char*)P.wv + h * 384 + wave * 96, lane, 12288u);
    const unsigned lo = (unsigned)(uintptr_t)lds + L_WO + wave * 20480, lv = (unsigned)(uintptr_t)lds + wave * 20480;
#pragma unroll
    for (int k = 0; k < 6; ++k) wv.issue(krow(k), lv + k * 3072);
    WSrc<10> wo; wo.init((const char*)P.wo + (size_t)h * E_ * 1280 + wave * 320, lane, 1280u);
    wo.issue(0, lo); wo.issue(32 * 1280, lo + 10240);
    WRd<3, false> rv; rv.init(lds + wave * 20480, lane);
    WRd<10, false> ro; ro.init(lds + L_WO + wave * 20480, lane);
#pragma unroll
    for (int k = 0; k < 20; ++k) {
        if (k == 1) { MARK(3); } if (k == 6) { MARK(4); } if (k == 12) { MARK(5); }
        if (k == 6 || k == 12) {
            const int ko = k / 3 - 2;
            vmwait<18>();
#pragma unroll
            for (int j = 0; j < 2; ++j) {
                u32x4 fo[10];
#pragma unroll
                for (int t = 0; t < 10; ++t) fo[t] = ro.get(j * 10240, t);
#pragma unroll
                for (int t = 0; t < 10; ++t) so_(t, ko + j, fo[t]);
            }
            lgkm0();
            wo.issue((ko + 2) * 32 * 1280, lo); wo.issue((ko + 3) * 32 * 1280, lo + 10240);
        }
        vmwait_n(k < 15 ? 35 : k < 18 ? 20 + (19 - k) * 3 : (19 - k) * 3);
        u32x4 fr[3];
#pragma unroll
        for (int t = 0; t < 3; ++t) fr[t] = rv.get((k % 6) * 3072, t);
#pragma unroll
        for (int t = 0; t < 3; ++t) sv(t, k, fr[t]);
        if (k + 6 < 20) { lgkm0(); wv.issue(krow(k + 6), lv + (k % 6) * 3072); }
    }
    vmwait<0>();
    MARK(2); FLUSH(128);
    bar();
}

DEV void role_UV(const Params& P, unsigned char* ws, int h, LAS char* lds, int wave, int lane, Spin& sp) {
    const rsrc_t rw = mk_rsrc(ws, (unsigned)WS_END);
    const int rot = MemStream::rot_of(h + 5, wave);
    u32x8 wv[3][10], wo[10][2];
    load_vo(P, h, lds, wave, lane, sp,
        [&](int t, int k, u32x4 f) { if (k & 1) { wv[t][k >> 1] = cat4(lo4(wv[t][k >> 1]), f); pin_a(wv[t][k >> 1]); } else wv[t][k >> 1] = cat4(f, f); },
        [&](int t, int k, u32x4 f) { if (k & 1) { wo[t][k >> 1] = cat4(lo4(wo[t][k >> 1]), f); if (k == 1 && t < 2) pin_a(wo[t][k >> 1]); } else wo[t][k >> 1] = cat4(f, f); }, rot);
    const unsigned par = load_par(rw, lane);
    const int idx = (lane & 1) ? 0xEEEE : 0x4444;
    const int g = lane >> 4, n = lane & 15;
    MemStream ms; ms.init(rw, lds, lane, rot, wave == 0);
    volatile LAS unsigned* lfail = (volatile LAS unsigned*)(lds + 14336);
    if (wave == 0) *lfail = ~0u;
    bar();
    AFrag af; af.init(lds + 8192, lane);
    WRd<10, false> wr; wr.init(lds + L_WO + wave * 20480, lane);
    const unsigned uo = amap(R_UB) + (wave * 10 + g) * 16384 + h * 256 + n * 16;
    const unsigned uo2 = amap(R_UB) + (wave * 10 + 8 + (g & 1)) * 16384 + h * 256 + n * 16 + (g >> 1) * 8;
    LAS char* vw0 = opq(lds + 8192 + wave * 768 + AFrag::wofs(lane, wave & 1));
    LAS char* vw1 = opq(lds + 8192 + wave * 768 + AFrag::wofs(lane, ~wave & 1));
    auto lwq = [&](int t, int q) { return trd(wr.at((q >> 1) * 10240 + (q & 1) * 2560, t)); };
    Clock ck; ck.init(ws);
    unsigned dur = 140, tn = 0;
    for (int s = 0; s < NSEG; ++s) {
        const unsigned rb = rec(par, s);
        f32x4 acc[3], c[10];
        u32x2 st[10][4]; u32x4 a[10];
#pragma unroll
        for (int i = 0; i < 12; ++i) st[i >> 2][i & 3] = lwq(i >> 2, i & 3);
#pragma unroll
        for (int t = 0; t < 3; ++t) zero_acc(acc[t]);
#pragma unroll
        for (int t = 0; t < 4; ++t) zero_acc(c[t]);
        MARK(0); SETV(7, 0);
        const unsigned o0 = ms.so(rb, 0), o1 = ms.so(rb, 1), o2 = ms.so(rb, 2), o3 = ms.so(rb, 3);
        unsigned tg = ck.wait_until(tn);
        ms.lda(a[0], o0); ms.lda(a[1], o1); ms.lda(a[2], o2); ms.lda(a[3], o3);
        ms.share(o0, 0); ms.share(o1, 1); ms.share(o2, 2);
        if (tg - tn < 10u) tg = tn;
        SETV(1, tg + ck.org);
        unsigned long long bad = 0, bad2 = 0;
        auto slot = [&](auto I) __attribute__((always_inline)) {
            constexpr int i = decltype(I)::value % 64;
            constexpr bool fast = decltype(I)::value < 64;
            constexpr int kb = i < 4 ? i : i < 12 ? (i - 4) / 2 : i / 3, t = i < 4 ? 0 : i < 12 ? 1 + (i & 1) : i % 3;
            if constexpr (fast && i < 4) vmw<6 - i>(a[i]);
            if constexpr (i == 10) {
                asm volatile("s_waitcnt vmcnt(0)\n\ts_barrier" ::: "memory");
#pragma unroll
                for (int k = 4; k < 7; ++k) a[k] = ms.get(k);
            }
            smf_a(acc[t], a[kb], wv[t][kb], idx);
            if constexpr (i == 0) { MARK2(2); }
            if constexpr (i == 7) { asm volatile("s_nop 3"); bad = nan2(acc[0][0], acc[0][1]); bad2 = nan2(acc[0][2], acc[0][3]); }
            if constexpr (i >= 10 && i < 13) a[i - 3] = ms.get(i - 3);
            if constexpr (i >= 14 && i < 26) { constexpr int q = i - 14; c[4 + q / 4][q & 3] = zero1(); }
        };
        sfor<0, 10>(slot);
        if (__builtin_expect((bad | bad2) != 0, 0)) {
            asm volatile("s_nop 7\n\ts_waitcnt vmcnt(0)" ::: "memory");
            *lfail = (unsigned)s; SETV(7, 1);
            for (unsigned c = 0;;) {
#pragma unroll
                for (int k = 0; k < 4; ++k) a[k] = ms.ld(rb, k);
                if (MemStream::bad(a[0], a[1], a[2], a[3]) == 0 || sp.fail(c, 0x100 + s)) break;
            }
            ms.share(o0, 0); ms.share(o1, 1); ms.share(o2, 2);
#pragma unroll
            for (int t = 0; t < 3; ++t) zero_acc(acc[t]);
            sfor<64, 74>(slot);
            mf_done(acc[0], acc[1], acc[2]);
        }
        sfor<10, 30>(slot);
        asm volatile("s_nop 7" : "+v"(acc[0]), "+v"(acc[1]), "+v"(acc[2]), "+v"(c[4]), "+v"(c[5]), "+v"(c[6]));
#pragma unroll
        for (int t = 0; t < 3; ++t) { LAS char* p = t == 1 ? vw1 : vw0; float f0, f1; st2x16(p, p + 4, t * 256, fold_pk(acc[t], f0, f1)); }
#pragma unroll
        for (int t = 7; t < 10; ++t) { c[t] = f32x4{zero1(), zero1(), zero1(), zero1()}; pin_v(c[t]); }
        MARK2(3);
        bar();
        MARK2(4);
        u32x4 va[3];
#pragma unroll
        for (int kb = 0; kb < 3; ++kb) va[kb] = af.get(kb * 1024);
        unsigned o[10];
        sfor<0, 36>([&](auto I) __attribute__((always_inline)) {
            constexpr int i = decltype(I)::value, tp = i / 6, k = (i % 6) >> 1, t = 2 * tp + (i & 1);
            if constexpr (i < 30) {
                if constexpr (k == 0) { if constexpr (t < 2) smf_a(c[t], va[0], wo[t][0], idx); else smf_v(c[t], va[0], wo[t][0], idx); }
                else if constexpr (k == 1) smf_v(c[t], va[1], wo[t][1], idx);
                else smf_v(c[t], va[2], cat4(cat2(st[t][0], st[t][1]), cat2(st[t][2], st[t][3])), idx);
                constexpr int q0 = i < 3 ? 0 : ((i - 3) * 4) / 3, q1 = i < 3 ? 0 : ((i - 2) * 4) / 3;
                sfor<q0, q1>([&](auto Q) __attribute__((always_inline)) { constexpr int q = 12 + decltype(Q)::value; if constexpr (q < 40) st[q >> 2][q & 3] = lwq(q >> 2, q & 3); });
            }
            if constexpr (i == 30) asm volatile("s_nop 3" : "+v"(c[8]), "+v"(c[9]));
            if constexpr (i >= 10 && (i - 10) % 6 < 2) {
                constexpr int tf = 2 * ((i - 10) / 6) + (i - 10) % 6;
                float f0, f1;
                o[tf] = nosent(fold_pk(c[tf], f0, f1)); pin_v(o[tf]);
            }
            if constexpr (i == 19 || i == 31) {
                constexpr int q = i == 19 ? 0 : 4;
                auto r02 = __builtin_amdgcn_permlane32_swap(o[q], o[q + 2], false, false);
                auto r13 = __builtin_amdgcn_permlane32_swap(o[q + 1], o[q + 3], false, false);
                o[q] = r02[0]; o[q + 2] = r02[1]; o[q + 1] = r13[0]; o[q + 3] = r13[1];
                asm volatile("" : "+v"(o[q]), "+v"(o[q + 1]), "+v"(o[q + 2]), "+v"(o[q + 3]));
            }
            if constexpr (i == 20 || i == 32) {
                constexpr int q = i == 20 ? 0 : 4;
                auto r01 = __builtin_amdgcn_permlane16_swap(o[q], o[q + 1], false, false);
                auto r23 = __builtin_amdgcn_permlane16_swap(o[q + 2], o[q + 3], false, false);
                st128(u32x4{r01[0], r01[1], r23[0], r23[1]}, rw, uo, rb + q * 16384);
            }
        });
        { auto r = __builtin_amdgcn_permlane16_swap(o[8], o[9], false, false); st64(u32x2{r[0], r[1]}, rw, uo2, rb); }
        if (wave == 0 && lane == 0) st32(dur, rw, (32 + h) * 128, rb + amap(R_NS));
        tn = ck.now();
        if (s > 0 && __builtin_amdgcn_readfirstlane(*lfail) != s) dur = track(dur, tn - tg, s);
        SETV(5, tn + ck.org);
        if (wave == 0) { const u32x2 pl = read_notes(ld64(rw, ms.vn, rb + amap(R_NF))); lfail[1] = mem_time(pl.x, pl.y, tn); }
        bar();
        tn = __builtin_amdgcn_readfirstlane(lfail[1]);
        MARKC(6);
        if (s + KARM < NSEG) {
            const unsigned ra = rec(par, s + KARM);
            st128(SENT4, rw, uo, ra); st128(SENT4, rw, uo, ra + 65536); st64(SENT2, rw, uo2, ra);
            if (wave == 0 && lane == 0) st32(sent1(), rw, (32 + h) * 128, ra + amap(R_NS));
        }
        FLUSH(s);
    }
}

DEV void role_VC(const Params& P, unsigned char* ws, int h, int odd, LAS char* lds, int wave, int lane, Spin& sp) {
    constexpr int L_V = 40960;
    const rsrc_t rw = mk_rsrc(ws, (unsigned)WS_END);
    u32x4 wv[3][20], wo[10][4];
    load_vo(P, h, lds, wave, lane, sp, [&](int t, int k, u32x4 f) { wv[t][k] = f; pin_a(wv[t][k]); }, [&](int t, int k, u32x4 f) { wo[t][k] = f; if (t < 2 && k < 2) pin_a(wo[t][k]); });
    const int g = lane >> 4, n = lane & 15;
    const char* xf = (const char*)ws + O_XF;
    WRd<10, false> wr; wr.init(lds + L_WO + wave * 20480, lane);
    LAS char* l16 = opq(lds + lane * 16);
    auto issue_x = [&](int s) {
#pragma unroll
        for (int i = 0; i < 10; ++i) glds<0>(lane * 16, xf + (size_t)s * 40960 + (wave * 10 + i) * 1024, (unsigned)(uintptr_t)lds + (wave * 10 + i) * 1024);
    };
    issue_x(odd);
    const unsigned par = load_par(rw, lane);
    unsigned seen = 0;
    for (int s = odd; s < NSEG; s += 2) {
        MARK(0);
        vmwait<0>();
        bar();
        if (s >= 2 && wave == 0 && lane == 0) st32((unsigned)(s - 1), rw, (unsigned)(O_CTRL + 4 * (W_VCPROG + 32 * odd + h)), 0);
        MARK(1);
        f32x4 acc[3][2];
        u32x4 xb[40];
#pragma unroll
        for (int i = 0; i < 4; ++i) xb[i] = lds128(l16 + (i & 1) * 20480 + (i >> 1) * 1024);
#pragma unroll
        for (int i = 0; i < 40; ++i) {
#pragma unroll
            for (int t = 0; t < 3; ++t) {
                mf_av(acc[t][i & 1], wv[t][i >> 1], xb[i], i < 2);
                if (t == 0 && i + 4 < 40) xb[i + 4] = lds128(l16 + (i & 1) * 20480 + ((i + 4) >> 1) * 1024);
            }
        }
        mf_done(acc[0][0], acc[1][0], acc[2][0]); mf_done(acc[0][1], acc[1][1], acc[2][1]);
#pragma unroll
        for (int t = 0; t < 3; ++t)
#pragma unroll
            for (int T = 0; T < 2; ++T) {
                const int et = wave * 3 + t;
                const int off = (et >> 1) * 2048 + T * 1024 + ((2 * (et & 1) + (g >> 1)) * 16 + n) * 16 + (g & 1) * 8;
                *(LAS u32x2*)(lds + L_V + off) = u32x2{pk(acc[t][T][0], acc[t][T][1]), pk(acc[t][T][2], acc[t][T][3])};
            }
        MARK(2);
        wait_slot(rw, s, seen, h & 7, sp);
        bar();
        MARK(3);
        u32x4 va[6][2], st[10][2];
        f32x4 c[10][2];
#pragma unroll
        for (int k = 0; k < 6; ++k)
#pragma unroll
            for (int T = 0; T < 2; ++T) va[k][T] = lds128(l16 + L_V + k * 2048 + T * 1024);
        st[0][0] = wr.get(0, 0); st[0][1] = wr.get(10240, 0);
        const char* xn = xf + (size_t)(s + 2 < NSEG ? s + 2 : s) * 40960 + wave * 10240;
        const unsigned xd = (unsigned)(uintptr_t)lds + wave * 10240;
        auto fin = [&](int t) {
            st128(u32x4{pk(c[t][0][0], c[t][0][1]), pk(c[t][0][2], c[t][0][3]), pk(c[t][1][0], c[t][1][1]), pk(c[t][1][2], c[t][1][3])}, rw,
                  (h >> 3) * 16384 + (h & 7) * 1024 + lane * 16, page(par, s, 2 + (wave >> 1)) + ((wave & 1) * 10 + t) * 65536);
        };
#pragma unroll
        for (int t = 0; t < 10; ++t) {
#pragma unroll
            for (int k = 0; k < 6; ++k) {
                const u32x4 b = k < 4 ? wo[t][k & 3] : st[t][k & 1];
                if (t < 2 && k < 2) { mf_va(c[t][0], va[k][0], b, k == 0); mf_va(c[t][1], va[k][1], b, k == 0); }
                else { mf_vv(c[t][0], va[k][0], b, k == 0); mf_vv(c[t][1], va[k][1], b, k == 0); }
                if (k < 2 && t + 1 < 10) st[t + 1][k] = wr.get(k * 10240, t + 1);
                if (k == 5 && t < 5) { glds<0>(lane * 16, xn + 2 * t * 1024, xd + 2 * t * 1024); }
                if (k == 2 && t < 5) { glds<1024>(lane * 16, xn + 2 * t * 1024, xd + 2 * t * 1024); }
                if (k == 3 && t >= 1) { pin_v(c[t - 1][0]); pin_v(c[t - 1][1]); fin(t - 1); }
            }
        }
        mf_done(c[9][0], c[9][1]);
        fin(9);
        MARK(4); FLUSH(s);
    }
    vmwait<0>();
    bar();
    if (wave == 0 && lane == 0) st32((unsigned)NSEG, rw, (unsigned)(O_CTRL + 4 * (W_VCPROG + 32 * odd + h)), 0);
}

DEV void role_QK(const Params& P, unsigned char* ws, int cu, LAS char* lds, int wave, int lane, Spin& sp) {
    const rsrc_t rw = mk_rsrc(ws, (unsigned)WS_END);
    u32x4 w[4][20];
    const int sel = cu >= 24, c0 = (cu - 24 * sel) * 256 + wave * 64;
    stream_w<4, 20, 20, 10, 0, true>((const char*)(sel ? P.wk : P.wq) + c0 * 2, 12288u, lds, wave * 40960, lane,
                                 [&](int t, int k, u32x4 f) { w[t][k] = f; if (k < 16) pin_a(w[t][k]); });
    vmwait<0>();
    bar();
    const char* xf = (const char*)ws + O_XF;
    auto issue_x = [&](int s) {
#pragma unroll
        for (int i = 0; i < 10; ++i) glds<0>(lane * 16, xf + (size_t)s * 40960 + (wave * 10 + i) * 1024, (unsigned)(uintptr_t)lds + (s & 1) * 40960 + (wave * 10 + i) * 1024);
    };
    issue_x(0);
    const unsigned ol = (sel ? R_KX : R_QX) + (c0 >> 5) * 2048, ob = ((ol >> 13) << 14) | (ol & 8191u);
    const unsigned par = load_par(rw, lane);
    unsigned seen = 0;
    for (int s = 0; s < NSEG; ++s) {
        MARK(0);
        vmwait<0>();
        bar();
        MARK(1);
        if (s >= 1 && wave == 0 && lane == 0) st32((unsigned)s, rw, (unsigned)(O_CTRL + 4 * (W_QKPROG + 32 * cu)), 0);
        wait_slot(rw, s, seen, cu & 7, sp);
        LAS char* l16 = opq(lds + (s & 1) * 40960 + lane * 16);
        f32x4 acc[4][2];
        u32x4 xb[40];
#pragma unroll
        for (int i = 0; i < 4; ++i) xb[i] = lds128(l16 + (i & 1) * 20480 + (i >> 1) * 1024);
        const char* xn = xf + (size_t)(s + 1 < NSEG ? s + 1 : s) * 40960 + wave * 10240;
        const unsigned xd = (unsigned)(uintptr_t)lds + (~s & 1) * 40960 + wave * 10240;
#pragma unroll
        for (int i = 0; i < 40; ++i)
#pragma unroll
            for (int t = 0; t < 4; ++t) {
                if (t == 2 && i % 3 == 0 && i < 30) glds<0>(lane * 16, xn + (i / 3) * 1024, xd + (i / 3) * 1024);
                if (i < 32) mf_av(acc[t][i & 1], w[t][i >> 1], xb[i], i < 2); else mf_vv(acc[t][i & 1], w[t][i >> 1], xb[i], false);
                if (t == 0 && i + 4 < 40) xb[i + 4] = lds128(l16 + (i & 1) * 20480 + ((i + 4) >> 1) * 1024);
            }
        mf_done(acc[0][0], acc[1][0], acc[2][0], acc[3][0]); mf_done(acc[0][1], acc[1][1], acc[2][1], acc[3][1]);
#pragma unroll
        for (int pr = 0; pr < 2; ++pr)
#pragma unroll
            for (int T = 0; T < 2; ++T) {
                const f32x4 a = acc[2 * pr][T], b = acc[2 * pr + 1][T];
                st128(u32x4{pk(a[0], a[1]), pk(a[2], a[3]), pk(b[0], b[1]), pk(b[2], b[3])}, rw, pr * 2048 + T * 1024 + lane * 16, page(par, s, 1) + ob);
            }
        MARK(2); MARKC(6); FLUSH(s);
    }
    vmwait<0>();
    bar();
    if (wave == 0 && lane == 0) st32((unsigned)NSEG, rw, (unsigned)(O_CTRL + 4 * (W_QKPROG + 32 * cu)), 0);
}

DEV void role_F(const Params& P, unsigned char* ws, int f, LAS char* lds, int wave, int lane, Spin& sp) {
    constexpr int L_FL = 131072, L_U = L_FL + 1024;
    const rsrc_t rw = mk_rsrc(ws, (unsigned)WS_END);
    volatile LAS unsigned* fl = (volatile LAS unsigned*)(lds + L_FL);
    if (wave == 0 && lane < 8) fl[lane] = 0u;
    bar();
    const unsigned par = load_par(rw, lane);
    const int g = lane >> 4, n = lane & 15;
    const unsigned l16 = lane * 16;
    const unsigned xr = ((f % 10) * 3) & 15;
    auto lwait = [&](int i, int need, unsigned code) {
        for (unsigned c = 0, k = 0; (int)__builtin_amdgcn_readfirstlane(fl[i]) < need;) { if ((++k & 63u) == 0u && sp.fail(c, code)) break; }
        asm volatile("" ::: "memory");
    };
    auto lset = [&](int i, int v) { asm volatile("s_waitcnt lgkmcnt(0)" ::: "memory"); if (lane == 0) fl[i] = (unsigned)v; };
    if (wave == 3) {
        unsigned have0 = 0, have1 = 0;
        const unsigned vf = min(lane, 39) * 128;
        for (int s = 0; s < NSEG; ++s) {
            MARK(0);
            unsigned hv = (s & 1) ? have1 : have0;
            for (unsigned c = 0; hv <= (unsigned)s;) {
                const unsigned v = ld32(rw, (lane & 31) * 4, (unsigned)(O_CTRL + 4 * (W_VCPROG + 32 * (s & 1))));
                if (!any_lane(v <= (unsigned)s)) {
                    hv = ~wave_max(~v);
                    break;
                }
                if (sp.fail(c, 0x300 + s)) break;
                __builtin_amdgcn_s_sleep(2);
            }
            if (s & 1) have1 = hv; else have0 = hv;
            lwait(2, s - 3, 0x341); lwait(3, s - 3, 0x342); lwait(1, s - 1, 0x340);
            MARK(1);
            const unsigned char* src = ws + (page(par, s, f < 20 ? 2 : 3) + (f % 20) * 65536);
            const unsigned no = rec(par, s - 1) + amap(R_NF);
            u32x2 nf;
            __builtin_amdgcn_s_sleep(4);
            asm volatile("buffer_load_dwordx2 %0, %1, %2, %3 offen sc1" : "=v"(nf) : "v"(vf), "s"(rw), "s"(no));
#pragma unroll
            for (int i = 0; i < 16; ++i) glds_dev<2, 1024>(l16, src + (((i ^ xr) >> 2) * 16384 + ((i ^ xr) & 3) * 2048), (unsigned)(uintptr_t)lds + (s & 3) * 32768 + i * 2048);
            asm volatile("s_waitcnt vmcnt(32)" : "+v"(nf));
            if (s >= 2) {
                for (unsigned c = 0; any_lane(max(nf.x, nf.y) == SENT);) { if (sp.fail(c, 0x390)) break; nf = ld64(rw, vf, no); }
                const u32x2 pl = read_notes(nf);
                if (lane == 0) { fl[6] = pl.x; fl[7] = pl.y; }
                lset(5, s - 1);
            }
            vmwait<0>();
            lset(0, s + 1);
            MARK(2); FLUSH(s);
        }
    } else if (wave == 0) {
        const int idx = (lane & 1) ? 0xEEEE : 0x4444;
        const unsigned pmo = g * 256 + n * 8;
        const unsigned mo = amap(R_MEM + f * 256) + (2 * (2 * g + (n & 1)) + ((n >> 1) & 1)) * 16 + (n >> 2) * 4;
        const unsigned psel = (n & 1) ? 0x07060302u : 0x01000504u;
        unsigned pv[4], ps[4];
#pragma unroll
        for (int j = 0; j < 4; ++j) { pv[j] = l16 + ((j ^ xr) & 3) * 1024; const unsigned k = j ^ (xr >> 2); ps[j] = amap(R_P) + (k & 1) * 4096 + (k >> 1) * 16384; }
        Clock ck; ck.init(ws);
        unsigned dur = 75, tp = 0, plan = 0, dS = 150, dU = 140;
        unsigned boost = 0;
        auto use_notes = [&](int s) __attribute__((always_inline)) {
            if (__builtin_amdgcn_readfirstlane(fl[5]) == s && s > 0) { tp = __builtin_amdgcn_readfirstlane(fl[6]); plan = __builtin_amdgcn_readfirstlane(fl[7]); }
        };
        __builtin_amdgcn_s_setprio(3);
        for (int s = 0; s < NSEG - 1; ++s) {
            MARK(0);
            lwait(0, s + 1, 0x343);
            LAS char* cb = opq(lds + (s & 3) * 32768 + l16);
            u32x8 bc[16];
#pragma unroll
            for (int t = 0; t < 16; ++t) bc[t] = cat4(lds128(cb + 2 * t * 1024), lds128(cb + (2 * t + 1) * 1024));
#pragma unroll
            for (int t = 0; t < 16; ++t) pin_a(bc[t]);
            const unsigned rb = rec(par, s), rn = rec(par, s + 1), ub = rb + amap(R_UB) + f * 16384, pb = rb + amap(R_P);
            ck.wait_until(tp - (max(dS, dU + 8u) - dU) - 12u);
            use_notes(s);
            const unsigned tu = tp - (max(dS, dU + 8u) - dU);
            unsigned ns;
            float r0, r1;
            u32x4 u[8], px[16]; u32x2 pm[8];
            f32x4 d; zero_acc(d);
            MARK(1);
            ck.wait_until(tu);
#pragma unroll
            for (int i = 0; i < 8; ++i) u[i] = ld128(rw, l16 + (i & 3) * 1024, ub + (i >> 2) * 4096);
            const unsigned tg = ck.wait_until(tp);
            SETV(3, tg + ck.org); SETV(4, tp + ck.org); SETV(5, plan - DELTA_B + ck.org); SETV(6, tu + ck.org);
            bool failed = false;
            for (unsigned c = 0;;) {
#pragma unroll
                for (int i = 0; i < 16; ++i) px[i] = ld128(rw, pv[i & 3], rb + ps[i >> 2]);
#pragma unroll
                for (int i = 0; i < 8; ++i) pm[i] = ld64(rw, pmo + (i >> 1) * 1024 + (i & 1) * 128, pb + 32768);
                ns = ld32(rw, lane * 128, rb + amap(R_NS));
#pragma unroll
                for (int i = 0; i < 16; ++i) smf_a(d, px[i], bc[i], idx);
#pragma unroll
                for (int i = 0; i < 4; ++i) smf_v(d, cat2(pm[2 * i], pm[2 * i + 1]), cat4(u[2 * i], u[2 * i + 1]), idx);
                mf_done(d);
                r0 = d[0] + d[1]; r1 = d[2] + d[3];
                if (__builtin_expect(!any_lane(__builtin_isunordered(r0, r1)), 1)) { SETV(7, c); break; }
                if (c > 0) {
                    unsigned sg = 0; bool miss = false;
#pragma unroll
                    for (int i = 0; i < 8; ++i) miss |= mx4(u[i]) == SENT;
#pragma unroll
                    for (int i = 0; i < 16; ++i) sg |= px[i].x | px[i].y | px[i].z | px[i].w;
#pragma unroll
                    for (int i = 0; i < 8; ++i) sg |= pm[i].x | pm[i].y;
                    if (!any_lane(miss | ((sg & 0x80008000u) != 0u))) break;
                }
                if (sp.fail(c, 0x400 + s)) break;
                failed = true;
                zero_acc(d);
#pragma unroll
                for (int i = 0; i < 8; ++i) u[i] = ld128(rw, l16 + (i & 3) * 1024, ub + (i >> 2) * 4096);
            }
            st32(nosent(pair_cols(pk(r0, r1), psel)), rw, mo, rn);
            const unsigned n5 = fl[5], n7 = fl[7];
            const unsigned tpub = ck.now();
            if (__builtin_amdgcn_readfirstlane(n5) == s) plan = __builtin_amdgcn_readfirstlane(n7);
            const unsigned prop = (tpub + VIS <= plan ? plan : tpub + LATE) + max(dS, dU + 8u) + DELTA_A + (failed ? boost : 0u);
            boost = failed ? min(boost + 40u, 400u) : 0u;
            if (lane == 0) { st64(u32x2{prop, dur}, rw, f * 128, rn + amap(R_NF)); fl[1] = (unsigned)(s + 1); }
            if (!failed && tg - tp < 15u) dur = track(dur, tpub - tp, s);
            SETV(2, tpub + ck.org);
            if (!any_lane(ns == SENT)) { const unsigned m = half_max(min(ns, 400u)); dS = __builtin_amdgcn_readlane(m, 0); dU = __builtin_amdgcn_readlane(m, 32); }
            lwait(2, s - 1, 0x346); lwait(3, s - 1, 0x347);
#pragma unroll
            for (int i = 0; i < 8; ++i) *(LAS u32x4*)(lds + L_U + (s & 1) * 8192 + i * 1024 + l16) = u[i];
            lset(4, s + 1);
            tp = prop; plan = prop + dur + DELTA_B;
            if (f == 0 && lane < 8) st32((unsigned)(s + 1), rw, lane * 128, (unsigned)(O_CTRL + 4 * W_PROG));
            if (s + KARM < NSEG) { const unsigned ra = rec(par, s + KARM); st32(sent1(), rw, mo, ra); if (lane == 0) st64(SENT2, rw, f * 128, ra + amap(R_NF)); }
            FLUSH(s);
        }
        {
            const unsigned ub = rec(par, NSEG - 1) + amap(R_UB) + f * 16384;
            u32x4 u[8];
            ck.wait_until(tp);
            lset(1, NSEG);
            for (unsigned c = 0;;) {
                unsigned m = 0;
#pragma unroll
                for (int i = 0; i < 8; ++i) u[i] = ld128(rw, l16 + (i & 3) * 1024, ub + (i >> 2) * 4096);
#pragma unroll
                for (int i = 0; i < 8; ++i) m = max(m, mx4(u[i]));
                if (!any_lane(m == SENT) || sp.fail(c, 0x4FF)) break;
            }
            lwait(2, NSEG - 2, 0x346); lwait(3, NSEG - 2, 0x347);
#pragma unroll
            for (int i = 0; i < 8; ++i) *(LAS u32x4*)(lds + L_U + ((NSEG - 1) & 1) * 8192 + i * 1024 + l16) = u[i];
            lset(4, NSEG);
        }
    } else {
        auto xrows = [&](auto TQ) __attribute__((always_inline)) {
            constexpr int Tq = decltype(TQ)::value, NX = Tq ? 32 : 16;
            const rsrc_t rx = mk_rsrc(P.x, S_ * D_ * 2);
            const unsigned ho = (f >> 1) * 1024 + ((2 * (f & 1) + (g >> 1)) * 16 + n) * 16 + (g & 1) * 8;
            for (int s = 0; s < NSEG; ++s) {
                MARK(0);
                lwait(0, s + 1, 0x345);
                LAS char* cb = opq(lds + (s & 3) * 32768 + l16);
                u32x4 ca[NX];
#pragma unroll
                for (int i = 0; i < NX; ++i) ca[i] = Tq ? lds128(cb + i * 1024) : cat2(*(LAS const u32x2*)(cb + i * 2048), *(LAS const u32x2*)(cb + i * 2048 + 1024));
#pragma unroll
                for (int i = 0; i < NX; ++i) pin_a(ca[i]);
                const unsigned pb = rec(par, s) + amap(R_PX) + Tq * amap(PX_B);
                lwait(1, s + 1, 0x344);
                MARK2(1);
                f32x4 d; u32x2 xv;
                for (unsigned c = 0;;) {
                    u32x4 u[8], px[NX + 8];
#pragma unroll
                    for (int i = 0; i < NX; ++i) {
                        if (Tq) { const unsigned k = (i >> 1) ^ xr; px[i] = ld128(rw, l16 + (i & 1) * 1024, pb + (k & 3) * 2048 + (k >> 2) * 16384); }
                        else { const unsigned k = i ^ xr; px[i] = ld128(rw, l16, pb + (k & 7) * 1024 + (k >> 3) * 16384); }
                    }
#pragma unroll
                    for (int i = 0; i < 8; ++i) px[NX + i] = ld128(rw, l16 + (i & 3) * 1024, pb + 65536 + (i >> 2) * 4096);
                    xv = __builtin_amdgcn_raw_buffer_load_b64(rx, ((32 * s + 16 * Tq + n) * D_ + 16 * f + 4 * g) * 2, 0, 0);
                    lwait(4, s + 1, 0x348);
#pragma unroll
                    for (int i = 0; i < 8; ++i) u[i] = lds128(lds + L_U + (s & 1) * 8192 + i * 1024 + l16);
#pragma unroll
                    for (int i = 0; i < NX; ++i) mf_av(d, ca[i], px[i], i == 0);
#pragma unroll
                    for (int i = 0; i < 8; ++i) mf_vv(d, u[i], px[NX + i], false);
                    mf_done(d);
                    if (!any_lane(__builtin_isunordered(d[0] + d[1], d[2] + d[3]))) { SETV(7, c); break; }
                    bool miss = false;
#pragma unroll
                    for (int i = 0; i < NX + 8; ++i) miss |= mx4(px[i]) == SENT;
                    if (!any_lane(miss)) break;
                    if (sp.fail(c, 0x500 + s)) break;
                }
                MARK(2);
                lset(2 + Tq, s + 1);
                const unsigned a01 = pk(d[0], d[1]), a23 = pk(d[2], d[3]);
                st64(u32x2{pks(bflo(xv.x) + bflo(a01), bfhi(xv.x) + bfhi(a01)), pks(bflo(xv.y) + bflo(a23), bfhi(xv.y) + bfhi(a23))}, rw, ho, (unsigned)(O_HF + (size_t)(2 * s + Tq) * XF_B));
                if (s + KARM < NSEG) st64(SENT2, rw, ho, (unsigned)(O_HF + (size_t)(2 * (s + KARM) + Tq) * XF_B));
                MARK(3); FLUSH(s);
            }
        };
        if (wave == 1) xrows(IC<0>{}); else xrows(IC<1>{});
    }
}

DEV float gelu_t(float x) {
    const float t = x * __builtin_fmaf(x * x, -0.10294324f, -2.30220820f);
    return x * __builtin_amdgcn_rcpf(1.f + __builtin_amdgcn_exp2f(t));
}

template <int NK> DEV bool any_sent(const u32x4 (&x)[NK]) {
    unsigned m = 0;
#pragma unroll
    for (int k = 0; k < NK; ++k) m = max(m, mx4(x[k]));
    return any_lane(m == SENT);
}
template <int MODE>
DEV void role_W(const Params& P, unsigned char* ws, int cb, LAS char* lds, int wave, int lane, Spin& sp) {
    const rsrc_t rw = mk_rsrc(ws, (unsigned)WS_END);
    constexpr unsigned stride = MODE == 3 ? F_ * 2 : D_ * 2;
    const char* wsrc = (const char*)(MODE == 1 ? P.w1 : MODE == 2 ? P.w2 : P.w3) + (size_t)wave * 160 * stride + cb * 256;
    u32x4 w[8][5];
    stream_w<8, 5, 5, 5, 0, true>(wsrc, stride, lds, wave * 40960, lane, [&](int t, int k, u32x4 f) { w[t][k] = f; pin_a(w[t][k]); }, 0, 2 * wave + 2);
    vmwait<0>();
    bar();
    const int g = lane >> 4, n = lane & 15;
    const unsigned l16 = lane * 16;
    constexpr size_t O_IN = MODE == 1 ? O_HF : MODE == 2 ? O_GF : O_X1F;
    LAS char* wr[3];
#pragma unroll
    for (int j = 0; j < 3; ++j) wr[j] = opq(lds + ((wave + 1 + j) & 3) * 6144 + j * 2048 + l16);
    LAS char* rd = opq(lds + wave * 6144 + l16);
    LAS char* sw = opq(lds + 24576 + wave * 512 + lane * 8);
    LAS char* sr = opq(lds + 24576 + ((n >> 2) * 16 + n) * 8);
    u32x4 ones = {0x3F803F80u, 0x3F803F80u, 0x3F803F80u, 0x3F803F80u}; pin_v(ones);
    auto fetch1 = [&](int u, int k) __attribute__((always_inline)) { return ld128(rw, l16, (unsigned)(O_IN + (size_t)u * XF_B) + (wave * 5 + k) * 1024); };
    auto probe = [&](int u) __attribute__((always_inline)) { return ld32(rw, 1008, (unsigned)(O_IN + (size_t)u * XF_B) + (wave * 5 + 4) * 1024); };
    auto product = [&](const u32x4 (&xb)[5], auto&& side, int buf, f32x4& o0, f32x4& o1, float& S1, float& S2) __attribute__((always_inline)) {
        f32x4 acc[8], s1, s2;
        if (MODE != 2) {
#pragma unroll
            for (int k = 0; k < 5; ++k) { mf_vv(s1, ones, xb[k], k == 0); mf_vv(s2, xb[k], xb[k], k == 0); }
        }
        sfor<0, 40>([&](auto I) __attribute__((always_inline)) {
            constexpr int i = decltype(I)::value, t = 2 * (i / 10) + (i & 1), k = (i % 10) >> 1;
            mf_av(acc[t], w[t][k], xb[k], k == 0);
            if constexpr (i % 4 == 0) side(IC<i / 4>{});
            if constexpr (i >= 17 && (i % 10 == 7 || i % 10 == 8)) { constexpr int tw = 2 * (i / 10 - 1) + (i % 10 - 7); pin_v(acc[tw]); *(LAS f32x4*)(wr[tw >> 1] + buf * 28672 + (tw & 1) * 1024) = acc[tw]; }
            if constexpr (MODE != 2 && i == 20) {
                asm volatile("" : "+v"(s1), "+v"(s2));
                const float d = (n & 2) ? ((n & 1) ? s2[3] : s2[2]) : ((n & 1) ? s2[1] : s2[0]);
                *(LAS f32x2*)(sw + buf * 28672) = f32x2{s1[0], d};
            }
        });
        bar();
        f32x4 p[6]; f32x2 q[4];
#pragma unroll
        for (int j = 0; j < 6; ++j) p[j] = *(LAS const f32x4*)(rd + buf * 28672 + j * 1024);
        if (MODE != 2) {
#pragma unroll
            for (int j = 0; j < 4; ++j) q[j] = *(LAS const f32x2*)(sr + buf * 28672 + j * 512);
        }
        mf_done(acc[6], acc[7]);
        o0 = (acc[6] + p[0]) + (p[2] + p[4]); o1 = (acc[7] + p[1]) + (p[3] + p[5]);
        if (MODE != 2) { S1 = (q[0].x + q[1].x) + (q[2].x + q[3].x); S2 = (q[0].y + q[1].y) + (q[2].y + q[3].y); }
    };
    f32x4 cs0 = {}, cs1 = {};
    if (MODE != 2) {
        u32x4 o4[5] = {ones, ones, ones, ones, ones};
        asm volatile("s_nop 1" : "+v"(o4[0]), "+v"(o4[1]), "+v"(o4[2]), "+v"(o4[3]), "+v"(o4[4]));
        float a, b; product(o4, [](auto) {}, 1, cs0, cs1, a, b);
    }
    const int kso = 4 * cb + wave;
    const int grp = kso / 5, sh = grp == 0 ? 1 : grp == 1 ? -1 : grp == 2 ? 2 : -2;
    if (MODE == 1) {
        const int td = sh > 0 ? n : S_ - 1 - n;
        if (n < (sh > 0 ? sh : -sh)) st128(u32x4{0u, 0u, 0u, 0u}, rw, (td >> 4) * (unsigned)XF_B + kso * 1024 + (g * 16 + (td & 15)) * 16, (unsigned)O_GF);
        vmwait<0>();
    }
    u32x4 xa[5], xc[5], hn; u32x2 sn;
    bool miss = true, ahead = false;
    auto extra = [&](int u) __attribute__((always_inline)) { if (MODE == 2) { hn = ld128(rw, l16, (unsigned)(O_HF + (size_t)u * XF_B) + kso * 1024); sn = ld64(rw, (16 * u + n) * 8, (unsigned)O_ST); } };
    auto unit = [&](int u, u32x4 (&xb)[5], u32x4 (&nx)[5]) __attribute__((always_inline)) {
        MARK(0);
        const int tok = 16 * u + n, un = u + 1 < NUNIT ? u + 1 : u, u2 = u + 2 < NUNIT ? u + 2 : u;
        for (unsigned c = 0; miss;) {
            while (__builtin_amdgcn_readfirstlane(probe(u)) == SENT) { if (sp.fail(c, 0x600 + MODE * 0x100 + u)) break; __builtin_amdgcn_s_sleep(PSLEEP); }
            __builtin_amdgcn_s_sleep(PWAIT);
#pragma unroll
            for (int k = 0; k < 5; ++k) xb[k] = fetch1(u, k);
            extra(u);
            miss = any_sent(xb);
            if (miss && sp.fail(c, 0x680 + u)) break;
        }
        MARK(1);
        asm volatile("s_nop 1" : "+v"(xb[0]), "+v"(xb[1]), "+v"(xb[2]), "+v"(xb[3]), "+v"(xb[4]));
        const bool early = ahead;
        unsigned pr = probe(early ? u2 : un);
        u32x4 hv = hn; u32x2 stt = sn;
        if (MODE == 2) asm volatile("" : "+v"(hv), "+v"(stt));
        f32x4 o0, o1; float S1, S2;
        product(xb, [&](auto J) __attribute__((always_inline)) {
            constexpr int j = decltype(J)::value;
            if constexpr (j < 5) { if (early) nx[j] = fetch1(un, j); }
            if constexpr (j == 5) { if (early) extra(un); }
        }, u & 1, o0, o1, S1, S2);
        if (!early) {
            ahead = __builtin_amdgcn_readfirstlane(pr) != SENT;
            if (ahead) {
#pragma unroll
                for (int k = 0; k < 5; ++k) nx[k] = fetch1(un, k);
                extra(un);
                pr = probe(u2);
            }
        }
        auto settle = [&]() __attribute__((always_inline)) { __builtin_amdgcn_s_waitcnt(0x0F70); miss = !ahead || any_sent(nx); ahead = ahead && __builtin_amdgcn_readfirstlane(pr) != SENT; if (MODE == 2) asm volatile("" : "+v"(hn), "+v"(sn)); };
        MARK(2);
        if (MODE != 2) {
            const float mu = S1 * (1.f / D_), var = fmaxf(S2 * (1.f / D_) - mu * mu, 0.f);
            float al = __builtin_amdgcn_rsqf(var + 1e-5f);
            if (MODE == 1) {
                if (cb == 0 && wave == 0 && g == 0) st64(u32x2{nosent(f2u(mu)), nosent(f2u(al))}, rw, tok * 8, (unsigned)O_ST);
                al *= __builtin_amdgcn_rsqf(var * al * al + 1e-5f);
            }
            float y[8];
#pragma unroll
            for (int r = 0; r < 4; ++r) { y[r] = gelu_t(al * (o0[r] - mu * cs0[r])); y[4 + r] = gelu_t(al * (o1[r] - mu * cs1[r])); }
            u32x4 o = {pks(y[0], y[1]), pks(y[2], y[3]), pks(y[4], y[5]), pks(y[6], y[7])};
            pin_v(o);
            settle();
            if (MODE == 1) {
                const int td = tok + sh;
                const unsigned vo = (td >> 4) * (int)XF_B + kso * 1024 + (g * 16 + (td & 15)) * 16;
                if (td >= 0 && td < S_) st128(o, rw, vo, (unsigned)O_GF);
                if (u + 2 * KARM < NUNIT && td + 32 * KARM < S_) st128(SENT4, rw, vo + 2 * KARM * (unsigned)XF_B, (unsigned)O_GF);
            } else {
                st128(o, rw, l16, (unsigned)(O_G3F + (size_t)u * G3_B) + kso * 1024);
                if (u + 2 * KARM < NUNIT) st128(SENT4, rw, l16, (unsigned)(O_G3F + (size_t)(u + 2 * KARM) * G3_B) + kso * 1024);
            }
        } else {
            for (unsigned c = 0; any_lane(max(mx4(hv), max(stt.x, stt.y)) == SENT);) {
                if (sp.fail(c, 0xA00 + u)) break;
                hv = ld128(rw, l16, (unsigned)(O_HF + (size_t)u * XF_B) + kso * 1024); stt = ld64(rw, tok * 8, (unsigned)O_ST);
            }
            const float mu = u2f(stt.x), al = u2f(stt.y);
            const u32x4 o = {pks((bflo(hv.x) - mu) * al + o0[0], (bfhi(hv.x) - mu) * al + o0[1]), pks((bflo(hv.y) - mu) * al + o0[2], (bfhi(hv.y) - mu) * al + o0[3]),
                             pks((bflo(hv.z) - mu) * al + o1[0], (bfhi(hv.z) - mu) * al + o1[1]), pks((bflo(hv.w) - mu) * al + o1[2], (bfhi(hv.w) - mu) * al + o1[3])};
            settle();
            st128(o, rw, l16, (unsigned)(O_X1F + (size_t)u * XF_B) + kso * 1024);
            if (u + 2 * KARM < NUNIT) st128(SENT4, rw, l16, (unsigned)(O_X1F + (size_t)(u + 2 * KARM) * XF_B) + kso * 1024);
        }
        MARK(3); FLUSH(u);
    };
    for (int u = 0; u < NUNIT; u += 2) { unit(u, xa, xc); unit(u + 1, xc, xa); }
}

DEV void role_W4(const Params& P, unsigned char* ws, int cb, LAS char* lds, int wave, int lane, Spin& sp) {
    const rsrc_t rw = mk_rsrc(ws, (unsigned)WS_END);
    const rsrc_t ro = mk_rsrc(P.out, S_ * D_ * 2);
    u32x4 w[4][12];
    stream_w<4, 12, 12, 10, 0, false>((const char*)P.w4 + (size_t)wave * 384 * 1280 + cb * 128, 1280u, lds, wave * 40960, lane,
                                  [&](int t, int k, u32x4 f) { w[t][k] = f; pin_a(w[t][k]); }, 0, wave + 1);
    vmwait<0>();
    bar();
    const int g = lane >> 4, n = lane & 15;
    const unsigned l16 = lane * 16;
    const int ct = 4 * cb + wave;
    const unsigned po = (ct >> 1) * 1024 + ((2 * (ct & 1) + (g >> 1)) * 16 + n) * 16 + (g & 1) * 8;
    LAS char* wr[3];
#pragma unroll
    for (int j = 0; j < 3; ++j) wr[j] = opq(lds + ((wave + 1 + j) & 3) * 3072 + j * 1024 + l16);
    LAS char* rd = opq(lds + wave * 3072 + l16);
    auto fetch = [&](u32x4 (&x)[12], int u) __attribute__((always_inline)) {
#pragma unroll
        for (int k = 0; k < 12; ++k) x[k] = ld128(rw, l16 + (k & 3) * 1024, (unsigned)(O_G3F + (size_t)u * G3_B) + (wave * 12 + (k & ~3)) * 1024);
    };
    auto probe = [&](int u) __attribute__((always_inline)) { return ld32(rw, 1008, (unsigned)(O_G3F + (size_t)u * G3_B) + (wave * 12 + 11) * 1024); };
    u32x4 xa[12], xc[12]; u32x2 hn, xn;
    bool miss = true, ahead = false;
    auto extra = [&](int u) __attribute__((always_inline)) { hn = ld64(rw, po, (unsigned)(O_HF + (size_t)u * XF_B)); xn = ld64(rw, po, (unsigned)(O_X1F + (size_t)u * XF_B)); };
    auto unit = [&](int u, u32x4 (&xb)[12], u32x4 (&nx)[12]) __attribute__((always_inline)) {
        MARK(0);
        for (unsigned c = 0; miss;) {
            while (__builtin_amdgcn_readfirstlane(probe(u)) == SENT) { if (sp.fail(c, 0xB00 + u)) break; __builtin_amdgcn_s_sleep(PSLEEP); }
            __builtin_amdgcn_s_sleep(PWAIT);
            fetch(xb, u); extra(u);
            miss = any_sent(xb);
            if (miss && sp.fail(c, 0xB80 + u)) break;
        }
        MARK(1);
#pragma unroll
        for (int k = 0; k < 12; ++k) pin_v(xb[k]);
        asm volatile("s_nop 1");
        u32x2 hv = hn, xv = xn;
        const int un = min(u + 1, NUNIT - 1), u2 = min(u + 2, NUNIT - 1);
        unsigned pr = SENT;
        const bool early = ahead;
        pr = probe(early ? u2 : un);
        asm volatile("" : "+v"(hv), "+v"(xv));
        f32x4 acc[4];
        sfor<0, 48>([&](auto I) __attribute__((always_inline)) {
            constexpr int i = decltype(I)::value, t = i / 12, k = i % 12;
            mf_av(acc[t], w[t][k], xb[k], k == 0);
            if constexpr (i % 3 == 0 && i < 36) { constexpr int kk = i / 3; if (early) nx[kk] = ld128(rw, l16 + (kk & 3) * 1024, (unsigned)(O_G3F + (size_t)un * G3_B) + (wave * 12 + (kk & ~3)) * 1024); }
            if constexpr (i == 35) { if (early) extra(un); }
            if constexpr (i == 36) { if (!early) { ahead = __builtin_amdgcn_readfirstlane(pr) != SENT; if (ahead) { fetch(nx, un); extra(un); pr = probe(u2); } } }
            if constexpr (t >= 1 && k == 8) { pin_v(acc[t - 1]); *(LAS f32x4*)(wr[t - 1] + (u & 1) * 12288) = acc[t - 1]; }
        });
        bar();
        f32x4 p[3];
#pragma unroll
        for (int j = 0; j < 3; ++j) p[j] = *(LAS const f32x4*)(rd + (u & 1) * 12288 + j * 1024);
        mf_done(acc[3]);
        const f32x4 o = (acc[3] + p[0]) + (p[1] + p[2]);
        MARK(2);
        for (unsigned c = 0; any_lane(max(max(hv.x, hv.y), max(xv.x, xv.y)) == SENT);) {
            if (sp.fail(c, 0xC00 + u)) break;
            hv = ld64(rw, po, (unsigned)(O_HF + (size_t)u * XF_B)); xv = ld64(rw, po, (unsigned)(O_X1F + (size_t)u * XF_B));
        }
        const u32x2 r = {pk(bflo(hv.x) + (bflo(xv.x) + o[0]), bfhi(hv.x) + (bfhi(xv.x) + o[1])), pk(bflo(hv.y) + (bflo(xv.y) + o[2]), bfhi(hv.y) + (bfhi(xv.y) + o[3]))};
        __builtin_amdgcn_s_waitcnt(0x0F70);
        miss = !ahead || any_sent(nx); ahead = ahead && __builtin_amdgcn_readfirstlane(pr) != SENT;
        asm volatile("" : "+v"(hn), "+v"(xn));
        __builtin_amdgcn_raw_buffer_store_b64(r, ro, ((16 * u + n) * D_ + 16 * ct + 4 * g) * 2, 0, 0);
        MARK(3); FLUSH(u);
    };
    for (int u = 0; u < NUNIT; u += 2) { unit(u, xa, xc); unit(u + 1, xc, xa); }
}

DEV unsigned char* ws_base(const Params& P) { return P.ws + ((0u - (unsigned)(uintptr_t)P.ws) & ((2u << 20) - 1u)); }

__global__ void __launch_bounds__(256, 1) k_main(Params P) {
    extern __shared__ __attribute__((aligned(16))) char smem[];
    LAS char* lds = (LAS char*)smem;
    const int tid = threadIdx.x, lane = tid & 63, wave = __builtin_amdgcn_readfirstlane(tid >> 6);
    unsigned char* ws = ws_base(P);
    gu32* ctrl = (gu32*)(ws + O_CTRL);
    if (tid == 0) {
        unsigned x = xcc_id() & 7u, slot = 0;
        for (int k = 0; k < 8; ++k) {
            const unsigned xx = (x + k) & 7u;
            const unsigned t = __hip_atomic_fetch_add(ctrl + W_TICKET + 32 * xx, 1u, RLX_AGENT);
            if (t < 32u) { slot = xx * 32 + t; break; }
        }
        *(LAS unsigned*)lds = slot;
    }
    __syncthreads();
    const int slot = __builtin_amdgcn_readfirstlane(*(LAS unsigned*)lds);
    __syncthreads();
    Spin sp; sp.tmo = ctrl + W_TMO; sp.dead = false;
    const int xq = slot >> 5, t = slot & 31;
    int mlp = -1;
    {
        const unsigned dly = xq < 4 ? (t < 26 ? DLY1 : DLY2) : t < 16 ? 0 : t < 28 ? DLY3 : DLY2, t0 = rawnow();
        while (rawnow() - t0 < dly) __builtin_amdgcn_s_sleep(4);
    }
    if (xq < 4) {
        if (t < 8) role_S(P, ws, xq * 8 + t, lds, wave, lane, sp);
        else if (t < 18) role_F(P, ws, xq * 10 + (t - 8), lds, wave, lane, sp);
        else if (t < 26) role_UV(P, ws, xq * 8 + (t - 18), lds, wave, lane, sp);
        else mlp = xq * 6 + (t - 26);
    } else {
        const int y = xq - 4;
        if (t < 16) role_VC(P, ws, y * 8 + (t >> 1), t & 1, lds, wave, lane, sp);
        else if (t < 28) role_QK(P, ws, y * 12 + (t - 16), lds, wave, lane, sp);
        else if (t < 30) mlp = 24 + y * 2 + (t - 28);
    }
    if (mlp >= 0) {
        if (mlp < 5) role_W<1>(P, ws, mlp, lds, wave, lane, sp);
        else if (mlp < 10) role_W<2>(P, ws, mlp - 5, lds, wave, lane, sp);
        else if (mlp < 22) role_W<3>(P, ws, mlp - 10, lds, wave, lane, sp);
        else role_W4(P, ws, mlp - 22, lds, wave, lane, sp);
    }
    MARK(0); MARKC(1); FLUSH(129);
}

__global__ void __launch_bounds__(256) k_pre(Params P) {
    __shared__ float red[2][4][16];
    const int tid = threadIdx.x, b = blockIdx.x;
    unsigned char* ws = ws_base(P);
    {
        const char* code; int len;
        asm volatile("s_getpc_b64 %0\n.Lpre_pc:\n\ts_mov_b32 %1, .Lpre_pc-_Z6k_main6Params" : "=s"(code), "=s"(len));
        len = max(len, 0); code -= len;
        const u32x4 v = __builtin_amdgcn_raw_buffer_load_b128(mk_rsrc(code, (unsigned)len & ~15u), ((b >> 3) * 256 + tid) * 16, 0, 0);
        asm volatile("" :: "v"(v));
    }
    if (b < 128) {
        const int r = tid & 15, pc = tid >> 4, wave = tid >> 6;
        u32x4 v[5]; float s1 = 0.f;
        for (int i = 0; i < 5; ++i) {
            v[i] = *(const u32x4*)(P.x + (size_t)(16 * b + r) * D_ + (pc + 16 * i) * 8);
            for (int j = 0; j < 4; ++j) s1 += bflo(v[i][j]) + bfhi(v[i][j]);
        }
        s1 = x32(x16(s1, false), false);
        if ((tid & 63) < 16) red[0][wave][r] = s1;
        __syncthreads();
        const float mu = (red[0][0][r] + red[0][1][r] + red[0][2][r] + red[0][3][r]) * (1.f / D_);
        float s2 = 0.f;
        for (int i = 0; i < 5; ++i) for (int j = 0; j < 4; ++j) { const float a = bflo(v[i][j]) - mu, c = bfhi(v[i][j]) - mu; s2 += a * a + c * c; }
        s2 = x32(x16(s2, false), false);
        if ((tid & 63) < 16) red[1][wave][r] = s2;
        __syncthreads();
        const float rs = rsqrtf((red[1][0][r] + red[1][1][r] + red[1][2][r] + red[1][3][r]) * (1.f / D_) + 1e-5f);
        for (int i = 0; i < 5; ++i) {
            u32x4 o;
            for (int j = 0; j < 4; ++j) o[j] = pk((bflo(v[i][j]) - mu) * rs, (bfhi(v[i][j]) - mu) * rs);
            *(u32x4*)(ws + O_XF + (size_t)b * XF_B + (size_t)(pc + 16 * i) * 256 + r * 16) = o;
        }
    } else if (b < 160) {
        const int pg = b - 128, wave = tid >> 6;
        const rsrc_t rw = mk_rsrc(ws, (unsigned)WS_END);
        const unsigned o = (unsigned)pg * (2u << 20) + (2u << 20) - 16384u + wave * 2048;
        unsigned lat[2] = {0u, 0u};
        if ((tid & 63) == 0) {
            st32(1u, rw, o, 0); st32(1u, rw, o + 8192, 0); st32(1u, rw, o + 1024, 0); st32(1u, rw, o + 8192 + 1024, 0);
            vmwait<0>();
            lat[0] = lat[1] = ~0u;
            for (int i = 0; i < 4; ++i) {
                unsigned long long t0, t1;
                asm volatile("s_memtime %0\n\ts_waitcnt lgkmcnt(0)" : "=s"(t0) :: "memory");
                unsigned v = ld32(rw, o + (i & 1) * 8192 + (i >> 1) * 1024, 0);
                asm volatile("s_waitcnt vmcnt(0)\n\ts_memtime %0\n\ts_waitcnt lgkmcnt(0)" : "=s"(t1), "+v"(v) :: "memory");
                const unsigned d = (unsigned)(t1 - t0);
                if (i & 1) lat[1] = min(lat[1], d); else lat[0] = min(lat[0], d);
            }
            red[0][wave][0] = (float)lat[0]; red[0][wave][1] = (float)lat[1];
        }
        __syncthreads();
        if (tid == 0) {
            const float l0 = fminf(fminf(red[0][0][0], red[0][1][0]), fminf(red[0][2][0], red[0][3][0]));
            const float l1 = fminf(fminf(red[0][0][1], red[0][1][1]), fminf(red[0][2][1], red[0][3][1]));
            ((unsigned*)(ws + O_PAR))[pg] = (unsigned)((l1 < l0) != ((xcc_id() & 4u) != 0u));
            if (pg == 0) ((unsigned*)(ws + O_PAR))[32] = rawnow() - 1000u;
        }
    } else if (b < 163) {
        const int p = (b - 160) * 256 + tid;
        if (p < 640) {
            const unsigned* src = (const unsigned*)(P.mem0 + ((p >> 1) & 7) * D_ + (p >> 4) * 16 + (p & 1) * 2);
            const u32x4 v = {min(src[0], SENT - 1u), min(src[2], SENT - 1u), min(src[4], SENT - 1u), min(src[6], SENT - 1u)};
            const unsigned o = p * 16, oa = ((o >> 13) << 14) | (o & 8191u);
            *(u32x4*)(ws + oa) = v; *(u32x4*)(ws + oa + 8192) = v;
        }
    } else if (b < 192) {
        const int i = (b - 163) * 256 + tid;
        if (i < (int)(CTRL_BYTES / 16)) ((u32x4*)(ws + O_CTRL))[i] = u32x4{0u, 0u, 0u, 0u};
        else if (i < (int)(CTRL_BYTES / 16) + S_ / 2) ((u32x4*)(ws + O_ST))[i - CTRL_BYTES / 16] = SENT4;
    } else {
        const int nb = gridDim.x - 192, q = (b - 192) * 256 + tid;
        constexpr int RU = REC_BLOCKS * 1024;
        for (int i = q; i < KARM * RU; i += nb * 256) { const int r = i / RU, j = i % RU; if (r > 0 || j >= 2048) ((u32x4*)(ws + (size_t)r * 4 * PAGE_B))[j] = SENT4; }
        constexpr int NA = (2 * KARM + 1) * (int)(XF_B / 16), NG = 2 * KARM * (int)(G3_B / 16);
        for (int i = q; i < NA; i += nb * 256) { ((u32x4*)(ws + O_HF))[i] = SENT4; ((u32x4*)(ws + O_GF))[i] = SENT4; ((u32x4*)(ws + O_X1F))[i] = SENT4; }
        for (int i = q; i < NG; i += nb * 256) ((u32x4*)(ws + O_G3F))[i] = SENT4;
    }
}

extern "C" void kernel_launch(void* const* d_in, const int* in_sizes, int n_in, void* d_out, int out_size, void* d_ws, size_t ws_size, hipStream_t stream) {
    Params P;
    P.x = (const unsigned short*)d_in[0]; P.mem0 = (const unsigned short*)d_in[1];
    P.wq = (const unsigned short*)d_in[2]; P.wk = (const unsigned short*)d_in[3]; P.wv = (const unsigned short*)d_in[4]; P.wo = (const unsigned short*)d_in[5];
    P.w1 = (const unsigned short*)d_in[8]; P.w2 = (const unsigned short*)d_in[9]; P.w3 = (const unsigned short*)d_in[10]; P.w4 = (const unsigned short*)d_in[11];
    P.out = (unsigned short*)d_out; P.ws = (unsigned char*)d_ws;
    (void)hipFuncSetAttribute((const void*)k_main, hipFuncAttributeMaxDynamicSharedMemorySize, LDS_BYTES);
    hipLaunchKernelGGL(k_pre, dim3(512), dim3(256), 0, stream, P);
    hipLaunchKernelGGL(k_main, dim3(256), dim3(256), LDS_BYTES, stream, P);
}
```

```cpp
#include <hip/hip_runtime.h>
#include <stdint.h>

#define GAS __attribute__((address_space(1)))
#define LAS __attribute__((address_space(3)))
#define RLX_AGENT __ATOMIC_RELAXED, __HIP_MEMORY_SCOPE_AGENT
#define DEV __device__ __forceinline__
typedef __bf16 bf16x2 __attribute__((ext_vector_type(2)));
typedef __bf16 bf16x8 __attribute__((ext_vector_type(8)));
typedef __bf16 bf16x16 __attribute__((ext_vector_type(16)));
typedef short s16x4 __attribute__((ext_vector_type(4)));
typedef float f32x2 __attribute__((ext_vector_type(2)));
typedef float f32x4 __attribute__((ext_vector_type(4)));
typedef unsigned u32x2 __attribute__((ext_vector_type(2)));
typedef unsigned u32x4 __attribute__((ext_vector_type(4)));
typedef unsigned u32x8 __attribute__((ext_vector_type(8)));
typedef GAS unsigned gu32;
typedef __amdgpu_buffer_rsrc_t rsrc_t;

constexpr int S_ = 2048, D_ = 640, H_ = 32, E_ = 192, F_ = 1536, NSEG = 64, NUNIT = 128;
constexpr unsigned SENT = 0xFFFFFFFFu;
constexpr unsigned SPINCAP = 1u << 16;

constexpr unsigned PAGE_B = 2u << 20;
constexpr int NSLOT = 8;
constexpr unsigned R_MEM = 0;
constexpr unsigned R_P = 2 * 8192;
constexpr unsigned R_UB = 5 * 8192;
constexpr unsigned R_PX = 45 * 8192;
constexpr unsigned PX_B = 40960;
constexpr unsigned R_NF = 55 * 8192;
constexpr unsigned R_NS = 56 * 8192;
constexpr int REC_BLOCKS = 57;
#define DELTA_A 15
#define DELTA_B 15
#define VIS 4
#define SLOW 105
#define BULK_FAR 1
#define PSLEEP 4
#define PWAIT 3
#define DLY1 0
#define DLY2 1500
#define DLY3 0
#define NTMASK 0
#define ROTB 0
#define U0_1 117
#define U0_2 115
#define U0_3 111
#define U0_4 106
#define LATE 40
#define PACE 10
constexpr unsigned amap(unsigned off) { return ((off >> 13) << 14) | (off & 8191u); }
constexpr int UTAIL = 104;
constexpr int KARM = 4;
constexpr size_t O_CTRL = (size_t)NSLOT * 4 * PAGE_B, CTRL_BYTES = 65536;
constexpr int W_TMO = 0  , W_EPOCH = 64, W_READY = 12288  , W_CLAIM = 15360  , W_QKPROG = 1024  , W_VCPROG = 4096  , W_PROG = 8192  ;
constexpr size_t O_ST = O_CTRL + CTRL_BYTES + 4096;
constexpr size_t O_HF = O_ST + S_ * 8, XF_B = 20480;
constexpr size_t O_GF = O_HF + NUNIT * XF_B;
constexpr size_t O_X1F = O_GF + NUNIT * XF_B;
constexpr size_t O_G3F = O_X1F + NUNIT * XF_B, G3_B = 49152;
constexpr size_t O_XF = O_G3F + NUNIT * G3_B;
constexpr unsigned QX_B = 12288, R_QX = 0, R_KX = H_ * QX_B;
constexpr size_t WS_END = O_XF + NUNIT * XF_B;
static_assert(WS_END < (238u << 20), "workspace");
constexpr int LDS_BYTES = 160 * 1024;

struct Params {
    const unsigned short *x, *mem0, *wq, *wk, *wv, *wo, *w1, *w2, *w3, *w4;
    unsigned short* out;
    unsigned char* ws;
};

DEV unsigned xcc_id() { return (unsigned)__builtin_amdgcn_s_getreg(20 | (3 << 11)) & 0xfu; }
DEV rsrc_t mk_rsrc(const void* p, unsigned bytes) { return __builtin_amdgcn_make_buffer_rsrc((void*)p, 0, bytes, 0x00020000); }
DEV u32x4 ld128(rsrc_t r, unsigned v, unsigned s) { return __builtin_amdgcn_raw_buffer_load_b128(r, v, s, 16); }
DEV u32x2 ld64(rsrc_t r, unsigned v, unsigned s) { return __builtin_amdgcn_raw_buffer_load_b64(r, v, s, 16); }
DEV unsigned ld32(rsrc_t r, unsigned v, unsigned s) { return __builtin_amdgcn_raw_buffer_load_b32(r, v, s, 16); }
DEV void st128(u32x4 d, rsrc_t r, unsigned v, unsigned s) {
    __builtin_amdgcn_raw_buffer_store_b128(d, r, v, s, 16);
    asm volatile("s_nop 1" :: "v"(d) : "memory");
}
DEV void st64(u32x2 d, rsrc_t r, unsigned v, unsigned s) { __builtin_amdgcn_raw_buffer_store_b64(d, r, v, s, 16); }
DEV void st32(unsigned d, rsrc_t r, unsigned v, unsigned s) { __builtin_amdgcn_raw_buffer_store_b32(d, r, v, s, 16); }
DEV void st16(unsigned short d, rsrc_t r, unsigned v, unsigned s) { __builtin_amdgcn_raw_buffer_store_b16(d, r, v, s, 16); }
DEV u32x2 trd(LAS const char* p) { return __builtin_bit_cast(u32x2, __builtin_amdgcn_ds_read_tr16_b64_v4i16((LAS s16x4*)p)); }
DEV u32x4 cat2(u32x2 a, u32x2 b) { return __builtin_shufflevector(a, b, 0, 1, 2, 3); }
DEV u32x8 cat4(u32x4 a, u32x4 b) { return __builtin_shufflevector(a, b, 0, 1, 2, 3, 4, 5, 6, 7); }
DEV u32x4 lo4(u32x8 a) { return __builtin_shufflevector(a, a, 0, 1, 2, 3); }
DEV u32x4 hi4(u32x8 a) { return __builtin_shufflevector(a, a, 4, 5, 6, 7); }
DEV u32x4 lds128(LAS const char* p) { return *(LAS const u32x4*)p; }
DEV unsigned pk(float a, float b) { bf16x2 v = {(__bf16)a, (__bf16)b}; return __builtin_bit_cast(unsigned, v); }
DEV unsigned nosent(unsigned u) { return min(u, SENT - 1u); }
DEV unsigned pks(float a, float b) { return min(pk(a, b), SENT - 1u); }
DEV unsigned pka(float a, float b) { unsigned r; asm("v_cvt_pk_bf16_f32 %0, |%1|, |%2|" : "=v"(r) : "v"(a), "v"(b)); return r; }
DEV float bflo(unsigned u) { return __builtin_bit_cast(float, u << 16); }
DEV float bfhi(unsigned u) { return __builtin_bit_cast(float, u & 0xFFFF0000u); }
DEV unsigned f2u(float f) { return __builtin_bit_cast(unsigned, f); }
DEV float u2f(unsigned u) { return __builtin_bit_cast(float, u); }
DEV unsigned mx4(u32x4 v) { return max(max(v.x, v.y), max(v.z, v.w)); }
DEV bool any_lane(bool c) { return __builtin_amdgcn_ballot_w64(c) != 0; }
DEV void bar() { asm volatile("s_waitcnt lgkmcnt(0)" ::: "memory"); __builtin_amdgcn_s_barrier(); asm volatile("" ::: "memory"); }
DEV void fold32(float& a, float& b) {
    auto r = __builtin_amdgcn_permlane32_swap(f2u(a), f2u(b), false, false); a = u2f(r[0]); b = u2f(r[1]);
}
DEV float x32(float v, bool mx) { auto r = __builtin_amdgcn_permlane32_swap(f2u(v), f2u(v), false, false); return mx ? fmaxf(u2f(r[0]), u2f(r[1])) : u2f(r[0]) + u2f(r[1]); }
DEV float x16(float v, bool mx) { auto r = __builtin_amdgcn_permlane16_swap(f2u(v), f2u(v), false, false); return mx ? fmaxf(u2f(r[0]), u2f(r[1])) : u2f(r[0]) + u2f(r[1]); }
DEV float fmax2(float a, float b) { float r; asm("v_max_f32 %0, %1, %2" : "=v"(r) : "v"(a), "v"(b)); return r; }
DEV float fmax3(float a, float b, float c) { float r; asm("v_max3_f32 %0, %1, %2, %3" : "=v"(r) : "v"(a), "v"(b), "v"(c)); return r; }
#define ROWOP2(op) asm volatile("s_nop 1\n\t" \
    op " %0, %0, %0 row_ror:8 row_mask:0xf bank_mask:0xf\n\t" op " %1, %1, %1 row_ror:8 row_mask:0xf bank_mask:0xf\n\ts_nop 0\n\t" \
    op " %0, %0, %0 row_ror:4 row_mask:0xf bank_mask:0xf\n\t" op " %1, %1, %1 row_ror:4 row_mask:0xf bank_mask:0xf\n\ts_nop 0\n\t" \
    op " %0, %0, %0 row_ror:2 row_mask:0xf bank_mask:0xf\n\t" op " %1, %1, %1 row_ror:2 row_mask:0xf bank_mask:0xf\n\ts_nop 0\n\t" \
    op " %0, %0, %0 row_ror:1 row_mask:0xf bank_mask:0xf\n\t" op " %1, %1, %1 row_ror:1 row_mask:0xf bank_mask:0xf" : "+v"(a), "+v"(b))
DEV void row_max2(float& a, float& b) { ROWOP2("v_max_f32_dpp"); }
DEV void row_sum2(float& a, float& b) { ROWOP2("v_add_f32_dpp"); }
DEV unsigned pair_cols(unsigned p, unsigned psel) { return __builtin_amdgcn_perm(p, __builtin_amdgcn_update_dpp(0u, p, 0xB1, 0xf, 0xf, true), psel); }
DEV float ror8(float v) { return u2f(__builtin_amdgcn_update_dpp(0u, f2u(v), 0x128, 0xf, 0xf, true)); }
template <int OFF> DEV void glds(unsigned voff, const void* sbase, unsigned lds_dst) {
    asm volatile("s_mov_b32 m0, %2\n\ts_nop 0\n\tglobal_load_lds_dwordx4 %0, %1 offset:%3 sc1" :: "v"(voff), "s"(sbase), "s"(lds_dst), "n"(OFF) : "memory");
}
template <int N, int STEP> DEV void glds_dev(unsigned voff, const void* sbase, unsigned lds_dst) {
    static_assert(N == 2 || N == 4, "");
    if (N == 2) asm volatile("s_mov_b32 m0, %2\n\ts_nop 0\n\tglobal_load_lds_dwordx4 %0, %1 sc1\n\tglobal_load_lds_dwordx4 %0, %1 offset:%3 sc1" :: "v"(voff), "s"(sbase), "s"(lds_dst), "n"(STEP) : "memory");
    else asm volatile("s_mov_b32 m0, %2\n\ts_nop 0\n\tglobal_load_lds_dwordx4 %0, %1 sc1\n\tglobal_load_lds_dwordx4 %0, %1 offset:%3 sc1\n\tglobal_load_lds_dwordx4 %0, %1 offset:%4 sc1\n\tglobal_load_lds_dwordx4 %0, %1 offset:%5 sc1"
                      :: "v"(voff), "s"(sbase), "s"(lds_dst), "n"(STEP), "n"(2 * STEP), "n"(3 * STEP) : "memory");
}
template <int N> DEV void vmwait() { asm volatile("s_waitcnt vmcnt(%0)" :: "n"(N) : "memory"); }
template <class T> DEV void pin_a(T& v) { asm volatile("" : "+a"(v)); }
template <class T> DEV void pin_v(T& v) { asm volatile("" : "+v"(v)); }
#define MF_ "v_mfma_f32_16x16x32_bf16 "
DEV void smf_a(f32x4& c, u32x4 a, u32x8 b, int idx) { asm volatile("v_smfmac_f32_16x16x64_bf16 %0, %1, %2, %3" : "+v"(c) : "v"(a), "a"(b), "v"(idx)); }
DEV void smf_v(f32x4& c, u32x4 a, u32x8 b, int idx) { asm volatile("v_smfmac_f32_16x16x64_bf16 %0, %1, %2, %3" : "+v"(c) : "v"(a), "v"(b), "v"(idx)); }
DEV void mf_vv(f32x4& c, u32x4 a, u32x4 b, bool first) { if (first) asm volatile(MF_ "%0, %1, %2, 0" : "=&v"(c) : "v"(a), "v"(b)); else asm volatile(MF_ "%0, %1, %2, %0" : "+v"(c) : "v"(a), "v"(b)); }
DEV void mf_av(f32x4& c, u32x4 a, u32x4 b, bool first) { if (first) asm volatile(MF_ "%0, %1, %2, 0" : "=&v"(c) : "a"(a), "v"(b)); else asm volatile(MF_ "%0, %1, %2, %0" : "+v"(c) : "a"(a), "v"(b)); }
DEV void mf_va(f32x4& c, u32x4 a, u32x4 b, bool first) { if (first) asm volatile(MF_ "%0, %1, %2, 0" : "=&v"(c) : "v"(a), "a"(b)); else asm volatile(MF_ "%0, %1, %2, %0" : "+v"(c) : "v"(a), "a"(b)); }
DEV void mf_done(f32x4& a) { asm volatile("s_nop 7" : "+v"(a)); }
DEV void mf_done(f32x4& a, f32x4& b) { asm volatile("s_nop 7" : "+v"(a), "+v"(b)); }
DEV void mf_done(f32x4& a, f32x4& b, f32x4& c) { asm volatile("s_nop 7" : "+v"(a), "+v"(b), "+v"(c)); }
DEV void mf_done(f32x4& a, f32x4& b, f32x4& c, f32x4& d) { asm volatile("s_nop 7" : "+v"(a), "+v"(b), "+v"(c), "+v"(d)); }
DEV float zero1() { float v; asm volatile("v_mov_b32 %0, 0" : "=v"(v)); return v; }
DEV void zero_acc(f32x4& a) { a = f32x4{zero1(), zero1(), zero1(), zero1()}; asm volatile("s_nop 1" : "+v"(a)); }
DEV LAS char* opq(LAS char* p) { unsigned u = (unsigned)(uintptr_t)p; asm volatile("" : "+v"(u)); return (LAS char*)(uintptr_t)u; }
template <int V> struct IC { static constexpr int value = V; constexpr operator int() const { return V; } };
template <int I, int N, class F> DEV void sfor(F&& f) { if constexpr (I < N) { f(IC<I>{}); sfor<I + 1, N>(f); } }
DEV void lgkm0() { asm volatile("s_waitcnt lgkmcnt(0)" ::: "memory"); __builtin_amdgcn_sched_barrier(0); }

#define MARK(i)
#define MARK2(i)
#define MARK3(i)
#define SETV(i, v)
#define MARKC(i)
#define FLUSH(step)
struct Spin {
    gu32* tmo; bool dead; unsigned tag;
    unsigned org, ev, cpf;
    DEV void start() { asm volatile("" :: "v"(cpf)); tag = __builtin_amdgcn_readfirstlane(ev) + 1u; }
    DEV bool fail(unsigned& n, unsigned code) {
        asm volatile("" ::: "memory");
        if (dead) return true;
        ++n;
        if (n > SPINCAP) { __hip_atomic_store(tmo + 1, code, RLX_AGENT); __hip_atomic_store(tmo, tag, RLX_AGENT); dead = true; return true; }
        if ((n & 63u) == 0u && (unsigned)__builtin_amdgcn_readfirstlane(__hip_atomic_load(tmo, RLX_AGENT)) == tag) { dead = true; return true; }
        return false;
    }
};

template <int NT> DEV int img_rot(int rho) { return (NT & 1) ? 0 : (NT & 2) ? ((rho >> 2) & 1) : (NT & 4) ? ((rho >> 1) & 3) : (rho & 7); }
DEV int swap23(int r) { return (r & 3) | ((r >> 1) & 4) | ((r << 1) & 8) | (r & 16); }
template <int NT> struct WSrc {
    unsigned voff[NT]; rsrc_t r;
    DEV void init(const char* src, int lane, unsigned stride, int trot = 0) {
        r = mk_rsrc(src - 4096, 0xFFFFFFFFu);
#pragma unroll
        for (int i = 0; i < NT; ++i) {
            const int P = 64 * i + lane, rho = P / (2 * NT), cs = P % (2 * NT);
            const int tile = (((cs >> 1) ^ img_rot<NT>(rho)) + trot) % NT;
            voff[i] = (unsigned)swap23(rho) * stride + (unsigned)(2 * tile + (cs & 1)) * 16u + 4096u - (i & 3) * 1024u;
        }
    }
    DEV void issue(unsigned so, unsigned lds_dst) {
        constexpr bool nt = (NTMASK >> (NT == 6 ? 1 : NT == 10 ? 2 : NT == 3 ? 3 : 0)) & 1;
#define BL_(v, o, p) "buffer_load_dwordx4 " v ", %0, %1 offen offset:" o p " lds\n\t"
#define GRP_(p) \
            if (i + 4 <= NT) asm volatile("s_mov_b32 m0, %2\n\ts_nop 0\n\t" BL_("%3", "0", p) BL_("%4", "1024", p) BL_("%5", "2048", p) BL_("%6", "3072", p) :: "s"(r), "s"(so), "s"(d), "v"(voff[i]), "v"(voff[(i + 1) % NT]), "v"(voff[(i + 2) % NT]), "v"(voff[(i + 3) % NT]) : "memory"); \
            else if (i + 3 == NT) asm volatile("s_mov_b32 m0, %2\n\ts_nop 0\n\t" BL_("%3", "0", p) BL_("%4", "1024", p) BL_("%5", "2048", p) :: "s"(r), "s"(so), "s"(d), "v"(voff[i]), "v"(voff[(i + 1) % NT]), "v"(voff[(i + 2) % NT]) : "memory"); \
            else asm volatile("s_mov_b32 m0, %2\n\ts_nop 0\n\t" BL_("%3", "0", p) BL_("%4", "1024", p) :: "s"(r), "s"(so), "s"(d), "v"(voff[i]), "v"(voff[(i + 1) % NT]) : "memory");
#pragma unroll
        for (int i = 0; i < NT; i += 4) {
            const unsigned d = lds_dst + i * 1024;
            if (nt) { GRP_(" nt") } else { GRP_("") }
        }
#undef GRP_
#undef BL_
    }
};
template <int NT, bool PAIR> struct WRd {
    static constexpr int RR = (NT & 1) ? 1 : (NT & 2) ? 2 : (NT & 4) ? 4 : 8, NB = PAIR ? RR / 2 : RR;
    LAS char* b[NB];
    DEV void init(LAS char* base, int lane) {
        const int g = lane >> 4, q = (lane >> 2) & 3, p = lane & 3, rho = q | ((g & 1) << 2) | ((g >> 1) << 4), rot = img_rot<NT>(rho);
#pragma unroll
        for (int j = 0; j < NB; ++j) b[j] = opq(base + rho * (NT * 32) + (PAIR ? ((2 * j + (p >> 1)) ^ rot) * 32 + 16 * (p & 1) : (j ^ rot) * 32 + 8 * p));
    }
    DEV LAS char* at(int off, int tile) const { return PAIR ? b[(tile >> 1) % NB] + off + (tile / RR) * RR * 32 + 8 * (tile & 1) : b[tile % NB] + off + (tile / RR) * RR * 32; }
    DEV u32x4 get(int off, int tile) const { LAS char* a = at(off, tile); return cat2(trd(a), trd(a + 8 * NT * 32)); }
};
DEV void vmwait_n(int n) {
    switch (n) {
#define VW(n) case n: vmwait<n>(); break;
        VW(0) VW(3) VW(4) VW(6) VW(8) VW(9) VW(10) VW(12) VW(15) VW(16) VW(18) VW(20) VW(24) VW(26) VW(28) VW(29) VW(30) VW(32) VW(35) VW(36) VW(40)
#undef VW
        default: vmwait<0>(); break;
    }
}
template <int NT, int NK, int NKT, int RING, int SROT, bool PAIR, class Sink>
DEV void stream_w(const char* src, unsigned stride, LAS char* lds, unsigned base, int lane, Sink&& sink, int rot = 0, int trot = 0) {
    auto krow = [&](int k) { int kk = k + 2 * rot; if (kk >= NKT) kk -= NKT; return (unsigned)kk * 32u * stride; };
    WSrc<NT> ws; ws.init(src, lane, stride, trot);
    const unsigned lbase = (unsigned)(uintptr_t)lds + base;
#pragma unroll
    for (int k = 0; k < RING && k < NKT; ++k) ws.issue(krow(k), lbase + ((k + SROT) % RING) * NT * 1024);
    WRd<NT, PAIR> rd; rd.init(lds + base, lane);
#pragma unroll
    for (int k = 0; k < NK; ++k) {
        const int issued = (k + RING < NKT) ? k + RING : NKT;
        vmwait_n((issued - k - 1) * NT);
        u32x4 fr[NT];
#pragma unroll
        for (int t = 0; t < NT; ++t) fr[t] = rd.get(((k + SROT) % RING) * NT * 1024, t);
#pragma unroll
        for (int t = 0; t < NT; ++t) sink(t, k, fr[t]);
        if (k + RING < NKT) { lgkm0(); ws.issue(krow(k + RING), lbase + ((k + SROT) % RING) * NT * 1024); }
    }
}

DEV unsigned page(unsigned par, int s, int p) { const unsigned i = (unsigned)(s & (NSLOT - 1)) * 4u + (unsigned)p; return i * PAGE_B + (((par >> i) & 1u) << 13); }
DEV unsigned rec(unsigned par, int s) { return page(par, s, 0); }
DEV void wait_slot(rsrc_t rw, int s, unsigned& seen, int rep, Spin& sp) {
    for (unsigned c = 0; (int)seen + 6 < s;) {
        seen = __builtin_amdgcn_readfirstlane(ld32(rw, 0, (unsigned)(O_CTRL + 4 * (W_PROG + 32 * rep))));
        if ((int)seen + 6 >= s || sp.fail(c, 0x280 + s)) break;
        __builtin_amdgcn_s_sleep(8);
    }
}
template <bool START = true> DEV unsigned load_par(rsrc_t rw, int lane, Spin& sp) {
    if (START) sp.start();
    u32x4 a, b;
    for (unsigned c = 0;;) {
        a = ld128(rw, lane * 128, (unsigned)(O_CTRL + 4 * W_READY)); b = ld128(rw, (64 + (lane & 15)) * 128, (unsigned)(O_CTRL + 4 * W_READY));
        if (!any_lane(a.x != sp.tag || a.w != ~sp.tag || b.x != sp.tag || b.w != ~sp.tag) || sp.fail(c, 0x40)) break;
        __builtin_amdgcn_s_sleep(16);
    }
    unsigned m = (unsigned)__builtin_amdgcn_ballot_w64(a.y == 1u);
    const int c = __builtin_popcount(m ^ 0xAAAAAAAAu);
    if (c <= 10) m = 0xAAAAAAAAu; else if (c >= 22) m = 0x55555555u;
    sp.org = __builtin_amdgcn_readfirstlane(a.z);
    return m ^ (BULK_FAR ? 0xEEEEEEEEu : 0u);
}
DEV unsigned sent1() { unsigned v; asm volatile("v_mov_b32 %0, -1" : "=v"(v)); return v; }
#define SENT4 (u32x4{sent1(), sent1(), sent1(), sent1()})
#define SENT2 (u32x2{sent1(), sent1()})
DEV unsigned rawnow() { unsigned long long v; asm volatile("s_memrealtime %0\n\ts_waitcnt lgkmcnt(0)" : "=s"(v) :: "memory"); return (unsigned)v; }
struct Clock {
    unsigned org;
    DEV unsigned now() const { return rawnow() - org; }
    DEV unsigned wait_until(unsigned T) const {
        unsigned t = rawnow(); T += org;
        if ((int)(T - t) > 1000) T = t + 1000u;
        while ((int)(t - T) < 0) t = rawnow();
        return t - org;
    }
};
DEV unsigned umax_dpp(unsigned v, int k) {
    switch (k) { case 1: return max(v, (unsigned)__builtin_amdgcn_update_dpp(0u, v, 0x121, 0xf, 0xf, true)); case 2: return max(v, (unsigned)__builtin_amdgcn_update_dpp(0u, v, 0x122, 0xf, 0xf, true));
                 case 4: return max(v, (unsigned)__builtin_amdgcn_update_dpp(0u, v, 0x124, 0xf, 0xf, true)); default: return max(v, (unsigned)__builtin_amdgcn_update_dpp(0u, v, 0x128, 0xf, 0xf, true)); }
}
DEV unsigned half_max(unsigned v) {
    v = umax_dpp(umax_dpp(umax_dpp(umax_dpp(v, 1), 2), 4), 8);
    auto r = __builtin_amdgcn_permlane16_swap(v, v, false, false);
    return max((unsigned)r[0], (unsigned)r[1]);
}
DEV unsigned wave_max(unsigned v) { const unsigned m = half_max(v); return max((unsigned)__builtin_amdgcn_readlane(m, 0), (unsigned)__builtin_amdgcn_readlane(m, 32)); }
DEV u32x2 read_notes(u32x2 nf) {
    const unsigned tp = wave_max(nf.x == SENT ? 0u : nf.x), df = wave_max(nf.y == SENT ? 0u : min(nf.y, 400u));
    return u32x2{tp, tp + df + DELTA_B};
}
DEV unsigned mem_time(unsigned tp, unsigned plan, unsigned tn) { return tn + VIS > tp ? max(plan, tn + SLOW) : plan; }
struct MemStream {
    rsrc_t rw; unsigned l16, vn, lbase, ko[10];
    static DEV int rot_of(int base, int wave) { return (ROTB * base + ((wave * 5 + 1) >> 1)) % 10; }
    DEV void init(rsrc_t r, LAS char* lds, int lane, int rot, bool reader) {
        rw = r; l16 = lane * 16; vn = reader ? min(lane, 39) * 128 : (lane & 15) * 8; lbase = (unsigned)(uintptr_t)lds;
#pragma unroll
        for (int k = 0; k < 10; ++k) { int kk = k + rot; if (kk >= 10) kk -= 10; ko[k] = kk * 1024 + (kk >= 8 ? 8192 : 0); }
    }
    DEV u32x4 ld(unsigned rb, int kb) const { return ld128(rw, l16, rb + ko[kb]); }
    DEV unsigned so(unsigned rb, int kb) const { unsigned v = rb + ko[kb]; asm volatile("" : "+s"(v)); return v; }
    DEV u32x4 lds_(unsigned s) const { u32x4 v = ld128(rw, l16, s); __builtin_amdgcn_sched_barrier(0); return v; }
    DEV LAS char* at(int kb) const { unsigned a; asm volatile("v_add_u32 %0, %1, %2" : "=v"(a) : "s"(ko[kb] + lbase), "v"(l16)); return (LAS char*)(uintptr_t)a; }
    DEV void share(unsigned so, int kb) const {
        asm volatile("s_mov_b32 m0, %3\n\ts_nop 0\n\tbuffer_load_dwordx4 %0, %1, %2 offen sc1 lds" :: "v"(l16), "s"(rw), "s"(so), "s"(ko[kb] + lbase) : "memory");
    }
    DEV void lda(u32x4& a, unsigned so) const { asm volatile("buffer_load_dwordx4 %0, %1, %2, %3 offen sc1" : "=v"(a) : "v"(l16), "s"(rw), "s"(so)); }
    DEV u32x4 get(int kb) const { return lds128(at(kb)); }
    static DEV unsigned long long bad(u32x4 a, u32x4 b, u32x4 c, u32x4 d) {
        unsigned long long m = __builtin_amdgcn_ballot_w64(max(max(mx4(a), mx4(b)), max(mx4(c), mx4(d))) == SENT); asm volatile("" : "+s"(m)); return m;
    }
};
template <int N> DEV void vmw(u32x4& a) { asm volatile("s_waitcnt vmcnt(%1)" : "+v"(a) : "n"(N)); }
DEV unsigned long long nan2(float a, float b) { unsigned long long m; asm volatile("v_cmp_u_f32 %0, %1, %2" : "=s"(m) : "v"(a), "v"(b)); return m; }
DEV float add1(float a, float b) { float r; asm volatile("v_add_f32 %0, %1, %2" : "=v"(r) : "v"(a), "v"(b)); return r; }
DEV unsigned pk1(float a, float b) { unsigned r; asm volatile("v_cvt_pk_bf16_f32 %0, %1, %2" : "=v"(r) : "v"(a), "v"(b)); return r; }
DEV unsigned track(unsigned est, unsigned m, int s) { return m > est ? min(m, est + (s < 8 ? 30u : 3u)) : est - 1u - ((est - m) >> 3); }
struct AFrag {
    LAS char *p0, *p1;
    DEV void init(LAS char* img, int lane) {
        const int ga = lane >> 4, base = ga * 256 + (lane & 15) * 8, sw = (ga & 1) * 128;
        p0 = opq(img + base + sw); p1 = opq(img + base + (128 ^ sw));
    }
    DEV u32x4 get(int off) const { return cat2(trd(p0 + off), trd(p1 + off)); }
    static DEV int wofs(int lane, int odd) { const int g = lane >> 4, n = lane & 15; return (((2 * (n >> 2) + (n & 1)) * 32) ^ (odd * 128)) + g * 8 + ((n >> 1) & 1) * 2; }
};
DEV unsigned fold_pk(f32x4 c, float& f0, float& f1) {
    unsigned r;
    asm volatile("v_add_f32 %1, %3, %4\n\tv_add_f32 %2, %5, %6\n\tv_cvt_pk_bf16_f32 %0, %1, %2" : "=v"(r), "=&v"(f0), "=&v"(f1) : "v"(c[0]), "v"(c[1]), "v"(c[2]), "v"(c[3]));
    return r;
}
DEV void st2x16(LAS char* p0, LAS char* p1, int off, unsigned v) { *(LAS unsigned short*)(p0 + off) = (unsigned short)v; *(LAS unsigned short*)(p1 + off) = (unsigned short)(v >> 16); }

constexpr float SCALE_L2E = 0.07216878364870322f * 1.4426950408889634f;

DEV void role_S(const Params& P, unsigned char* ws, int h, LAS char* lds, int wave, int lane, Spin& sp) {
    constexpr int L_QK = 8192, L_FL = 14336, L_W = 16384, L_WS = 36864, L_KX = L_W + L_WS, L_QX = L_W + 3 * L_WS;
    const rsrc_t rw = mk_rsrc(ws, (unsigned)WS_END);
    const int rot = MemStream::rot_of(h, wave);
    u32x8 w[6][8];
    {
        const char* src = (const char*)(wave < 2 ? P.wq : P.wk) + h * 384 + (wave & 1) * 192;
        stream_w<6, 16, 20, 6, 4, false>(src, 12288u, lds, L_W + wave * L_WS, lane,
            [&](int t, int k, u32x4 f) {
                if (k & 1) { w[t][k >> 1] = cat4(lo4(w[t][k >> 1]), f); if ((k >> 1) * 6 + t < 32) pin_a(w[t][k >> 1]); } else w[t][k >> 1] = cat4(f, f); }, rot);
        vmwait<0>();
    }
    MARK(2); FLUSH(128);
    bar();
    const unsigned par = load_par(rw, lane, sp);
    const int idx = (lane & 1) ? 0xEEEE : 0x4444;
    const int g = lane >> 4, n = lane & 15;
    const unsigned cq = (unsigned)(O_CTRL + 4 * (W_QKPROG + 32 * (((wave < 2 ? 384 : 0) + h * 12 + (lane & 1) * 11) >> 4)));
    unsigned have = 0;
    auto wait_qk = [&](int s) {
        if (have > (unsigned)s) return;
        for (unsigned c = 0;;) {
            const unsigned v = ld32(rw, cq, 0);
            have = min((unsigned)__builtin_amdgcn_readlane(v, 0), (unsigned)__builtin_amdgcn_readlane(v, 1));
            if (have > (unsigned)s || sp.fail(c, 0x200 + s)) break;
            __builtin_amdgcn_s_sleep(2);
        }
    };
    Clock ck; ck.org = sp.org;
    MemStream ms; ms.init(rw, lds, lane, rot, wave == 1);
    volatile LAS unsigned* lfail = (volatile LAS unsigned*)(lds + L_FL);
    if (wave == 0) *lfail = ~0u;
    WRd<6, false> wr; wr.init(lds + L_W + wave * L_WS + 12288, lane);
    AFrag af; af.init(lds + L_QK, lane);
    LAS char* we = opq(lds + L_QK + wave * 1536 + AFrag::wofs(lane, 0));
    LAS char* wo = opq(lds + L_QK + wave * 1536 + AFrag::wofs(lane, 1));
    const unsigned psel = (n & 1) ? 0x07060302u : 0x01000504u, psel2 = (n & 2) ? 0x07060302u : 0x01000504u;
    unsigned so1, so2;
    if (wave == 0) {
        const int i = 2 * g + (n & 1), half = (n >> 1) & 1;
        so1 = amap(R_P) + (h >> 4) * 16384 + (h & 15) * 512 + ((n >> 3) * 16 + 2 * i + half) * 16 + ((n >> 2) & 1) * 8;
        so2 = amap(R_P + 16384) + h * 128 + (2 * (2 * g + half) + ((n >> 2) & 1)) * 8 + (n >> 3) * 4;
    } else {
        const unsigned b = amap(R_PX) + (wave & 1) * amap(PX_B);
        so1 = (wave & 1) ? b + (h >> 3) * 16384 + (h & 7) * 1024 + lane * 16 : b + (h >> 4) * 16384 + ((h >> 1) & 7) * 1024 + lane * 16 + (h & 1) * 8;
        so2 = b + 65536 + h * 256 + n * 16 + g * 4;
    }
    unsigned xo[3];
#pragma unroll
    for (int j = 0; j < 3; ++j) { const unsigned o = (wave < 2 ? R_KX : R_QX) + h * QX_B + j * 4096; xo[j] = (((o >> 13) << 14) | (o & 8191u)) + (wave < 2 ? 0 : (wave & 1) * 1024); }
    auto stage_k = [&](int s) __attribute__((always_inline)) {
        wait_qk(s);
#pragma unroll
        for (int j = 0; j < 3; ++j) glds_dev<4, 1024>(ms.l16, ws + (page(par, s, 1) + xo[j]), (unsigned)(uintptr_t)lds + L_KX + (s & 1) * L_WS + j * 4096);
    };
    auto stage_q = [&](int s) __attribute__((always_inline)) {
        wait_qk(s);
#pragma unroll
        for (int j = 0; j < 3; ++j) glds_dev<2, 2048>(ms.l16, ws + (page(par, s, 1) + xo[j]), (unsigned)(uintptr_t)lds + L_QX + (wave & 1) * 1024 + j * 4096);
    };
    auto lwq = [&](int j, int q) {
        const int t = j % 6, kb = 1 - j / 6;
        return trd(wr.at((2 * kb + (q >> 1)) * 6144 + (q & 1) * 1536, t));
    };
    unsigned dur = 150, tn = 0;
    if (wave == 0) __builtin_amdgcn_s_setprio(3);
    if (wave == 1) stage_k(0); else if (wave >= 2) stage_q(0);
    vmwait<0>();
    bar();
    if (wave == 1) stage_k(1);
    for (int s = 0; s < NSEG; ++s) {
        const unsigned rb = rec(par, s);
        f32x4 acc[6];
        u32x2 st[12][4], nf; u32x4 a[10], kxf[12];
        LAS const char* kx = opq(lds + L_KX + (s & 1) * L_WS + lane * 16);
        unsigned long long bad = 0;
        auto prep = [&]() __attribute__((always_inline)) {
#pragma unroll
            for (int t = 0; t < 6; ++t) zero_acc(acc[t]);
#pragma unroll
            for (int i = 0; i < 12; ++i) st[i >> 2][i & 3] = lwq(i >> 2, i & 3);
        };
        float fa[6], fb[6]; unsigned fp[6];
        auto fin = [&](int t, int step) __attribute__((always_inline)) {
            if (step == 0) fa[t] = add1(acc[t][0], acc[t][1]);
            if (step == 1) fb[t] = add1(acc[t][2], acc[t][3]);
            if (step == 2) fp[t] = pk1(fa[t], fb[t]);
            if (step == 3) *(LAS unsigned short*)(((t & 1) ? wo : we) + t * 256) = (unsigned short)fp[t];
            if (step == 4) *(LAS unsigned short*)(((t & 1) ? wo : we) + t * 256 + 4) = (unsigned short)(fp[t] >> 16);
        };
        unsigned long long bad2 = 0;
        unsigned qp;
        auto slot = [&](auto I) __attribute__((always_inline)) {
            constexpr int i = decltype(I)::value % 64;
            constexpr bool fast = decltype(I)::value < 64;
            if constexpr (fast) {
                if constexpr (i == 0) vmw<6>(a[0]);
                if constexpr (i == 6) vmw<5>(a[9]);
                if constexpr (i == 7) vmw<4>(a[1]);
                if constexpr (i == 8) vmw<3>(a[2]);
            }
            if constexpr (i == 15) asm volatile("s_waitcnt vmcnt(0)\n\ts_barrier" ::: "memory");
            if constexpr (i == 15) { MARK3(0); }
            if constexpr (i == 36) { MARK3(5); }
            constexpr bool isl = i >= 6 && i <= 50 && (i & 3) == 2;
            constexpr int nl = i < 6 ? 0 : i > 50 ? 12 : (i - 6 + 3) / 4;
            if constexpr (isl) {
                smf_v(acc[nl % 6], a[9 - nl / 6], cat4(cat2(st[nl][0], st[nl][1]), cat2(st[nl][2], st[nl][3])), idx);
            } else {
                constexpr int r = i - nl;
                constexpr int kb = r < 7 ? r / 6 : r == 7 ? 2 : r < 13 ? 1 : r < 30 ? r / 6 : 5 + ((r - 30) % 6) / 2;
                constexpr int t = r < 7 ? r % 6 : r == 7 ? 0 : r < 13 ? r - 7 : r < 30 ? r % 6 : 2 * ((r - 30) / 6) + (r & 1);
                if constexpr (kb * 6 + t < 32) smf_a(acc[t], a[kb], w[t][kb], idx); else smf_v(acc[t], a[kb], w[t][kb], idx);
            }
            if constexpr (i == 0) { MARK2(2); }
            if constexpr (i == 12) bad = nan2(acc[0][0], acc[0][1]);
            if constexpr (i == 13) bad2 = nan2(acc[0][2], acc[0][3]);
            if constexpr (i == 15) a[3] = ms.get(3);
            if constexpr (i == 18) nf = ld64(rw, ms.vn, rb + amap(R_NF));
            if constexpr (i == 25) qp = ld32(rw, cq, 0);
            if constexpr (i == 20) a[8] = ms.get(8);
            if constexpr (i == 22) a[4] = ms.get(4);
            if constexpr (i == 28) a[5] = ms.get(5);
            if constexpr (i == 31) a[6] = ms.get(6);
            if constexpr (i == 34) a[7] = ms.get(7);
            if constexpr (i >= 7 && i < 43) st[(i - 7) / 4 + 3][(i - 7) & 3] = lwq((i - 7) / 4 + 3, (i - 7) & 3);
            if constexpr (i >= 43 && i < 51) kxf[i - 43] = lds128(kx + (i - 43) * 1024);
            if constexpr (i >= 55 && i < 59) kxf[i - 47] = lds128(kx + (i - 47) * 1024);
            if constexpr (i >= 49 && i < 52) { fin(0, i - 49); } if constexpr (i >= 52 && i < 55) { fin(1, i - 52); if constexpr (i < 54) fin(0, i - 49); }
            if constexpr (i >= 55 && i < 57) fin(1, i - 52);
            if constexpr (i >= 57) { fin(2, i - 57); fin(3, i - 57); }
        };
        prep();
        MARK(0); SETV(7, 0);
        const unsigned o0 = ms.so(rb, 0), o9 = ms.so(rb, 9), o1 = ms.so(rb, 1), o2 = ms.so(rb, 2);
        unsigned tg = ck.wait_until(tn);
        ms.lda(a[0], o0); ms.lda(a[9], o9); ms.lda(a[1], o1); ms.lda(a[2], o2);
        ms.share(o0, 0); ms.share(o1, 1); ms.share(o2, 2);
        if (tg - tn < 10u) tg = tn;
        SETV(1, tg + ck.org);
        sfor<0, 15>(slot);
        if (__builtin_expect((bad | bad2) != 0, 0)) {
            asm volatile("s_nop 7\n\ts_waitcnt vmcnt(0)" ::: "memory");
            *lfail = (unsigned)s; SETV(7, 1);
            for (unsigned c = 0;;) {
                a[0] = ms.ld(rb, 0); a[9] = ms.ld(rb, 9); a[1] = ms.ld(rb, 1); a[2] = ms.ld(rb, 2);
                if (MemStream::bad(a[0], a[9], a[1], a[2]) == 0 || sp.fail(c, 0x100 + s)) break;
            }
            ms.share(o0, 0); ms.share(o1, 1); ms.share(o2, 2);
            prep();
            sfor<64, 79>(slot);
            asm volatile("s_nop 7" : "+v"(acc[0]), "+v"(acc[1]), "+v"(acc[2]), "+v"(acc[3]), "+v"(acc[4]), "+v"(acc[5]));
        }
        MARK3(6);
        sfor<15, 60>(slot);
        fin(2, 3); fin(2, 4); fin(3, 3); fin(3, 4);
        asm volatile("s_nop 0");
        fin(4, 0); fin(4, 1); fin(5, 0); fin(5, 1); fin(4, 2); fin(5, 2); fin(4, 3); fin(4, 4); fin(5, 3); fin(5, 4);
        f32x4 am, a0, a1;
        MARK2(3);
        bar();
        MARK2(4);
        u32x4 qa[3], ka[3];
#pragma unroll
        for (int kb = 0; kb < 3; ++kb) { qa[kb] = af.get(kb * 1024); __builtin_amdgcn_sched_barrier(0); }
#pragma unroll
        for (int kb = 0; kb < 3; ++kb) { ka[kb] = af.get(3072 + kb * 1024); __builtin_amdgcn_sched_barrier(0); }
        asm volatile("" ::: "memory");
        if (wave >= 2) {
            const int Tq = wave & 1;
            have = min((unsigned)__builtin_amdgcn_readlane(qp, 0), (unsigned)__builtin_amdgcn_readlane(qp, 1));
            u32x4 qx[6];
#pragma unroll
            for (int ep = 0; ep < 2; ++ep) qx[ep] = lds128(lds + L_QX + lane * 16 + Tq * 1024 + ep * 2048);
            zero_acc(am);
#pragma unroll
            for (int ep = 0; ep < 6; ++ep) {
                if (ep < 4) qx[ep + 2] = lds128(lds + L_QX + lane * 16 + Tq * 1024 + (ep + 2) * 2048);
                mf_vv(a0, kxf[2 * ep], qx[ep], ep == 0); mf_vv(a1, kxf[2 * ep + 1], qx[ep], ep == 0);
                if (ep & 1) smf_v(am, ka[ep >> 1], cat4(qx[ep - 1], qx[ep]), idx);
            }
            mf_done(am, a0, a1);
            float v[10];
            constexpr float SC = 0.07216878364870322f, L2E = 1.4426950408889634f;
#pragma unroll
            for (int r = 0; r < 4; ++r) {
                v[r] = (4 * g + r <= 16 * Tq + n) ? a0[r] * SC : -1e9f;
                v[4 + r] = (16 + 4 * g + r <= 16 * Tq + n) ? a1[r] * SC : -1e9f;
            }
            v[8] = (am[0] + am[1]) * SC; v[9] = (am[2] + am[3]) * SC;
            float mx = v[0];
#pragma unroll
            for (int r = 1; r < 10; ++r) mx = fmaxf(mx, v[r]);
            mx = x16(mx, true); mx = x32(mx, true);
            float sum = 0.f;
#pragma unroll
            for (int r = 0; r < 10; ++r) { v[r] = __builtin_amdgcn_exp2f((v[r] - mx) * L2E); sum += v[r]; }
            sum = x16(sum, false); sum = x32(sum, false);
            const float inv = __builtin_amdgcn_rcpf(sum);
            u32x4 ox = {pka(v[0] * inv, v[1] * inv), pka(v[2] * inv, v[3] * inv), pka(v[4] * inv, v[5] * inv), pka(v[6] * inv, v[7] * inv)};
            unsigned om = pka(v[8] * inv, v[9] * inv);
            asm volatile("s_barrier" : "+v"(ox), "+v"(om) :: "memory");
            if (Tq) st128(ox, rw, so1, rb); else st64(u32x2{ox.x, ox.y}, rw, so1, rb);
            st32(om, rw, so2, rb);
            if (s + 1 < NSEG) stage_q(s + 1);
            if (s + KARM < NSEG) { const unsigned ra = rec(par, s + KARM); if (Tq) st128(SENT4, rw, so1, ra); else st64(SENT2, rw, so1, ra); st32(sent1(), rw, so2, ra); }
        } else if (wave == 1) {
            have = min((unsigned)__builtin_amdgcn_readlane(qp, 0), (unsigned)__builtin_amdgcn_readlane(qp, 1));
            if (s + 2 < NSEG) stage_k(s + 2);
            { const u32x2 pl = read_notes(nf); lfail[2] = pl.x; lfail[3] = pl.y; }
            asm volatile("s_waitcnt lgkmcnt(0)\n\ts_barrier" ::: "memory");
        } else {
            if (s < NSEG - 1) {
                a0 = f32x4{zero1(), zero1(), zero1(), zero1()}; a1 = f32x4{zero1(), zero1(), zero1(), zero1()};
                asm volatile("s_nop 0" : "+v"(a0), "+v"(a1));
#pragma unroll
                for (int kb = 0; kb < 3; ++kb) {
                    smf_v(a0, qa[kb], cat4(kxf[4 * kb], kxf[4 * kb + 2]), idx);
                    smf_v(a1, qa[kb], cat4(kxf[4 * kb + 1], kxf[4 * kb + 3]), idx);
                }
#pragma unroll
                for (int kb = 0; kb < 3; ++kb) mf_vv(am, qa[kb], ka[kb], kb == 0);
                asm volatile("s_nop 4" : "+v"(a0), "+v"(a1));
                float v[6];
                v[0] = a0[0] + a0[1]; v[3] = a0[2] + a0[3]; v[1] = a1[0] + a1[1]; v[4] = a1[2] + a1[3];
                asm volatile("" : "+v"(am), "+v"(v[0]), "+v"(v[1]), "+v"(v[3]), "+v"(v[4]));
                asm volatile("v_add_f32_dpp %0, %3, %2 quad_perm:[1,1,3,3] row_mask:0xf bank_mask:0xf\n\tv_add_f32_dpp %1, %5, %4 quad_perm:[1,1,3,3] row_mask:0xf bank_mask:0xf\n\ts_nop 0\n\t"
                             "v_mov_b32_dpp %0, %0 quad_perm:[0,0,2,2] row_mask:0xf bank_mask:0xf\n\tv_mov_b32_dpp %1, %1 quad_perm:[0,0,2,2] row_mask:0xf bank_mask:0xf"
                             : "=&v"(v[2]), "=&v"(v[5]) : "v"(am[0]), "v"(am[1]), "v"(am[2]), "v"(am[3]));
                float ma = fmax3(v[0], v[1], v[2]), mb = fmax3(v[3], v[4], v[5]);
                row_max2(ma, mb);
                ma *= SCALE_L2E; mb *= SCALE_L2E;
#pragma unroll
                for (int r = 0; r < 6; ++r) v[r] = __builtin_amdgcn_exp2f(__builtin_fmaf(v[r], SCALE_L2E, r < 3 ? -ma : -mb));
                float sa = __builtin_fmaf(v[2], 0.5f, v[0] + v[1]), sb = __builtin_fmaf(v[5], 0.5f, v[3] + v[4]);
                row_sum2(sa, sb);
                sa = __builtin_amdgcn_rcpf(sa); sb = __builtin_amdgcn_rcpf(sb);
                unsigned p0 = pka(v[0] * sa, v[3] * sb), p1 = pka(v[1] * sa, v[4] * sb), p2 = pka(v[2] * sa, v[5] * sb);
                asm volatile("s_nop 0" : "+v"(p0), "+v"(p1), "+v"(p2));
                st64(u32x2{pair_cols(p0, psel), pair_cols(p1, psel)}, rw, so1, rb);
                st32(__builtin_amdgcn_perm(p2, __builtin_amdgcn_update_dpp(0u, p2, 0x4E, 0xf, 0xf, true), psel2), rw, so2, rb);
                if (lane == 0) st32(dur, rw, h * 128, rb + amap(R_NS));
                tn = ck.now();
                lfail[1] = tn;
                asm volatile("s_waitcnt lgkmcnt(0)\n\ts_barrier" ::: "memory");
                if (s > 0 && __builtin_amdgcn_readfirstlane(*lfail) != s) dur = track(dur, tn - tg, s);
                SETV(5, tn + ck.org); SETV(6, tg + dur + ck.org);
                if (s + KARM < NSEG) { const unsigned ra = rec(par, s + KARM); st64(SENT2, rw, so1, ra); st32(sent1(), rw, so2, ra); if (lane == 0) st32(sent1(), rw, h * 128, ra + amap(R_NS)); }
            } else asm volatile("s_barrier" ::: "memory");
        }
        tn = mem_time(__builtin_amdgcn_readfirstlane(lfail[2]), __builtin_amdgcn_readfirstlane(lfail[3]), __builtin_amdgcn_readfirstlane(lfail[1]));
        FLUSH(s);
    }
    vmwait<0>();
}

constexpr int L_WO = 81920;
template <class SV, class SO>
DEV void load_vo(const Params& P, int h, LAS char* lds, int wave, int lane, Spin& sp, SV&& sv, SO&& so_, int rot = 0) {
    auto krow = [&](int k) { int kk = k + 2 * rot; if (kk >= 20) kk -= 20; return (unsigned)kk * 32u * 12288u; };
    WSrc<3> wv; wv.init((const char*)P.wv + h * 384 + wave * 96, lane, 12288u);
    const unsigned lo = (unsigned)(uintptr_t)lds + L_WO + wave * 20480, lv = (unsigned)(uintptr_t)lds + wave * 20480;
#pragma unroll
    for (int k = 0; k < 6; ++k) wv.issue(krow(k), lv + k * 3072);
    WSrc<10> wo; wo.init((const char*)P.wo + (size_t)h * E_ * 1280 + wave * 320, lane, 1280u);
    wo.issue(0, lo); wo.issue(32 * 1280, lo + 10240);
    WRd<3, false> rv; rv.init(lds + wave * 20480, lane);
    WRd<10, false> ro; ro.init(lds + L_WO + wave * 20480, lane);
#pragma unroll
    for (int k = 0; k < 20; ++k) {
        if (k == 1) { MARK(3); } if (k == 6) { MARK(4); } if (k == 12) { MARK(5); }
        if (k == 6 || k == 12) {
            const int ko = k / 3 - 2;
            vmwait<18>();
#pragma unroll
            for (int j = 0; j < 2; ++j) {
                u32x4 fo[10];
#pragma unroll
                for (int t = 0; t < 10; ++t) fo[t] = ro.get(j * 10240, t);
#pragma unroll
                for (int t = 0; t < 10; ++t) so_(t, ko + j, fo[t]);
            }
            lgkm0();
            wo.issue((ko + 2) * 32 * 1280, lo); wo.issue((ko + 3) * 32 * 1280, lo + 10240);
        }
        vmwait_n(k < 15 ? 35 : k < 18 ? 20 + (19 - k) * 3 : (19 - k) * 3);
        u32x4 fr[3];
#pragma unroll
        for (int t = 0; t < 3; ++t) fr[t] = rv.get((k % 6) * 3072, t);
#pragma unroll
        for (int t = 0; t < 3; ++t) sv(t, k, fr[t]);
        if (k + 6 < 20) { lgkm0(); wv.issue(krow(k + 6), lv + (k % 6) * 3072); }
    }
    vmwait<0>();
    MARK(2); FLUSH(128);
    bar();
}

DEV void role_UV(const Params& P, unsigned char* ws, int h, LAS char* lds, int wave, int lane, Spin& sp) {
    const rsrc_t rw = mk_rsrc(ws, (unsigned)WS_END);
    const int rot = MemStream::rot_of(h + 5, wave);
    u32x8 wv[3][10], wo[10][2];
    load_vo(P, h, lds, wave, lane, sp,
        [&](int t, int k, u32x4 f) { if (k & 1) { wv[t][k >> 1] = cat4(lo4(wv[t][k >> 1]), f); pin_a(wv[t][k >> 1]); } else wv[t][k >> 1] = cat4(f, f); },
        [&](int t, int k, u32x4 f) { if (k & 1) { wo[t][k >> 1] = cat4(lo4(wo[t][k >> 1]), f); if (k == 1 && t < 2) pin_a(wo[t][k >> 1]); } else wo[t][k >> 1] = cat4(f, f); }, rot);
    const unsigned par = load_par(rw, lane, sp);
    const int idx = (lane & 1) ? 0xEEEE : 0x4444;
    const int g = lane >> 4, n = lane & 15;
    MemStream ms; ms.init(rw, lds, lane, rot, wave == 0);
    volatile LAS unsigned* lfail = (volatile LAS unsigned*)(lds + 14336);
    if (wave == 0) *lfail = ~0u;
    bar();
    AFrag af; af.init(lds + 8192, lane);
    WRd<10, false> wr; wr.init(lds + L_WO + wave * 20480, lane);
    const unsigned uo = amap(R_UB) + (wave * 10 + g) * 16384 + h * 256 + n * 16;
    const unsigned uo2 = amap(R_UB) + (wave * 10 + 8 + (g & 1)) * 16384 + h * 256 + n * 16 + (g >> 1) * 8;
    LAS char* vw0 = opq(lds + 8192 + wave * 768 + AFrag::wofs(lane, wave & 1));
    LAS char* vw1 = opq(lds + 8192 + wave * 768 + AFrag::wofs(lane, ~wave & 1));
    auto lwq = [&](int t, int q) { return trd(wr.at((q >> 1) * 10240 + (q & 1) * 2560, t)); };
    Clock ck; ck.org = sp.org;
    unsigned dur = 140, tn = 0;
    for (int s = 0; s < NSEG; ++s) {
        const unsigned rb = rec(par, s);
        f32x4 acc[3], c[10];
        u32x2 st[10][4]; u32x4 a[10];
#pragma unroll
        for (int i = 0; i < 12; ++i) st[i >> 2][i & 3] = lwq(i >> 2, i & 3);
#pragma unroll
        for (int t = 0; t < 3; ++t) zero_acc(acc[t]);
#pragma unroll
        for (int t = 0; t < 4; ++t) zero_acc(c[t]);
        MARK(0); SETV(7, 0);
        const unsigned o0 = ms.so(rb, 0), o1 = ms.so(rb, 1), o2 = ms.so(rb, 2), o3 = ms.so(rb, 3);
        unsigned tg = ck.wait_until(tn);
        ms.lda(a[0], o0); ms.lda(a[1], o1); ms.lda(a[2], o2); ms.lda(a[3], o3);
        ms.share(o0, 0); ms.share(o1, 1); ms.share(o2, 2);
        if (tg - tn < 10u) tg = tn;
        SETV(1, tg + ck.org);
        unsigned long long bad = 0, bad2 = 0;
        auto slot = [&](auto I) __attribute__((always_inline)) {
            constexpr int i = decltype(I)::value % 64;
            constexpr bool fast = decltype(I)::value < 64;
            constexpr int kb = i < 4 ? i : i < 12 ? (i - 4) / 2 : i / 3, t = i < 4 ? 0 : i < 12 ? 1 + (i & 1) : i % 3;
            if constexpr (fast && i < 4) vmw<6 - i>(a[i]);
            if constexpr (i == 10) {
                asm volatile("s_waitcnt vmcnt(0)\n\ts_barrier" ::: "memory");
#pragma unroll
                for (int k = 4; k < 7; ++k) a[k] = ms.get(k);
            }
            smf_a(acc[t], a[kb], wv[t][kb], idx);
            if constexpr (i == 0) { MARK2(2); }
            if constexpr (i == 7) { asm volatile("s_nop 3"); bad = nan2(acc[0][0], acc[0][1]); bad2 = nan2(acc[0][2], acc[0][3]); }
            if constexpr (i >= 10 && i < 13) a[i - 3] = ms.get(i - 3);
            if constexpr (i >= 14 && i < 26) { constexpr int q = i - 14; c[4 + q / 4][q & 3] = zero1(); }
        };
        sfor<0, 10>(slot);
        if (__builtin_expect((bad | bad2) != 0, 0)) {
            asm volatile("s_nop 7\n\ts_waitcnt vmcnt(0)" ::: "memory");
            *lfail = (unsigned)s; SETV(7, 1);
            for (unsigned c = 0;;) {
#pragma unroll
                for (int k = 0; k < 4; ++k) a[k] = ms.ld(rb, k);
                if (MemStream::bad(a[0], a[1], a[2], a[3]) == 0 || sp.fail(c, 0x100 + s)) break;
            }
            ms.share(o0, 0); ms.share(o1, 1); ms.share(o2, 2);
#pragma unroll
            for (int t = 0; t < 3; ++t) zero_acc(acc[t]);
            sfor<64, 74>(slot);
            mf_done(acc[0], acc[1], acc[2]);
        }
        sfor<10, 30>(slot);
        asm volatile("s_nop 7" : "+v"(acc[0]), "+v"(acc[1]), "+v"(acc[2]), "+v"(c[4]), "+v"(c[5]), "+v"(c[6]));
#pragma unroll
        for (int t = 0; t < 3; ++t) { LAS char* p = t == 1 ? vw1 : vw0; float f0, f1; st2x16(p, p + 4, t * 256, fold_pk(acc[t], f0, f1)); }
#pragma unroll
        for (int t = 7; t < 10; ++t) { c[t] = f32x4{zero1(), zero1(), zero1(), zero1()}; pin_v(c[t]); }
        MARK2(3);
        bar();
        MARK2(4);
        u32x4 va[3];
#pragma unroll
        for (int kb = 0; kb < 3; ++kb) va[kb] = af.get(kb * 1024);
        unsigned o[10];
        sfor<0, 36>([&](auto I) __attribute__((always_inline)) {
            constexpr int i = decltype(I)::value, tp = i / 6, k = (i % 6) >> 1, t = 2 * tp + (i & 1);
            if constexpr (i < 30) {
                if constexpr (k == 0) { if constexpr (t < 2) smf_a(c[t], va[0], wo[t][0], idx); else smf_v(c[t], va[0], wo[t][0], idx); }
                else if constexpr (k == 1) smf_v(c[t], va[1], wo[t][1], idx);
                else smf_v(c[t], va[2], cat4(cat2(st[t][0], st[t][1]), cat2(st[t][2], st[t][3])), idx);
                constexpr int q0 = i < 3 ? 0 : ((i - 3) * 4) / 3, q1 = i < 3 ? 0 : ((i - 2) * 4) / 3;
                sfor<q0, q1>([&](auto Q) __attribute__((always_inline)) { constexpr int q = 12 + decltype(Q)::value; if constexpr (q < 40) st[q >> 2][q & 3] = lwq(q >> 2, q & 3); });
            }
            if constexpr (i == 30) asm volatile("s_nop 3" : "+v"(c[8]), "+v"(c[9]));
            if constexpr (i >= 10 && (i - 10) % 6 < 2) {
                constexpr int tf = 2 * ((i - 10) / 6) + (i - 10) % 6;
                float f0, f1;
                o[tf] = nosent(fold_pk(c[tf], f0, f1)); pin_v(o[tf]);
            }
            if constexpr (i == 19 || i == 31) {
                constexpr int q = i == 19 ? 0 : 4;
                auto r02 = __builtin_amdgcn_permlane32_swap(o[q], o[q + 2], false, false);
                auto r13 = __builtin_amdgcn_permlane32_swap(o[q + 1], o[q + 3], false, false);
                o[q] = r02[0]; o[q + 2] = r02[1]; o[q + 1] = r13[0]; o[q + 3] = r13[1];
                asm volatile("" : "+v"(o[q]), "+v"(o[q + 1]), "+v"(o[q + 2]), "+v"(o[q + 3]));
            }
            if constexpr (i == 20 || i == 32) {
                constexpr int q = i == 20 ? 0 : 4;
                auto r01 = __builtin_amdgcn_permlane16_swap(o[q], o[q + 1], false, false);
                auto r23 = __builtin_amdgcn_permlane16_swap(o[q + 2], o[q + 3], false, false);
                st128(u32x4{r01[0], r01[1], r23[0], r23[1]}, rw, uo, rb + q * 16384);
            }
        });
        { auto r = __builtin_amdgcn_permlane16_swap(o[8], o[9], false, false); st64(u32x2{r[0], r[1]}, rw, uo2, rb); }
        if (wave == 0 && lane == 0) st32(dur, rw, (32 + h) * 128, rb + amap(R_NS));
        tn = ck.now();
        if (s > 0 && __builtin_amdgcn_readfirstlane(*lfail) != s) dur = track(dur, tn - tg, s);
        SETV(5, tn + ck.org);
        if (wave == 0) { const u32x2 pl = read_notes(ld64(rw, ms.vn, rb + amap(R_NF))); lfail[1] = mem_time(pl.x, pl.y, tn); }
        bar();
        tn = __builtin_amdgcn_readfirstlane(lfail[1]);
        MARKC(6);
        if (s + KARM < NSEG) {
            const unsigned ra = rec(par, s + KARM);
            st128(SENT4, rw, uo, ra); st128(SENT4, rw, uo, ra + 65536); st64(SENT2, rw, uo2, ra);
            if (wave == 0 && lane == 0) st32(sent1(), rw, (32 + h) * 128, ra + amap(R_NS));
        }
        FLUSH(s);
    }
}

DEV void role_VC(const Params& P, unsigned char* ws, int h, int odd, LAS char* lds, int wave, int lane, Spin& sp) {
    constexpr int L_V = 40960;
    const rsrc_t rw = mk_rsrc(ws, (unsigned)WS_END);
    u32x4 wv[3][20], wo[10][4];
    load_vo(P, h, lds, wave, lane, sp, [&](int t, int k, u32x4 f) { wv[t][k] = f; pin_a(wv[t][k]); }, [&](int t, int k, u32x4 f) { wo[t][k] = f; if (t < 2 && k < 2) pin_a(wo[t][k]); });
    const int g = lane >> 4, n = lane & 15;
    const char* xf = (const char*)ws + O_XF;
    WRd<10, false> wr; wr.init(lds + L_WO + wave * 20480, lane);
    LAS char* l16 = opq(lds + lane * 16);
    auto issue_x = [&](int s) {
#pragma unroll
        for (int i = 0; i < 10; ++i) glds<0>(lane * 16, xf + (size_t)s * 40960 + (wave * 10 + i) * 1024, (unsigned)(uintptr_t)lds + (wave * 10 + i) * 1024);
    };
    const unsigned par = load_par(rw, lane, sp);
    issue_x(odd);
    unsigned seen = 0;
    for (int s = odd; s < NSEG; s += 2) {
        MARK(0);
        vmwait<0>();
        bar();
        if (s >= 2 && wave == 0 && lane == 0) st32((unsigned)(s - 1), rw, (unsigned)(O_CTRL + 4 * (W_VCPROG + 32 * odd + h)), 0);
        MARK(1);
        f32x4 acc[3][2];
        u32x4 xb[40];
#pragma unroll
        for (int i = 0; i < 4; ++i) xb[i] = lds128(l16 + (i & 1) * 20480 + (i >> 1) * 1024);
#pragma unroll
        for (int i = 0; i < 40; ++i) {
#pragma unroll
            for (int t = 0; t < 3; ++t) {
                mf_av(acc[t][i & 1], wv[t][i >> 1], xb[i], i < 2);
                if (t == 0 && i + 4 < 40) xb[i + 4] = lds128(l16 + (i & 1) * 20480 + ((i + 4) >> 1) * 1024);
            }
        }
        mf_done(acc[0][0], acc[1][0], acc[2][0]); mf_done(acc[0][1], acc[1][1], acc[2][1]);
#pragma unroll
        for (int t = 0; t < 3; ++t)
#pragma unroll
            for (int T = 0; T < 2; ++T) {
                const int et = wave * 3 + t;
                const int off = (et >> 1) * 2048 + T * 1024 + ((2 * (et & 1) + (g >> 1)) * 16 + n) * 16 + (g & 1) * 8;
                *(LAS u32x2*)(lds + L_V + off) = u32x2{pk(acc[t][T][0], acc[t][T][1]), pk(acc[t][T][2], acc[t][T][3])};
            }
        MARK(2);
        wait_slot(rw, s, seen, h & 7, sp);
        bar();
        MARK(3);
        u32x4 va[6][2], st[10][2];
        f32x4 c[10][2];
#pragma unroll
        for (int k = 0; k < 6; ++k)
#pragma unroll
            for (int T = 0; T < 2; ++T) va[k][T] = lds128(l16 + L_V + k * 2048 + T * 1024);
        st[0][0] = wr.get(0, 0); st[0][1] = wr.get(10240, 0);
        const char* xn = xf + (size_t)(s + 2 < NSEG ? s + 2 : s) * 40960 + wave * 10240;
        const unsigned xd = (unsigned)(uintptr_t)lds + wave * 10240;
        auto fin = [&](int t) {
            st128(u32x4{pk(c[t][0][0], c[t][0][1]), pk(c[t][0][2], c[t][0][3]), pk(c[t][1][0], c[t][1][1]), pk(c[t][1][2], c[t][1][3])}, rw,
                  (h >> 3) * 16384 + (h & 7) * 1024 + lane * 16, page(par, s, 2 + (wave >> 1)) + ((wave & 1) * 10 + t) * 65536);
        };
#pragma unroll
        for (int t = 0; t < 10; ++t) {
#pragma unroll
            for (int k = 0; k < 6; ++k) {
                const u32x4 b = k < 4 ? wo[t][k & 3] : st[t][k & 1];
                if (t < 2 && k < 2) { mf_va(c[t][0], va[k][0], b, k == 0); mf_va(c[t][1], va[k][1], b, k == 0); }
                else { mf_vv(c[t][0], va[k][0], b, k == 0); mf_vv(c[t][1], va[k][1], b, k == 0); }
                if (k < 2 && t + 1 < 10) st[t + 1][k] = wr.get(k * 10240, t + 1);
                if (k == 5 && t < 5) { glds<0>(lane * 16, xn + 2 * t * 1024, xd + 2 * t * 1024); }
                if (k == 2 && t < 5) { glds<1024>(lane * 16, xn + 2 * t * 1024, xd + 2 * t * 1024); }
                if (k == 3 && t >= 1) { pin_v(c[t - 1][0]); pin_v(c[t - 1][1]); fin(t - 1); }
            }
        }
        mf_done(c[9][0], c[9][1]);
        fin(9);
        MARK(4); FLUSH(s);
    }
    vmwait<0>();
    bar();
    if (wave == 0 && lane == 0) st32((unsigned)NSEG, rw, (unsigned)(O_CTRL + 4 * (W_VCPROG + 32 * odd + h)), 0);
}

DEV void role_QK(const Params& P, unsigned char* ws, int cu, LAS char* lds, int wave, int lane, Spin& sp) {
    const rsrc_t rw = mk_rsrc(ws, (unsigned)WS_END);
    u32x4 w[4][20];
    const int sel = cu >= 24, c0 = (cu - 24 * sel) * 256 + wave * 64;
    stream_w<4, 20, 20, 10, 0, true>((const char*)(sel ? P.wk : P.wq) + c0 * 2, 12288u, lds, wave * 40960, lane,
                                 [&](int t, int k, u32x4 f) { w[t][k] = f; if (k < 16) pin_a(w[t][k]); });
    vmwait<0>();
    bar();
    const char* xf = (const char*)ws + O_XF;
    auto issue_x = [&](int s) {
#pragma unroll
        for (int i = 0; i < 10; ++i) glds<0>(lane * 16, xf + (size_t)s * 40960 + (wave * 10 + i) * 1024, (unsigned)(uintptr_t)lds + (s & 1) * 40960 + (wave * 10 + i) * 1024);
    };
    const unsigned par = load_par(rw, lane, sp);
    issue_x(0);
    const unsigned ol = (sel ? R_KX : R_QX) + (c0 >> 5) * 2048, ob = ((ol >> 13) << 14) | (ol & 8191u);
    unsigned seen = 0;
    for (int s = 0; s < NSEG; ++s) {
        MARK(0);
        vmwait<0>();
        bar();
        MARK(1);
        if (s >= 1 && wave == 0 && lane == 0) st32((unsigned)s, rw, (unsigned)(O_CTRL + 4 * (W_QKPROG + 32 * cu)), 0);
        wait_slot(rw, s, seen, cu & 7, sp);
        LAS char* l16 = opq(lds + (s & 1) * 40960 + lane * 16);
        f32x4 acc[4][2];
        u32x4 xb[40];
#pragma unroll
        for (int i = 0; i < 4; ++i) xb[i] = lds128(l16 + (i & 1) * 20480 + (i >> 1) * 1024);
        const char* xn = xf + (size_t)(s + 1 < NSEG ? s + 1 : s) * 40960 + wave * 10240;
        const unsigned xd = (unsigned)(uintptr_t)lds + (~s & 1) * 40960 + wave * 10240;
#pragma unroll
        for (int i = 0; i < 40; ++i)
#pragma unroll
            for (int t = 0; t < 4; ++t) {
                if (t == 2 && i % 3 == 0 && i < 30) glds<0>(lane * 16, xn + (i / 3) * 1024, xd + (i / 3) * 1024);
                if (i < 32) mf_av(acc[t][i & 1], w[t][i >> 1], xb[i], i < 2); else mf_vv(acc[t][i & 1], w[t][i >> 1], xb[i], false);
                if (t == 0 && i + 4 < 40) xb[i + 4] = lds128(l16 + (i & 1) * 20480 + ((i + 4) >> 1) * 1024);
            }
        mf_done(acc[0][0], acc[1][0], acc[2][0], acc[3][0]); mf_done(acc[0][1], acc[1][1], acc[2][1], acc[3][1]);
#pragma unroll
        for (int pr = 0; pr < 2; ++pr)
#pragma unroll
            for (int T = 0; T < 2; ++T) {
                const f32x4 a = acc[2 * pr][T], b = acc[2 * pr + 1][T];
                st128(u32x4{pk(a[0], a[1]), pk(a[2], a[3]), pk(b[0], b[1]), pk(b[2], b[3])}, rw, pr * 2048 + T * 1024 + lane * 16, page(par, s, 1) + ob);
            }
        MARK(2); MARKC(6); FLUSH(s);
    }
    vmwait<0>();
    bar();
    if (wave == 0 && lane == 0) st32((unsigned)NSEG, rw, (unsigned)(O_CTRL + 4 * (W_QKPROG + 32 * cu)), 0);
}

DEV void role_F(const Params& P, unsigned char* ws, int f, LAS char* lds, int wave, int lane, Spin& sp) {
    constexpr int L_FL = 131072, L_U = L_FL + 1024;
    const rsrc_t rw = mk_rsrc(ws, (unsigned)WS_END);
    volatile LAS unsigned* fl = (volatile LAS unsigned*)(lds + L_FL);
    if (wave == 0 && lane < 8) fl[lane] = 0u;
    bar();
    const unsigned par = load_par(rw, lane, sp);
    const int g = lane >> 4, n = lane & 15;
    const unsigned l16 = lane * 16;
    const unsigned xr = ((f % 10) * 3) & 15;
    auto lwait = [&](int i, int need, unsigned code) {
        for (unsigned c = 0, k = 0; (int)__builtin_amdgcn_readfirstlane(fl[i]) < need;) { if ((++k & 63u) == 0u && sp.fail(c, code)) break; }
        asm volatile("" ::: "memory");
    };
    auto lset = [&](int i, int v) { asm volatile("s_waitcnt lgkmcnt(0)" ::: "memory"); if (lane == 0) fl[i] = (unsigned)v; };
    if (wave == 3) {
        unsigned have0 = 0, have1 = 0;
        const unsigned vf = min(lane, 39) * 128;
        for (int s = 0; s < NSEG; ++s) {
            MARK(0);
            unsigned hv = (s & 1) ? have1 : have0;
            for (unsigned c = 0; hv <= (unsigned)s;) {
                const unsigned v = ld32(rw, (lane & 31) * 4, (unsigned)(O_CTRL + 4 * (W_VCPROG + 32 * (s & 1))));
                if (!any_lane(v <= (unsigned)s)) {
                    hv = ~wave_max(~v);
                    break;
                }
                if (sp.fail(c, 0x300 + s)) break;
                __builtin_amdgcn_s_sleep(2);
            }
            if (s & 1) have1 = hv; else have0 = hv;
            lwait(2, s - 3, 0x341); lwait(3, s - 3, 0x342); lwait(1, s - 1, 0x340);
            MARK(1);
            const unsigned char* src = ws + (page(par, s, f < 20 ? 2 : 3) + (f % 20) * 65536);
            const unsigned no = rec(par, s - 1) + amap(R_NF);
            u32x2 nf;
            __builtin_amdgcn_s_sleep(4);
            asm volatile("buffer_load_dwordx2 %0, %1, %2, %3 offen sc1" : "=v"(nf) : "v"(vf), "s"(rw), "s"(no));
#pragma unroll
            for (int i = 0; i < 16; ++i) glds_dev<2, 1024>(l16, src + (((i ^ xr) >> 2) * 16384 + ((i ^ xr) & 3) * 2048), (unsigned)(uintptr_t)lds + (s & 3) * 32768 + i * 2048);
            asm volatile("s_waitcnt vmcnt(32)" : "+v"(nf));
            if (s >= 2) {
                for (unsigned c = 0; any_lane(max(nf.x, nf.y) == SENT);) { if (sp.fail(c, 0x390)) break; nf = ld64(rw, vf, no); }
                const u32x2 pl = read_notes(nf);
                if (lane == 0) { fl[6] = pl.x; fl[7] = pl.y; }
                lset(5, s - 1);
            }
            vmwait<0>();
            lset(0, s + 1);
            MARK(2); FLUSH(s);
        }
    } else if (wave == 0) {
        const int idx = (lane & 1) ? 0xEEEE : 0x4444;
        const unsigned pmo = g * 256 + n * 8;
        const unsigned mo = amap(R_MEM + f * 256) + (2 * (2 * g + (n & 1)) + ((n >> 1) & 1)) * 16 + (n >> 2) * 4;
        const unsigned psel = (n & 1) ? 0x07060302u : 0x01000504u;
        unsigned pv[4], ps[4];
#pragma unroll
        for (int j = 0; j < 4; ++j) { pv[j] = l16 + ((j ^ xr) & 3) * 1024; const unsigned k = j ^ (xr >> 2); ps[j] = amap(R_P) + (k & 1) * 4096 + (k >> 1) * 16384; }
        Clock ck; ck.org = sp.org;
        unsigned dur = 75, tp = 0, plan = 0, dS = 150, dU = 140;
        unsigned boost = 0;
        auto use_notes = [&](int s) __attribute__((always_inline)) {
            if (__builtin_amdgcn_readfirstlane(fl[5]) == s && s > 0) { tp = __builtin_amdgcn_readfirstlane(fl[6]); plan = __builtin_amdgcn_readfirstlane(fl[7]); }
        };
        __builtin_amdgcn_s_setprio(3);
        for (int s = 0; s < NSEG - 1; ++s) {
            MARK(0);
            lwait(0, s + 1, 0x343);
            LAS char* cb = opq(lds + (s & 3) * 32768 + l16);
            u32x8 bc[16];
#pragma unroll
            for (int t = 0; t < 16; ++t) bc[t] = cat4(lds128(cb + 2 * t * 1024), lds128(cb + (2 * t + 1) * 1024));
#pragma unroll
            for (int t = 0; t < 16; ++t) pin_a(bc[t]);
            const unsigned rb = rec(par, s), rn = rec(par, s + 1), ub = rb + amap(R_UB) + f * 16384, pb = rb + amap(R_P);
            ck.wait_until(tp - (max(dS, dU + 8u) - dU) - 12u);
            use_notes(s);
            const unsigned tu = tp - (max(dS, dU + 8u) - dU);
            unsigned ns;
            float r0, r1;
            u32x4 u[8], px[16]; u32x2 pm[8];
            f32x4 d; zero_acc(d);
            MARK(1);
            ck.wait_until(tu);
#pragma unroll
            for (int i = 0; i < 8; ++i) u[i] = ld128(rw, l16 + (i & 3) * 1024, ub + (i >> 2) * 4096);
            const unsigned tg = ck.wait_until(tp);
            SETV(3, tg + ck.org); SETV(4, tp + ck.org); SETV(5, plan - DELTA_B + ck.org); SETV(6, tu + ck.org);
            bool failed = false;
            for (unsigned c = 0;;) {
#pragma unroll
                for (int i = 0; i < 16; ++i) px[i] = ld128(rw, pv[i & 3], rb + ps[i >> 2]);
#pragma unroll
                for (int i = 0; i < 8; ++i) pm[i] = ld64(rw, pmo + (i >> 1) * 1024 + (i & 1) * 128, pb + 32768);
                ns = ld32(rw, lane * 128, rb + amap(R_NS));
#pragma unroll
                for (int i = 0; i < 16; ++i) smf_a(d, px[i], bc[i], idx);
#pragma unroll
                for (int i = 0; i < 4; ++i) smf_v(d, cat2(pm[2 * i], pm[2 * i + 1]), cat4(u[2 * i], u[2 * i + 1]), idx);
                mf_done(d);
                r0 = d[0] + d[1]; r1 = d[2] + d[3];
                if (__builtin_expect(!any_lane(__builtin_isunordered(r0, r1)), 1)) { SETV(7, c); break; }
                if (c > 0) {
                    unsigned sg = 0; bool miss = false;
#pragma unroll
                    for (int i = 0; i < 8; ++i) miss |= mx4(u[i]) == SENT;
#pragma unroll
                    for (int i = 0; i < 16; ++i) sg |= px[i].x | px[i].y | px[i].z | px[i].w;
#pragma unroll
                    for (int i = 0; i < 8; ++i) sg |= pm[i].x | pm[i].y;
                    if (!any_lane(miss | ((sg & 0x80008000u) != 0u))) break;
                }
                if (sp.fail(c, 0x400 + s)) break;
                failed = true;
                zero_acc(d);
#pragma unroll
                for (int i = 0; i < 8; ++i) u[i] = ld128(rw, l16 + (i & 3) * 1024, ub + (i >> 2) * 4096);
            }
            st32(nosent(pair_cols(pk(r0, r1), psel)), rw, mo, rn);
            const unsigned n5 = fl[5], n7 = fl[7];
            const unsigned tpub = ck.now();
            if (__builtin_amdgcn_readfirstlane(n5) == s) plan = __builtin_amdgcn_readfirstlane(n7);
            const unsigned prop = (tpub + VIS <= plan ? plan : tpub + LATE) + max(dS, dU + 8u) + DELTA_A + (failed ? boost : 0u);
            boost = failed ? min(boost + 40u, 400u) : 0u;
            if (lane == 0) { st64(u32x2{prop, dur}, rw, f * 128, rn + amap(R_NF)); fl[1] = (unsigned)(s + 1); }
            if (!failed && tg - tp < 15u) dur = track(dur, tpub - tp, s);
            SETV(2, tpub + ck.org);
            if (!any_lane(ns == SENT)) { const unsigned m = half_max(min(ns, 400u)); dS = __builtin_amdgcn_readlane(m, 0); dU = __builtin_amdgcn_readlane(m, 32); }
            lwait(2, s - 1, 0x346); lwait(3, s - 1, 0x347);
#pragma unroll
            for (int i = 0; i < 8; ++i) *(LAS u32x4*)(lds + L_U + (s & 1) * 8192 + i * 1024 + l16) = u[i];
            lset(4, s + 1);
            tp = prop; plan = prop + dur + DELTA_B;
            if (f == 0 && lane < 8) st32((unsigned)(s + 1), rw, lane * 128, (unsigned)(O_CTRL + 4 * W_PROG));
            if (s + KARM < NSEG) { const unsigned ra = rec(par, s + KARM); st32(sent1(), rw, mo, ra); if (lane == 0) st64(SENT2, rw, f * 128, ra + amap(R_NF)); }
            FLUSH(s);
        }
        {
            const unsigned ub = rec(par, NSEG - 1) + amap(R_UB) + f * 16384;
            u32x4 u[8];
            ck.wait_until(tp);
            lset(1, NSEG);
            for (unsigned c = 0;;) {
                unsigned m = 0;
#pragma unroll
                for (int i = 0; i < 8; ++i) u[i] = ld128(rw, l16 + (i & 3) * 1024, ub + (i >> 2) * 4096);
#pragma unroll
                for (int i = 0; i < 8; ++i) m = max(m, mx4(u[i]));
                if (!any_lane(m == SENT) || sp.fail(c, 0x4FF)) break;
            }
            lwait(2, NSEG - 2, 0x346); lwait(3, NSEG - 2, 0x347);
#pragma unroll
            for (int i = 0; i < 8; ++i) *(LAS u32x4*)(lds + L_U + ((NSEG - 1) & 1) * 8192 + i * 1024 + l16) = u[i];
            lset(4, NSEG);
        }
    } else {
        auto xrows = [&](auto TQ) __attribute__((always_inline)) {
            constexpr int Tq = decltype(TQ)::value, NX = Tq ? 32 : 16;
            const rsrc_t rx = mk_rsrc(P.x, S_ * D_ * 2);
            const unsigned ho = (f >> 1) * 1024 + ((2 * (f & 1) + (g >> 1)) * 16 + n) * 16 + (g & 1) * 8;
            for (int s = 0; s < NSEG; ++s) {
                MARK(0);
                lwait(0, s + 1, 0x345);
                LAS char* cb = opq(lds + (s & 3) * 32768 + l16);
                u32x4 ca[NX];
#pragma unroll
                for (int i = 0; i < NX; ++i) ca[i] = Tq ? lds128(cb + i * 1024) : cat2(*(LAS const u32x2*)(cb + i * 2048), *(LAS const u32x2*)(cb + i * 2048 + 1024));
#pragma unroll
                for (int i = 0; i < NX; ++i) pin_a(ca[i]);
                const unsigned pb = rec(par, s) + amap(R_PX) + Tq * amap(PX_B);
                lwait(1, s + 1, 0x344);
                MARK2(1);
                f32x4 d; u32x2 xv;
                for (unsigned c = 0;;) {
                    u32x4 u[8], px[NX + 8];
#pragma unroll
                    for (int i = 0; i < NX; ++i) {
                        if (Tq) { const unsigned k = (i >> 1) ^ xr; px[i] = ld128(rw, l16 + (i & 1) * 1024, pb + (k & 3) * 2048 + (k >> 2) * 16384); }
                        else { const unsigned k = i ^ xr; px[i] = ld128(rw, l16, pb + (k & 7) * 1024 + (k >> 3) * 16384); }
                    }
#pragma unroll
                    for (int i = 0; i < 8; ++i) px[NX + i] = ld128(rw, l16 + (i & 3) * 1024, pb + 65536 + (i >> 2) * 4096);
                    xv = __builtin_amdgcn_raw_buffer_load_b64(rx, ((32 * s + 16 * Tq + n) * D_ + 16 * f + 4 * g) * 2, 0, 0);
                    lwait(4, s + 1, 0x348);
#pragma unroll
                    for (int i = 0; i < 8; ++i) u[i] = lds128(lds + L_U + (s & 1) * 8192 + i * 1024 + l16);
#pragma unroll
                    for (int i = 0; i < NX; ++i) mf_av(d, ca[i], px[i], i == 0);
#pragma unroll
                    for (int i = 0; i < 8; ++i) mf_vv(d, u[i], px[NX + i], false);
                    mf_done(d);
                    if (!any_lane(__builtin_isunordered(d[0] + d[1], d[2] + d[3]))) { SETV(7, c); break; }
                    bool miss = false;
#pragma unroll
                    for (int i = 0; i < NX + 8; ++i) miss |= mx4(px[i]) == SENT;
                    if (!any_lane(miss)) break;
                    if (sp.fail(c, 0x500 + s)) break;
                }
                MARK(2);
                lset(2 + Tq, s + 1);
                const unsigned a01 = pk(d[0], d[1]), a23 = pk(d[2], d[3]);
                st64(u32x2{pks(bflo(xv.x) + bflo(a01), bfhi(xv.x) + bfhi(a01)), pks(bflo(xv.y) + bflo(a23), bfhi(xv.y) + bfhi(a23))}, rw, ho, (unsigned)(O_HF + (size_t)(2 * s + Tq) * XF_B));
                if (2 * (s + KARM) < UTAIL) st64(SENT2, rw, ho, (unsigned)(O_HF + (size_t)(2 * (s + KARM) + Tq) * XF_B));
                MARK(3); FLUSH(s);
            }
        };
        if (wave == 1) xrows(IC<0>{}); else xrows(IC<1>{});
    }
}

DEV float gelu_t(float x) {
    const float t = x * __builtin_fmaf(x * x, -0.10294324f, -2.30220820f);
    return x * __builtin_amdgcn_rcpf(1.f + __builtin_amdgcn_exp2f(t));
}

template <int NK> DEV bool any_sent(const u32x4 (&x)[NK]) {
    unsigned m = 0;
#pragma unroll
    for (int k = 0; k < NK; ++k) m = max(m, mx4(x[k]));
    return any_lane(m == SENT);
}
template <int U0> DEV int next_unit(int u) { return u < U0 ? u + 1 : u + 4; }
template <int MODE>
DEV void role_W(const Params& P, unsigned char* ws, int cb, int rep, LAS char* lds, int wave, int lane, Spin& sp) {
    constexpr int U0 = MODE == 1 ? U0_1 : MODE == 2 ? U0_2 : U0_3;
    const rsrc_t rw = mk_rsrc(ws, (unsigned)WS_END);
    constexpr unsigned stride = MODE == 3 ? F_ * 2 : D_ * 2;
    const char* wsrc = (const char*)(MODE == 1 ? P.w1 : MODE == 2 ? P.w2 : P.w3) + (size_t)wave * 160 * stride + cb * 256;
    u32x4 w[8][5];
    stream_w<8, 5, 5, 5, 0, true>(wsrc, stride, lds, wave * 40960, lane, [&](int t, int k, u32x4 f) { w[t][k] = f; pin_a(w[t][k]); }, 0, 2 * wave + 2);
    vmwait<0>();
    bar();
    const int g = lane >> 4, n = lane & 15;
    const unsigned l16 = lane * 16;
    if (rep == 0) load_par<false>(rw, lane, sp);
    constexpr size_t O_IN = MODE == 1 ? O_HF : MODE == 2 ? O_GF : O_X1F;
    LAS char* wr[3];
#pragma unroll
    for (int j = 0; j < 3; ++j) wr[j] = opq(lds + ((wave + 1 + j) & 3) * 6144 + j * 2048 + l16);
    LAS char* rd = opq(lds + wave * 6144 + l16);
    LAS char* sw = opq(lds + 24576 + wave * 512 + lane * 8);
    LAS char* sr = opq(lds + 24576 + ((n >> 2) * 16 + n) * 8);
    u32x4 ones = {0x3F803F80u, 0x3F803F80u, 0x3F803F80u, 0x3F803F80u}; pin_v(ones);
    auto fetch1 = [&](int u, int k) __attribute__((always_inline)) { return ld128(rw, l16, (unsigned)(O_IN + (size_t)u * XF_B) + (wave * 5 + k) * 1024); };
    auto probe = [&](int u) __attribute__((always_inline)) { return ld32(rw, 1008, (unsigned)(O_IN + (size_t)u * XF_B) + (wave * 5 + 4) * 1024); };
    auto product = [&](const u32x4 (&xb)[5], auto&& side, int buf, f32x4& o0, f32x4& o1, float& S1, float& S2) __attribute__((always_inline)) {
        f32x4 acc[8], s1, s2;
        if (MODE != 2) {
#pragma unroll
            for (int k = 0; k < 5; ++k) { mf_vv(s1, ones, xb[k], k == 0); mf_vv(s2, xb[k], xb[k], k == 0); }
        }
        sfor<0, 40>([&](auto I) __attribute__((always_inline)) {
            constexpr int i = decltype(I)::value, t = 2 * (i / 10) + (i & 1), k = (i % 10) >> 1;
            mf_av(acc[t], w[t][k], xb[k], k == 0);
            if constexpr (i % 4 == 0) side(IC<i / 4>{});
            if constexpr (i >= 17 && (i % 10 == 7 || i % 10 == 8)) { constexpr int tw = 2 * (i / 10 - 1) + (i % 10 - 7); pin_v(acc[tw]); *(LAS f32x4*)(wr[tw >> 1] + buf * 28672 + (tw & 1) * 1024) = acc[tw]; }
            if constexpr (MODE != 2 && i == 20) {
                asm volatile("" : "+v"(s1), "+v"(s2));
                const float d = (n & 2) ? ((n & 1) ? s2[3] : s2[2]) : ((n & 1) ? s2[1] : s2[0]);
                *(LAS f32x2*)(sw + buf * 28672) = f32x2{s1[0], d};
            }
        });
        bar();
        f32x4 p[6]; f32x2 q[4];
#pragma unroll
        for (int j = 0; j < 6; ++j) p[j] = *(LAS const f32x4*)(rd + buf * 28672 + j * 1024);
        if (MODE != 2) {
#pragma unroll
            for (int j = 0; j < 4; ++j) q[j] = *(LAS const f32x2*)(sr + buf * 28672 + j * 512);
        }
        mf_done(acc[6], acc[7]);
        o0 = (acc[6] + p[0]) + (p[2] + p[4]); o1 = (acc[7] + p[1]) + (p[3] + p[5]);
        if (MODE != 2) { S1 = (q[0].x + q[1].x) + (q[2].x + q[3].x); S2 = (q[0].y + q[1].y) + (q[2].y + q[3].y); }
    };
    f32x4 cs0 = {}, cs1 = {};
    if (MODE != 2) {
        u32x4 o4[5] = {ones, ones, ones, ones, ones};
        asm volatile("s_nop 1" : "+v"(o4[0]), "+v"(o4[1]), "+v"(o4[2]), "+v"(o4[3]), "+v"(o4[4]));
        float a, b; product(o4, [](auto) {}, 1, cs0, cs1, a, b);
    }
    const int kso = 4 * cb + wave;
    const int grp = kso / 5, sh = grp == 0 ? 1 : grp == 1 ? -1 : grp == 2 ? 2 : -2;
    if (MODE == 1 && rep == 0) {
        const int td = sh > 0 ? n : S_ - 1 - n;
        if (n < (sh > 0 ? sh : -sh)) st128(u32x4{0u, 0u, 0u, 0u}, rw, (td >> 4) * (unsigned)XF_B + kso * 1024 + (g * 16 + (td & 15)) * 16, (unsigned)O_GF);
        vmwait<0>();
    }
    u32x4 xa[5], xc[5], hn; u32x2 sn;
    bool miss = true, ahead = false;
    auto extra = [&](int u) __attribute__((always_inline)) { if (MODE == 2) { hn = ld128(rw, l16, (unsigned)(O_HF + (size_t)u * XF_B) + kso * 1024); sn = ld64(rw, (16 * u + n) * 8, (unsigned)O_ST); } };
    auto unit = [&](int u, int buf, u32x4 (&xb)[5], u32x4 (&nx)[5]) __attribute__((always_inline)) {
        MARK(0);
        const int tok = 16 * u + n, un = next_unit<U0>(u), u2 = next_unit<U0>(un);
        for (unsigned c = 0; miss;) {
            while (__builtin_amdgcn_readfirstlane(probe(u)) == SENT) { if (sp.fail(c, 0x600 + MODE * 0x100 + u)) break; __builtin_amdgcn_s_sleep(PSLEEP); }
            __builtin_amdgcn_s_sleep(PWAIT);
#pragma unroll
            for (int k = 0; k < 5; ++k) xb[k] = fetch1(u, k);
            extra(u);
            miss = any_sent(xb);
            if (miss && sp.fail(c, 0x680 + u)) break;
        }
        MARK(1);
        asm volatile("s_nop 1" : "+v"(xb[0]), "+v"(xb[1]), "+v"(xb[2]), "+v"(xb[3]), "+v"(xb[4]));
        const bool early = ahead;
        unsigned pr = SENT;
        if ((early ? u2 : un) < NUNIT) pr = probe(early ? u2 : un);
        u32x4 hv = hn; u32x2 stt = sn;
        if (MODE == 2) asm volatile("" : "+v"(hv), "+v"(stt));
        f32x4 o0, o1; float S1, S2;
        product(xb, [&](auto J) __attribute__((always_inline)) {
            constexpr int j = decltype(J)::value;
            if constexpr (j < 5) { if (early) nx[j] = fetch1(un, j); }
            if constexpr (j == 5) { if (early) extra(un); }
        }, buf, o0, o1, S1, S2);
        if (!early) {
            ahead = __builtin_amdgcn_readfirstlane(pr) != SENT;
            if (ahead) {
#pragma unroll
                for (int k = 0; k < 5; ++k) nx[k] = fetch1(un, k);
                extra(un);
                pr = SENT;
                if (u2 < NUNIT) pr = probe(u2);
            }
        }
        auto settle = [&]() __attribute__((always_inline)) { __builtin_amdgcn_s_waitcnt(0x0F70); miss = !ahead || any_sent(nx); ahead = ahead && __builtin_amdgcn_readfirstlane(pr) != SENT; if (MODE == 2) asm volatile("" : "+v"(hn), "+v"(sn)); };
        MARK(2);
        if (MODE != 2) {
            const float mu = S1 * (1.f / D_), var = fmaxf(S2 * (1.f / D_) - mu * mu, 0.f);
            float al = __builtin_amdgcn_rsqf(var + 1e-5f);
            if (MODE == 1) {
                if (cb == 0 && wave == 0 && g == 0) st64(u32x2{nosent(f2u(mu)), nosent(f2u(al))}, rw, tok * 8, (unsigned)O_ST);
                al *= __builtin_amdgcn_rsqf(var * al * al + 1e-5f);
            }
            float y[8];
#pragma unroll
            for (int r = 0; r < 4; ++r) { y[r] = gelu_t(al * (o0[r] - mu * cs0[r])); y[4 + r] = gelu_t(al * (o1[r] - mu * cs1[r])); }
            u32x4 o = {pks(y[0], y[1]), pks(y[2], y[3]), pks(y[4], y[5]), pks(y[6], y[7])};
            pin_v(o);
            settle();
            if (MODE == 1) {
                const int td = tok + sh;
                const unsigned vo = (td >> 4) * (int)XF_B + kso * 1024 + (g * 16 + (td & 15)) * 16;
                if (td >= 0 && td < S_) st128(o, rw, vo, (unsigned)O_GF);
                if (td + 32 * KARM < 16 * UTAIL) st128(SENT4, rw, vo + 2 * KARM * (unsigned)XF_B, (unsigned)O_GF);
            } else {
                st128(o, rw, l16, (unsigned)(O_G3F + (size_t)u * G3_B) + kso * 1024);
                if (u + 2 * KARM < UTAIL) st128(SENT4, rw, l16, (unsigned)(O_G3F + (size_t)(u + 2 * KARM) * G3_B) + kso * 1024);
            }
        } else {
            for (unsigned c = 0; any_lane(max(mx4(hv), max(stt.x, stt.y)) == SENT);) {
                if (sp.fail(c, 0xA00 + u)) break;
                hv = ld128(rw, l16, (unsigned)(O_HF + (size_t)u * XF_B) + kso * 1024); stt = ld64(rw, tok * 8, (unsigned)O_ST);
            }
            const float mu = u2f(stt.x), al = u2f(stt.y);
            const u32x4 o = {pks((bflo(hv.x) - mu) * al + o0[0], (bfhi(hv.x) - mu) * al + o0[1]), pks((bflo(hv.y) - mu) * al + o0[2], (bfhi(hv.y) - mu) * al + o0[3]),
                             pks((bflo(hv.z) - mu) * al + o1[0], (bfhi(hv.z) - mu) * al + o1[1]), pks((bflo(hv.w) - mu) * al + o1[2], (bfhi(hv.w) - mu) * al + o1[3])};
            settle();
            st128(o, rw, l16, (unsigned)(O_X1F + (size_t)u * XF_B) + kso * 1024);
            if (u + 2 * KARM < UTAIL) st128(SENT4, rw, l16, (unsigned)(O_X1F + (size_t)(u + 2 * KARM) * XF_B) + kso * 1024);
        }
        MARK(3); FLUSH(u);
    };
    for (int u = rep ? U0 + rep : 0;;) {
        unit(u, 0, xa, xc); u = next_unit<U0>(u); if (u >= NUNIT) break;
        unit(u, 1, xc, xa); u = next_unit<U0>(u); if (u >= NUNIT) break;
    }
}

DEV void role_W4(const Params& P, unsigned char* ws, int cb, int rep, LAS char* lds, int wave, int lane, Spin& sp) {
    constexpr int U0 = U0_4;
    const rsrc_t rw = mk_rsrc(ws, (unsigned)WS_END);
    const rsrc_t ro = mk_rsrc(P.out, S_ * D_ * 2);
    u32x4 w[4][12];
    stream_w<4, 12, 12, 10, 0, false>((const char*)P.w4 + (size_t)wave * 384 * 1280 + cb * 128, 1280u, lds, wave * 40960, lane,
                                  [&](int t, int k, u32x4 f) { w[t][k] = f; pin_a(w[t][k]); }, 0, wave + 1);
    vmwait<0>();
    bar();
    const int g = lane >> 4, n = lane & 15;
    const unsigned l16 = lane * 16;
    if (rep == 0) load_par<false>(rw, lane, sp);
    const int ct = 4 * cb + wave;
    const unsigned po = (ct >> 1) * 1024 + ((2 * (ct & 1) + (g >> 1)) * 16 + n) * 16 + (g & 1) * 8;
    LAS char* wr[3];
#pragma unroll
    for (int j = 0; j < 3; ++j) wr[j] = opq(lds + ((wave + 1 + j) & 3) * 3072 + j * 1024 + l16);
    LAS char* rd = opq(lds + wave * 3072 + l16);
    auto fetch = [&](u32x4 (&x)[12], int u) __attribute__((always_inline)) {
#pragma unroll
        for (int k = 0; k < 12; ++k) x[k] = ld128(rw, l16 + (k & 3) * 1024, (unsigned)(O_G3F + (size_t)u * G3_B) + (wave * 12 + (k & ~3)) * 1024);
    };
    auto probe = [&](int u) __attribute__((always_inline)) { return ld32(rw, 1008, (unsigned)(O_G3F + (size_t)u * G3_B) + (wave * 12 + 11) * 1024); };
    u32x4 xa[12], xc[12]; u32x2 hn, xn;
    bool miss = true, ahead = false;
    auto extra = [&](int u) __attribute__((always_inline)) { hn = ld64(rw, po, (unsigned)(O_HF + (size_t)u * XF_B)); xn = ld64(rw, po, (unsigned)(O_X1F + (size_t)u * XF_B)); };
    auto unit = [&](int u, int buf, u32x4 (&xb)[12], u32x4 (&nx)[12]) __attribute__((always_inline)) {
        MARK(0);
        for (unsigned c = 0; miss;) {
            while (__builtin_amdgcn_readfirstlane(probe(u)) == SENT) { if (sp.fail(c, 0xB00 + u)) break; __builtin_amdgcn_s_sleep(PSLEEP); }
            __builtin_amdgcn_s_sleep(PWAIT);
            fetch(xb, u); extra(u);
            miss = any_sent(xb);
            if (miss && sp.fail(c, 0xB80 + u)) break;
        }
        MARK(1);
#pragma unroll
        for (int k = 0; k < 12; ++k) pin_v(xb[k]);
        asm volatile("s_nop 1");
        u32x2 hv = hn, xv = xn;
        const int un = next_unit<U0>(u), u2 = next_unit<U0>(un);
        unsigned pr = SENT;
        const bool early = ahead;
        if ((early ? u2 : un) < NUNIT) pr = probe(early ? u2 : un);
        asm volatile("" : "+v"(hv), "+v"(xv));
        f32x4 acc[4];
        sfor<0, 48>([&](auto I) __attribute__((always_inline)) {
            constexpr int i = decltype(I)::value, t = i / 12, k = i % 12;
            mf_av(acc[t], w[t][k], xb[k], k == 0);
            if constexpr (i % 3 == 0 && i < 36) { constexpr int kk = i / 3; if (early) nx[kk] = ld128(rw, l16 + (kk & 3) * 1024, (unsigned)(O_G3F + (size_t)un * G3_B) + (wave * 12 + (kk & ~3)) * 1024); }
            if constexpr (i == 35) { if (early) extra(un); }
            if constexpr (i == 36) { if (!early) { ahead = __builtin_amdgcn_readfirstlane(pr) != SENT; if (ahead) { fetch(nx, un); extra(un); pr = SENT; if (u2 < NUNIT) pr = probe(u2); } } }
            if constexpr (t >= 1 && k == 8) { pin_v(acc[t - 1]); *(LAS f32x4*)(wr[t - 1] + buf * 12288) = acc[t - 1]; }
        });
        bar();
        f32x4 p[3];
#pragma unroll
        for (int j = 0; j < 3; ++j) p[j] = *(LAS const f32x4*)(rd + buf * 12288 + j * 1024);
        mf_done(acc[3]);
        const f32x4 o = (acc[3] + p[0]) + (p[1] + p[2]);
        MARK(2);
        for (unsigned c = 0; any_lane(max(max(hv.x, hv.y), max(xv.x, xv.y)) == SENT);) {
            if (sp.fail(c, 0xC00 + u)) break;
            hv = ld64(rw, po, (unsigned)(O_HF + (size_t)u * XF_B)); xv = ld64(rw, po, (unsigned)(O_X1F + (size_t)u * XF_B));
        }
        const u32x2 r = {pk(bflo(hv.x) + (bflo(xv.x) + o[0]), bfhi(hv.x) + (bfhi(xv.x) + o[1])), pk(bflo(hv.y) + (bflo(xv.y) + o[2]), bfhi(hv.y) + (bfhi(xv.y) + o[3]))};
        __builtin_amdgcn_s_waitcnt(0x0F70);
        miss = !ahead || any_sent(nx); ahead = ahead && __builtin_amdgcn_readfirstlane(pr) != SENT;
        asm volatile("" : "+v"(hn), "+v"(xn));
        __builtin_amdgcn_raw_buffer_store_b64(r, ro, ((16 * u + n) * D_ + 16 * ct + 4 * g) * 2, 0, 0);
        MARK(3); FLUSH(u);
    };
    for (int u = rep ? U0 + rep : 0;;) {
        unit(u, 0, xa, xc); u = next_unit<U0>(u); if (u >= NUNIT) break;
        unit(u, 1, xc, xa); u = next_unit<U0>(u); if (u >= NUNIT) break;
    }
}

DEV unsigned char* ws_base(const Params& P) { return P.ws + ((0u - (unsigned)(uintptr_t)P.ws) & ((2u << 20) - 1u)); }

DEV void publish_ready(rsrc_t rw, int i, unsigned near, int tid, Spin& sp) {
    sp.start();
    vmwait<0>();
    bar();
    if (tid == 0) st128(u32x4{sp.tag, near, rawnow() - 3000u, ~sp.tag}, rw, i * 128, (unsigned)(O_CTRL + 4 * W_READY));
}
DEV void init_F(const Params& P, unsigned char* ws, int f, LAS char* lds, int wave, int lane, Spin& sp) {
    const rsrc_t rw = mk_rsrc(ws, (unsigned)WS_END);
    const int tid = wave * 64 + lane;
    LAS float* red = (LAS float*)lds;
    {
        const unsigned o = (unsigned)(f & 31) * PAGE_B + PAGE_B - 16384u + wave * 1024 + (f >> 5) * 4096;
        unsigned lat[2] = {~0u, ~0u};
        vmwait<0>();
#pragma unroll
        for (int i = 0; i < 4; ++i) {
            unsigned long long t0, t1;
            asm volatile("s_memtime %0\n\ts_waitcnt lgkmcnt(0)" : "=s"(t0) :: "memory");
            st32(1u, rw, 0, o + (i & 1) * 8192 + (i >> 1) * 512);
            asm volatile("s_waitcnt vmcnt(0)\n\ts_memtime %0\n\ts_waitcnt lgkmcnt(0)" : "=s"(t1) :: "memory");
            lat[i & 1] = min(lat[i & 1], (unsigned)(t1 - t0));
        }
        red[1024 + wave] = lat[1] < lat[0] ? 1.f : -1.f;
        red[1028 + wave] = (float)lat[0] - (float)lat[1];
    }
    const int r = tid & 15, pc = tid >> 4;
    const rsrc_t rx = mk_rsrc(P.x, S_ * D_ * 2);
    u32x4 v[4][5];
#pragma unroll
    for (int j = 0; j < 4; ++j)
#pragma unroll
        for (int i = 0; i < 5; ++i) v[j][i] = __builtin_amdgcn_raw_buffer_load_b128(rx, (r * D_ + (pc + 16 * i) * 8) * 2, min(f + 40 * j, NUNIT - 1) * (16 * D_ * 2), 0);
    if (f < 3) {
        const int p = f * 256 + tid;
        if (p < 640) {
            const unsigned* src = (const unsigned*)(P.mem0 + ((p >> 1) & 7) * D_ + (p >> 4) * 16 + (p & 1) * 2);
            const u32x4 m = {nosent(src[0]), nosent(src[2]), nosent(src[4]), nosent(src[6])};
            const unsigned o = p * 16, oa = ((o >> 13) << 14) | (o & 8191u);
            st128(m, rw, oa, 0); st128(m, rw, oa, 8192);
        }
    }
    float mu[4];
#pragma unroll
    for (int j = 0; j < 4; ++j) {
        float s1 = 0.f;
#pragma unroll
        for (int i = 0; i < 5; ++i)
#pragma unroll
            for (int k = 0; k < 4; ++k) s1 += bflo(v[j][i][k]) + bfhi(v[j][i][k]);
        s1 = x32(x16(s1, false), false);
        red[(j * 2) * 64 + wave * 16 + r] = s1;
    }
    bar();
#pragma unroll
    for (int j = 0; j < 4; ++j) {
        const LAS float* q = red + (j * 2) * 64 + r;
        mu[j] = ((q[0] + q[16]) + (q[32] + q[48])) * (1.f / D_);
        float s2 = 0.f;
#pragma unroll
        for (int i = 0; i < 5; ++i)
#pragma unroll
            for (int k = 0; k < 4; ++k) { const float a = bflo(v[j][i][k]) - mu[j], c = bfhi(v[j][i][k]) - mu[j]; s2 += a * a + c * c; }
        s2 = x32(x16(s2, false), false);
        red[(j * 2 + 1) * 64 + wave * 16 + r] = s2;
    }
    bar();
#pragma unroll
    for (int j = 0; j < 4; ++j) {
        const LAS float* q = red + (j * 2 + 1) * 64 + r;
        const float rs = __builtin_amdgcn_rsqf(((q[0] + q[16]) + (q[32] + q[48])) * (1.f / D_) + 1e-5f);
        if (f + 40 * j < NUNIT) {
#pragma unroll
            for (int i = 0; i < 5; ++i) {
                u32x4 o;
#pragma unroll
                for (int k = 0; k < 4; ++k) o[k] = pk((bflo(v[j][i][k]) - mu[j]) * rs, (bfhi(v[j][i][k]) - mu[j]) * rs);
                st128(o, rw, (pc + 16 * i) * 256 + r * 16, (unsigned)(O_XF + (size_t)(f + 40 * j) * XF_B));
            }
        }
    }
    const float vote = (red[1024] + red[1025]) + (red[1026] + red[1027]), dsum = (red[1028] + red[1029]) + (red[1030] + red[1031]);
    publish_ready(rw, f, (vote > 0.f || (vote == 0.f && dsum > 0.f)) ? 1u : 0u, tid, sp);
    bar();
}
DEV void init_arm(unsigned char* ws, int i, int tid, Spin& sp) {
    const rsrc_t rw = mk_rsrc(ws, (unsigned)WS_END);
    const int q = i * 256 + tid;
    constexpr int NT = 40 * 256;
    const u32x4 sv = SENT4;
    if (q < (W_READY - W_QKPROG) / 4) st128(u32x4{0u, 0u, 0u, 0u}, rw, q * 16, (unsigned)(O_CTRL + 4 * W_QKPROG));
    if (q < S_ / 2) st128(sv, rw, q * 16, (unsigned)O_ST);
    constexpr int RU = REC_BLOCKS * 1024;
    for (int r = 0; r < KARM; ++r)
        for (int j = q; j < RU; j += NT) if (r > 0 || j >= 2048) st128(sv, rw, j * 16, (unsigned)r * 4 * PAGE_B);
    constexpr int NA = (2 * KARM + 1) * (int)(XF_B / 16), NG = 2 * KARM * (int)(G3_B / 16);
    for (int j = q; j < NA; j += NT) { st128(sv, rw, j * 16, (unsigned)O_HF); st128(sv, rw, j * 16, (unsigned)O_GF); st128(sv, rw, j * 16, (unsigned)O_X1F); }
    for (int j = q; j < NG; j += NT) st128(sv, rw, j * 16, (unsigned)O_G3F);
    constexpr int TA = (NUNIT - UTAIL) * (int)(XF_B / 16), TG = (NUNIT - UTAIL) * (int)(G3_B / 16);
    for (int j = q; j < TA; j += NT) { st128(sv, rw, j * 16, (unsigned)(O_HF + UTAIL * XF_B)); st128(sv, rw, j * 16, (unsigned)(O_GF + UTAIL * XF_B)); st128(sv, rw, j * 16, (unsigned)(O_X1F + UTAIL * XF_B)); }
    for (int j = q; j < TG; j += NT) st128(sv, rw, j * 16, (unsigned)(O_G3F + UTAIL * G3_B));
    publish_ready(rw, 40 + i, 0u, tid, sp);
}

__global__ void __launch_bounds__(256, 1) k_main(Params P) {
    extern __shared__ __attribute__((aligned(16))) char smem[];
    LAS char* lds = (LAS char*)smem;
    const int tid = threadIdx.x, lane = tid & 63, wave = __builtin_amdgcn_readfirstlane(tid >> 6);
    Spin sp;
    {
        const char* code; unsigned len;
        asm volatile("s_getpc_b64 %0\n.Lmain_pc:\n\ts_mov_b32 %1, .Lmain_end-.Lmain_pc" : "=s"(code), "=s"(len));
        sp.cpf = __builtin_amdgcn_raw_buffer_load_b32(mk_rsrc(code, len & ~3u), ((blockIdx.x >> 3) * 256 + tid) * 128, 0, 0);
    }
    unsigned char* ws = ws_base(P);
    gu32* ctrl = (gu32*)(ws + O_CTRL);
    sp.tmo = ctrl + W_TMO; sp.dead = false;
    sp.ev = __hip_atomic_load(ctrl + W_EPOCH, RLX_AGENT);
    if (tid == 0) {
        unsigned slot = (xcc_id() & 7u) * 32u + (blockIdx.x >> 3);
        for (int k = 0; k < 256; ++k, slot = (slot + 1u) & 255u)
            if (__hip_atomic_exchange((GAS unsigned long long*)(ctrl + W_CLAIM) + slot * 2u, (sp.ev + 1u) | ((unsigned long long)~(sp.ev + 1u) << 32), RLX_AGENT) != ((sp.ev + 1u) | ((unsigned long long)~(sp.ev + 1u) << 32))) break;
        *(LAS unsigned*)lds = slot;
    }
    __syncthreads();
    const int slot = __builtin_amdgcn_readfirstlane(*(LAS unsigned*)lds);
    __syncthreads();
    const int xq = slot >> 5, t = slot & 31;
    int mlp = -1, rep = 0;
    if (xq < 4) {
        if (t < 8) role_S(P, ws, xq * 8 + t, lds, wave, lane, sp);
        else if (t < 18) { init_F(P, ws, xq * 10 + (t - 8), lds, wave, lane, sp); role_F(P, ws, xq * 10 + (t - 8), lds, wave, lane, sp); }
        else if (t < 26) role_UV(P, ws, xq * 8 + (t - 18), lds, wave, lane, sp);
        else mlp = xq * 6 + (t - 26);
    } else {
        const int y = xq - 4;
        if (t < 16) { role_VC(P, ws, y * 8 + (t >> 1), t & 1, lds, wave, lane, sp); mlp = (t >> 1) * 4 + y; rep = 1 + (t & 1); }
        else if (t < 28) { role_QK(P, ws, y * 12 + (t - 16), lds, wave, lane, sp); if (t < 24) { mlp = (t - 16) * 4 + y; rep = 3; } }
        else mlp = t < 30 ? 24 + y * 2 + (t - 28) : 32 + y * 2 + (t - 30);
    }
    if (mlp >= 0) {
        if (rep == 0) {
            const unsigned t0 = rawnow();
            init_arm(ws, mlp, tid, sp);
            while (rawnow() - t0 < DLY2) __builtin_amdgcn_s_sleep(4);
        }
        if (mlp < 5) role_W<1>(P, ws, mlp, rep, lds, wave, lane, sp);
        else if (mlp < 10) role_W<2>(P, ws, mlp - 5, rep, lds, wave, lane, sp);
        else if (mlp < 22) role_W<3>(P, ws, mlp - 10, rep, lds, wave, lane, sp);
        else if (mlp < 32) role_W4(P, ws, mlp - 22, rep, lds, wave, lane, sp);
        if (mlp == 22 && rep == 0 && tid == 0) __hip_atomic_store(ctrl + W_EPOCH, sp.tag, RLX_AGENT);
    }
    MARK(0); MARKC(1); FLUSH(129);
    asm volatile(".Lmain_end:");
}

extern "C" void kernel_launch(void* const* d_in, const int* in_sizes, int n_in, void* d_out, int out_size, void* d_ws, size_t ws_size, hipStream_t stream) {
    Params P;
    P.x = (const unsigned short*)d_in[0]; P.mem0 = (const unsigned short*)d_in[1];
    P.wq = (const unsigned short*)d_in[2]; P.wk = (const unsigned short*)d_in[3]; P.wv = (const unsigned short*)d_in[4]; P.wo = (const unsigned short*)d_in[5];
    P.w1 = (const unsigned short*)d_in[8]; P.w2 = (const unsigned short*)d_in[9]; P.w3 = (const unsigned short*)d_in[10]; P.w4 = (const unsigned short*)d_in[11];
    P.out = (unsigned short*)d_out; P.ws = (unsigned char*)d_ws;
    (void)hipFuncSetAttribute((const void*)k_main, hipFuncAttributeMaxDynamicSharedMemorySize, LDS_BYTES);
    hipLaunchKernelGGL(k_main, dim3(256), dim3(256), LDS_BYTES, stream, P);
}
```

```cpp
#include <hip/hip_runtime.h>
#include <stdint.h>

#define GAS __attribute__((address_space(1)))
#define LAS __attribute__((address_space(3)))
#define RLX_AGENT __ATOMIC_RELAXED, __HIP_MEMORY_SCOPE_AGENT
#define DEV __device__ __forceinline__
typedef __bf16 bf16x2 __attribute__((ext_vector_type(2)));
typedef __bf16 bf16x8 __attribute__((ext_vector_type(8)));
typedef __bf16 bf16x16 __attribute__((ext_vector_type(16)));
typedef short s16x4 __attribute__((ext_vector_type(4)));
typedef float f32x2 __attribute__((ext_vector_type(2)));
typedef float f32x4 __attribute__((ext_vector_type(4)));
typedef unsigned u32x2 __attribute__((ext_vector_type(2)));
typedef unsigned u32x4 __attribute__((ext_vector_type(4)));
typedef unsigned u32x8 __attribute__((ext_vector_type(8)));
typedef GAS unsigned gu32;
typedef __amdgpu_buffer_rsrc_t rsrc_t;

constexpr int S_ = 2048, D_ = 640, H_ = 32, E_ = 192, F_ = 1536, NSEG = 64, NUNIT = 128;
constexpr unsigned SENT = 0xFFFFFFFFu;
constexpr unsigned SPINCAP = 1u << 16;

constexpr unsigned PAGE_B = 2u << 20;
constexpr int NSLOT = 8;
constexpr unsigned R_MEM = 0;
constexpr unsigned R_P = 2 * 8192;
constexpr unsigned R_UB = 5 * 8192;
constexpr unsigned R_PX = 45 * 8192;
constexpr unsigned PX_B = 40960;
constexpr unsigned R_NF = 55 * 8192;
constexpr unsigned R_NS = 56 * 8192;
constexpr int REC_BLOCKS = 57;
#define DELTA_A 15
#define DELTA_B 15
#define VIS 4
#define SLOW 105
#define BULK_FAR 14
#define PSLEEP 4
#define PWAIT 3
#define DLY1 0
#define DLY2 1500
#define DLY3 0
#define NTMASK 0
#define ROTB 0
#define U0_1 117
#define U0_2 115
#define U0_3 111
#define U0_4 106
#define CMIX 1
#define LSLEEP 1
#define MLP0 4
#define EARLY 36u
#define LATE 40
#define PACE 10
constexpr unsigned amap(unsigned off) { return ((off >> 13) << 14) | (off & 8191u); }
constexpr int UTAIL = 104;
constexpr int KARM = 4;
constexpr size_t O_CTRL = (size_t)NSLOT * 4 * PAGE_B, CTRL_BYTES = 65536;
constexpr int W_TMO = 0  , W_EPOCH = 64, W_READY = 12288  , W_CLAIM = 15360  , W_QKPROG = 1024  , W_VCPROG = 4096  , W_PROG = 8192  ;
constexpr size_t O_ST = O_CTRL + CTRL_BYTES + 4096;
constexpr size_t O_HF = O_ST + S_ * 8, XF_B = 20480;
constexpr size_t O_GF = O_HF + NUNIT * XF_B;
constexpr size_t O_X1F = O_GF + NUNIT * XF_B;
constexpr size_t O_G3F = O_X1F + NUNIT * XF_B, G3_B = 49152;
constexpr size_t O_XF = O_G3F + NUNIT * G3_B;
constexpr unsigned QX_B = 12288, R_QX = 0, R_KX = H_ * QX_B;
constexpr size_t WS_END = O_XF + NUNIT * XF_B;
static_assert(WS_END < (238u << 20), "workspace");
constexpr int LDS_BYTES = 160 * 1024;

struct Params {
    const unsigned short *x, *mem0, *wq, *wk, *wv, *wo, *w1, *w2, *w3, *w4;
    unsigned short* out;
    unsigned char* ws;
};

DEV unsigned xcc_id() { return (unsigned)__builtin_amdgcn_s_getreg(20 | (3 << 11)) & 0xfu; }
DEV rsrc_t mk_rsrc(const void* p, unsigned bytes) { return __builtin_amdgcn_make_buffer_rsrc((void*)p, 0, bytes, 0x00020000); }
DEV u32x4 ld128(rsrc_t r, unsigned v, unsigned s) { return __builtin_amdgcn_raw_buffer_load_b128(r, v, s, 16); }
DEV u32x2 ld64(rsrc_t r, unsigned v, unsigned s) { return __builtin_amdgcn_raw_buffer_load_b64(r, v, s, 16); }
DEV unsigned ld32(rsrc_t r, unsigned v, unsigned s) { return __builtin_amdgcn_raw_buffer_load_b32(r, v, s, 16); }
DEV void st128(u32x4 d, rsrc_t r, unsigned v, unsigned s) {
    __builtin_amdgcn_raw_buffer_store_b128(d, r, v, s, 16);
    asm volatile("s_nop 1" :: "v"(d) : "memory");
}
DEV void st64(u32x2 d, rsrc_t r, unsigned v, unsigned s) { __builtin_amdgcn_raw_buffer_store_b64(d, r, v, s, 16); }
DEV void st32(unsigned d, rsrc_t r, unsigned v, unsigned s) { __builtin_amdgcn_raw_buffer_store_b32(d, r, v, s, 16); }
DEV void st16(unsigned short d, rsrc_t r, unsigned v, unsigned s) { __builtin_amdgcn_raw_buffer_store_b16(d, r, v, s, 16); }
DEV u32x2 trd(LAS const char* p) { return __builtin_bit_cast(u32x2, __builtin_amdgcn_ds_read_tr16_b64_v4i16((LAS s16x4*)p)); }
DEV u32x4 cat2(u32x2 a, u32x2 b) { return __builtin_shufflevector(a, b, 0, 1, 2, 3); }
DEV u32x8 cat4(u32x4 a, u32x4 b) { return __builtin_shufflevector(a, b, 0, 1, 2, 3, 4, 5, 6, 7); }
DEV u32x4 lo4(u32x8 a) { return __builtin_shufflevector(a, a, 0, 1, 2, 3); }
DEV u32x4 hi4(u32x8 a) { return __builtin_shufflevector(a, a, 4, 5, 6, 7); }
DEV u32x4 lds128(LAS const char* p) { return *(LAS const u32x4*)p; }
DEV unsigned pk(float a, float b) { bf16x2 v = {(__bf16)a, (__bf16)b}; return __builtin_bit_cast(unsigned, v); }
DEV unsigned nosent(unsigned u) { return min(u, SENT - 1u); }
DEV unsigned pks(float a, float b) { return min(pk(a, b), SENT - 1u); }
DEV unsigned pka(float a, float b) { unsigned r; asm("v_cvt_pk_bf16_f32 %0, |%1|, |%2|" : "=v"(r) : "v"(a), "v"(b)); return r; }
DEV float bflo(unsigned u) { return __builtin_bit_cast(float, u << 16); }
DEV float bfhi(unsigned u) { return __builtin_bit_cast(float, u & 0xFFFF0000u); }
DEV unsigned f2u(float f) { return __builtin_bit_cast(unsigned, f); }
DEV float u2f(unsigned u) { return __builtin_bit_cast(float, u); }
DEV unsigned mx4(u32x4 v) { return max(max(v.x, v.y), max(v.z, v.w)); }
DEV bool any_lane(bool c) { return __builtin_amdgcn_ballot_w64(c) != 0; }
DEV void bar() { asm volatile("s_waitcnt lgkmcnt(0)" ::: "memory"); __builtin_amdgcn_s_barrier(); asm volatile("" ::: "memory"); }
DEV void fold32(float& a, float& b) {
    auto r = __builtin_amdgcn_permlane32_swap(f2u(a), f2u(b), false, false); a = u2f(r[0]); b = u2f(r[1]);
}
DEV float x32(float v, bool mx) { auto r = __builtin_amdgcn_permlane32_swap(f2u(v), f2u(v), false, false); return mx ? fmaxf(u2f(r[0]), u2f(r[1])) : u2f(r[0]) + u2f(r[1]); }
DEV float x16(float v, bool mx) { auto r = __builtin_amdgcn_permlane16_swap(f2u(v), f2u(v), false, false); return mx ? fmaxf(u2f(r[0]), u2f(r[1])) : u2f(r[0]) + u2f(r[1]); }
DEV float fmax2(float a, float b) { float r; asm("v_max_f32 %0, %1, %2" : "=v"(r) : "v"(a), "v"(b)); return r; }
DEV float fmax3(float a, float b, float c) { float r; asm("v_max3_f32 %0, %1, %2, %3" : "=v"(r) : "v"(a), "v"(b), "v"(c)); return r; }
#define ROWOP2(op) asm volatile("s_nop 1\n\t" \
    op " %0, %0, %0 row_ror:8 row_mask:0xf bank_mask:0xf\n\t" op " %1, %1, %1 row_ror:8 row_mask:0xf bank_mask:0xf\n\ts_nop 0\n\t" \
    op " %0, %0, %0 row_ror:4 row_mask:0xf bank_mask:0xf\n\t" op " %1, %1, %1 row_ror:4 row_mask:0xf bank_mask:0xf\n\ts_nop 0\n\t" \
    op " %0, %0, %0 row_ror:2 row_mask:0xf bank_mask:0xf\n\t" op " %1, %1, %1 row_ror:2 row_mask:0xf bank_mask:0xf\n\ts_nop 0\n\t" \
    op " %0, %0, %0 row_ror:1 row_mask:0xf bank_mask:0xf\n\t" op " %1, %1, %1 row_ror:1 row_mask:0xf bank_mask:0xf" : "+v"(a), "+v"(b))
DEV void row_max2(float& a, float& b) { ROWOP2("v_max_f32_dpp"); }
DEV void row_sum2(float& a, float& b) { ROWOP2("v_add_f32_dpp"); }
DEV unsigned pair_cols(unsigned p, unsigned psel) { return __builtin_amdgcn_perm(p, __builtin_amdgcn_update_dpp(0u, p, 0xB1, 0xf, 0xf, true), psel); }
DEV float ror8(float v) { return u2f(__builtin_amdgcn_update_dpp(0u, f2u(v), 0x128, 0xf, 0xf, true)); }
template <int OFF> DEV void glds(unsigned voff, const void* sbase, unsigned lds_dst) {
    asm volatile("s_mov_b32 m0, %2\n\ts_nop 0\n\tglobal_load_lds_dwordx4 %0, %1 offset:%3 sc1" :: "v"(voff), "s"(sbase), "s"(lds_dst), "n"(OFF) : "memory");
}
template <int N, int STEP> DEV void glds_dev(unsigned voff, const void* sbase, unsigned lds_dst) {
    static_assert(N == 2 || N == 4, "");
    if (N == 2) asm volatile("s_mov_b32 m0, %2\n\ts_nop 0\n\tglobal_load_lds_dwordx4 %0, %1 sc1\n\tglobal_load_lds_dwordx4 %0, %1 offset:%3 sc1" :: "v"(voff), "s"(sbase), "s"(lds_dst), "n"(STEP) : "memory");
    else asm volatile("s_mov_b32 m0, %2\n\ts_nop 0\n\tglobal_load_lds_dwordx4 %0, %1 sc1\n\tglobal_load_lds_dwordx4 %0, %1 offset:%3 sc1\n\tglobal_load_lds_dwordx4 %0, %1 offset:%4 sc1\n\tglobal_load_lds_dwordx4 %0, %1 offset:%5 sc1"
                      :: "v"(voff), "s"(sbase), "s"(lds_dst), "n"(STEP), "n"(2 * STEP), "n"(3 * STEP) : "memory");
}
template <int N> DEV void vmwait() { asm volatile("s_waitcnt vmcnt(%0)" :: "n"(N) : "memory"); }
template <class T> DEV void pin_a(T& v) { asm volatile("" : "+a"(v)); }
template <class T> DEV void pin_v(T& v) { asm volatile("" : "+v"(v)); }
#define MF_ "v_mfma_f32_16x16x32_bf16 "
DEV void smf_a(f32x4& c, u32x4 a, u32x8 b, int idx) { asm volatile("v_smfmac_f32_16x16x64_bf16 %0, %1, %2, %3" : "+v"(c) : "v"(a), "a"(b), "v"(idx)); }
DEV void smf_v(f32x4& c, u32x4 a, u32x8 b, int idx) { asm volatile("v_smfmac_f32_16x16x64_bf16 %0, %1, %2, %3" : "+v"(c) : "v"(a), "v"(b), "v"(idx)); }
DEV void mf_vv(f32x4& c, u32x4 a, u32x4 b, bool first) { if (first) asm volatile(MF_ "%0, %1, %2, 0" : "=&v"(c) : "v"(a), "v"(b)); else asm volatile(MF_ "%0, %1, %2, %0" : "+v"(c) : "v"(a), "v"(b)); }
DEV void mf_av(f32x4& c, u32x4 a, u32x4 b, bool first) { if (first) asm volatile(MF_ "%0, %1, %2, 0" : "=&v"(c) : "a"(a), "v"(b)); else asm volatile(MF_ "%0, %1, %2, %0" : "+v"(c) : "a"(a), "v"(b)); }
DEV void mf_va(f32x4& c, u32x4 a, u32x4 b, bool first) { if (first) asm volatile(MF_ "%0, %1, %2, 0" : "=&v"(c) : "v"(a), "a"(b)); else asm volatile(MF_ "%0, %1, %2, %0" : "+v"(c) : "v"(a), "a"(b)); }
DEV void mf_done(f32x4& a) { asm volatile("s_nop 7" : "+v"(a)); }
DEV void mf_done(f32x4& a, f32x4& b) { asm volatile("s_nop 7" : "+v"(a), "+v"(b)); }
DEV void mf_done(f32x4& a, f32x4& b, f32x4& c) { asm volatile("s_nop 7" : "+v"(a), "+v"(b), "+v"(c)); }
DEV void mf_done(f32x4& a, f32x4& b, f32x4& c, f32x4& d) { asm volatile("s_nop 7" : "+v"(a), "+v"(b), "+v"(c), "+v"(d)); }
DEV float zero1() { float v; asm volatile("v_mov_b32 %0, 0" : "=v"(v)); return v; }
DEV void zero_acc(f32x4& a) { a = f32x4{zero1(), zero1(), zero1(), zero1()}; asm volatile("s_nop 1" : "+v"(a)); }
DEV LAS char* opq(LAS char* p) { unsigned u = (unsigned)(uintptr_t)p; asm volatile("" : "+v"(u)); return (LAS char*)(uintptr_t)u; }
template <int V> struct IC { static constexpr int value = V; constexpr operator int() const { return V; } };
template <int I, int N, class F> DEV void sfor(F&& f) { if constexpr (I < N) { f(IC<I>{}); sfor<I + 1, N>(f); } }
DEV void lgkm0() { asm volatile("s_waitcnt lgkmcnt(0)" ::: "memory"); __builtin_amdgcn_sched_barrier(0); }

#define MARK(i)
#define MARK2(i)
#define MARK3(i)
#define SETV(i, v)
#define MARKC(i)
#define FLUSH(step)
struct Spin {
    gu32* tmo; bool dead; unsigned tag;
    unsigned org, ev, cpf;
    DEV void start() { asm volatile("" :: "v"(cpf)); tag = __builtin_amdgcn_readfirstlane(ev) + 1u; }
    DEV bool fail(unsigned& n, unsigned code) {
        asm volatile("" ::: "memory");
        if (dead) return true;
        ++n;
        if (n > SPINCAP) { __hip_atomic_store(tmo + 1, code, RLX_AGENT); __hip_atomic_store(tmo, tag, RLX_AGENT); dead = true; return true; }
        if ((n & 63u) == 0u && (unsigned)__builtin_amdgcn_readfirstlane(__hip_atomic_load(tmo, RLX_AGENT)) == tag) { dead = true; return true; }
        return false;
    }
};

template <int NT> DEV int img_rot(int rho) { return (NT & 1) ? 0 : (NT & 2) ? ((rho >> 2) & 1) : (NT & 4) ? ((rho >> 1) & 3) : (rho & 7); }
DEV int swap23(int r) { return (r & 3) | ((r >> 1) & 4) | ((r << 1) & 8) | (r & 16); }
template <int NT> struct WSrc {
    unsigned voff[NT]; rsrc_t r;
    DEV void init(const char* src, int lane, unsigned stride, int trot = 0) {
        r = mk_rsrc(src - 4096, 0xFFFFFFFFu);
#pragma unroll
        for (int i = 0; i < NT; ++i) {
            const int P = 64 * i + lane, rho = P / (2 * NT), cs = P % (2 * NT);
            const int tile = (((cs >> 1) ^ img_rot<NT>(rho)) + trot) % NT;
            voff[i] = (unsigned)swap23(rho) * stride + (unsigned)(2 * tile + (cs & 1)) * 16u + 4096u - (i & 3) * 1024u;
        }
    }
    DEV void issue(unsigned so, unsigned lds_dst) {
        constexpr bool nt = (NTMASK >> (NT == 6 ? 1 : NT == 10 ? 2 : NT == 3 ? 3 : 0)) & 1;
#define BL_(v, o, p) "buffer_load_dwordx4 " v ", %0, %1 offen offset:" o p " lds\n\t"
#define GRP_(p) \
            if (i + 4 <= NT) asm volatile("s_mov_b32 m0, %2\n\ts_nop 0\n\t" BL_("%3", "0", p) BL_("%4", "1024", p) BL_("%5", "2048", p) BL_("%6", "3072", p) :: "s"(r), "s"(so), "s"(d), "v"(voff[i]), "v"(voff[(i + 1) % NT]), "v"(voff[(i + 2) % NT]), "v"(voff[(i + 3) % NT]) : "memory"); \
            else if (i + 3 == NT) asm volatile("s_mov_b32 m0, %2\n\ts_nop 0\n\t" BL_("%3", "0", p) BL_("%4", "1024", p) BL_("%5", "2048", p) :: "s"(r), "s"(so), "s"(d), "v"(voff[i]), "v"(voff[(i + 1) % NT]), "v"(voff[(i + 2) % NT]) : "memory"); \
            else asm volatile("s_mov_b32 m0, %2\n\ts_nop 0\n\t" BL_("%3", "0", p) BL_("%4", "1024", p) :: "s"(r), "s"(so), "s"(d), "v"(voff[i]), "v"(voff[(i + 1) % NT]) : "memory");
#pragma unroll
        for (int i = 0; i < NT; i += 4) {
            const unsigned d = lds_dst + i * 1024;
            if (nt) { GRP_(" nt") } else { GRP_("") }
        }
#undef GRP_
#undef BL_
    }
};
template <int NT, bool PAIR> struct WRd {
    static constexpr int RR = (NT & 1) ? 1 : (NT & 2) ? 2 : (NT & 4) ? 4 : 8, NB = PAIR ? RR / 2 : RR;
    LAS char* b[NB];
    DEV void init(LAS char* base, int lane) {
        const int g = lane >> 4, q = (lane >> 2) & 3, p = lane & 3, rho = q | ((g & 1) << 2) | ((g >> 1) << 4), rot = img_rot<NT>(rho);
#pragma unroll
        for (int j = 0; j < NB; ++j) b[j] = opq(base + rho * (NT * 32) + (PAIR ? ((2 * j + (p >> 1)) ^ rot) * 32 + 16 * (p & 1) : (j ^ rot) * 32 + 8 * p));
    }
    DEV LAS char* at(int off, int tile) const { return PAIR ? b[(tile >> 1) % NB] + off + (tile / RR) * RR * 32 + 8 * (tile & 1) : b[tile % NB] + off + (tile / RR) * RR * 32; }
    DEV u32x4 get(int off, int tile) const { LAS char* a = at(off, tile); return cat2(trd(a), trd(a + 8 * NT * 32)); }
};
DEV void vmwait_n(int n) {
    switch (n) {
#define VW(n) case n: vmwait<n>(); break;
        VW(0) VW(3) VW(4) VW(6) VW(8) VW(9) VW(10) VW(12) VW(15) VW(16) VW(18) VW(20) VW(24) VW(26) VW(28) VW(29) VW(30) VW(32) VW(35) VW(36) VW(40)
#undef VW
        default: vmwait<0>(); break;
    }
}
template <int NT, int NK, int NKT, int RING, int SROT, bool PAIR, class Sink>
DEV void stream_w(const char* src, unsigned stride, LAS char* lds, unsigned base, int lane, Sink&& sink, int rot = 0, int trot = 0) {
    auto krow = [&](int k) { int kk = k + 2 * rot; if (kk >= NKT) kk -= NKT; return (unsigned)kk * 32u * stride; };
    WSrc<NT> ws; ws.init(src, lane, stride, trot);
    const unsigned lbase = (unsigned)(uintptr_t)lds + base;
#pragma unroll
    for (int k = 0; k < RING && k < NKT; ++k) ws.issue(krow(k), lbase + ((k + SROT) % RING) * NT * 1024);
    WRd<NT, PAIR> rd; rd.init(lds + base, lane);
#pragma unroll
    for (int k = 0; k < NK; ++k) {
        const int issued = (k + RING < NKT) ? k + RING : NKT;
        vmwait_n((issued - k - 1) * NT);
        u32x4 fr[NT];
#pragma unroll
        for (int t = 0; t < NT; ++t) fr[t] = rd.get(((k + SROT) % RING) * NT * 1024, t);
#pragma unroll
        for (int t = 0; t < NT; ++t) sink(t, k, fr[t]);
        if (k + RING < NKT) { lgkm0(); ws.issue(krow(k + RING), lbase + ((k + SROT) % RING) * NT * 1024); }
    }
}

DEV unsigned page(unsigned par, int s, int p) { const unsigned i = (unsigned)(s & (NSLOT - 1)) * 4u + (unsigned)p; return i * PAGE_B + (((par >> i) & 1u) << 13); }
DEV unsigned rec(unsigned par, int s) { return page(par, s, 0); }
DEV void wait_slot(rsrc_t rw, int s, unsigned& seen, int rep, Spin& sp) {
    for (unsigned c = 0; (int)seen + 6 < s;) {
        seen = __builtin_amdgcn_readfirstlane(ld32(rw, 0, (unsigned)(O_CTRL + 4 * (W_PROG + 32 * rep))));
        if ((int)seen + 6 >= s || sp.fail(c, 0x280 + s)) break;
        __builtin_amdgcn_s_sleep(8);
    }
}
template <bool START = true> DEV unsigned load_par(rsrc_t rw, int lane, Spin& sp) {
    if (START) sp.start();
    u32x4 a, b;
    for (unsigned c = 0;;) {
        a = ld128(rw, lane * 128, (unsigned)(O_CTRL + 4 * W_READY)); b = ld128(rw, (64 + (lane & 15)) * 128, (unsigned)(O_CTRL + 4 * W_READY));
        if (!any_lane(a.x != sp.tag || a.w != ~sp.tag || b.x != sp.tag || b.w != ~sp.tag) || sp.fail(c, 0x40)) break;
        __builtin_amdgcn_s_sleep(16);
    }
    unsigned m = (unsigned)__builtin_amdgcn_ballot_w64(a.y == 1u);
    const int c = __builtin_popcount(m & 0xAAAAAAAAu) - __builtin_popcount(m & 0x55555555u);
    if (c >= 5) m = 0xAAAAAAAAu; else if (c <= -5) m = 0x55555555u;
    sp.org = __builtin_amdgcn_readfirstlane(a.z);
    return m ^ (BULK_FAR * 0x11111111u);
}
DEV unsigned sent1() { unsigned v; asm volatile("v_mov_b32 %0, -1" : "=v"(v)); return v; }
#define SENT4 (u32x4{sent1(), sent1(), sent1(), sent1()})
#define SENT2 (u32x2{sent1(), sent1()})
DEV unsigned rawnow() { unsigned long long v; asm volatile("s_memrealtime %0\n\ts_waitcnt lgkmcnt(0)" : "=s"(v) :: "memory"); return (unsigned)v; }
struct Clock {
    unsigned org;
    DEV unsigned now() const { return rawnow() - org; }
    DEV unsigned wait_until(unsigned T) const {
        unsigned t = rawnow(); T += org;
        if ((int)(T - t) > 1000) T = t + 1000u;
        while ((int)(t - T) < 0) t = rawnow();
        return t - org;
    }
    DEV unsigned wait_fine(unsigned T) const {
        unsigned t = rawnow(); T += org;
        if ((int)(T - t) > 1000) T = t + 1000u;
        if ((int)(t - T) >= -2) return t + 2u - org;
        const unsigned E = T - 4u;
        while ((int)(t - E) < 0) t = rawnow();
        const unsigned rem = T - t;
        if (rem > 2) asm volatile("s_nop 7\n\ts_nop 1");
        if (rem & 1) return T - org;
        asm volatile("s_nop 4");
        return T - org;
    }
};
DEV unsigned umax_dpp(unsigned v, int k) {
    switch (k) { case 1: return max(v, (unsigned)__builtin_amdgcn_update_dpp(0u, v, 0x121, 0xf, 0xf, true)); case 2: return max(v, (unsigned)__builtin_amdgcn_update_dpp(0u, v, 0x122, 0xf, 0xf, true));
                 case 4: return max(v, (unsigned)__builtin_amdgcn_update_dpp(0u, v, 0x124, 0xf, 0xf, true)); default: return max(v, (unsigned)__builtin_amdgcn_update_dpp(0u, v, 0x128, 0xf, 0xf, true)); }
}
DEV unsigned half_max(unsigned v) {
    v = umax_dpp(umax_dpp(umax_dpp(umax_dpp(v, 1), 2), 4), 8);
    auto r = __builtin_amdgcn_permlane16_swap(v, v, false, false);
    return max((unsigned)r[0], (unsigned)r[1]);
}
DEV unsigned wave_max(unsigned v) { const unsigned m = half_max(v); return max((unsigned)__builtin_amdgcn_readlane(m, 0), (unsigned)__builtin_amdgcn_readlane(m, 32)); }
DEV u32x2 read_notes(u32x2 nf) {
    const unsigned tp = wave_max(nf.x == SENT ? 0u : nf.x), df = wave_max(nf.y == SENT ? 0u : min(nf.y, 400u));
    return u32x2{tp, tp + df + DELTA_B};
}
DEV u32x2 first_notes(rsrc_t rw, unsigned vn, unsigned so, Spin& sp) {
    u32x2 nf;
    for (unsigned c = 0;;) { nf = ld64(rw, vn, so); if (!any_lane(max(nf.x, nf.y) == SENT) || sp.fail(c, 0x50)) break; __builtin_amdgcn_s_sleep(2); }
    return nf;
}
DEV unsigned mem_time(unsigned tp, unsigned plan, unsigned tn) { return tn + VIS > tp ? max(plan, tn + SLOW) : plan; }
struct MemStream {
    rsrc_t rw; unsigned l16, vn, lbase, ko[10];
    static DEV int rot_of(int base, int wave) { return (ROTB * base + ((wave * 5 + 1) >> 1)) % 10; }
    DEV void init(rsrc_t r, LAS char* lds, int lane, int rot, bool reader) {
        rw = r; l16 = lane * 16; vn = reader ? min(lane, 39) * 128 : (lane & 15) * 8; lbase = (unsigned)(uintptr_t)lds;
#pragma unroll
        for (int k = 0; k < 10; ++k) { int kk = k + rot; if (kk >= 10) kk -= 10; ko[k] = kk * 1024 + (kk >= 8 ? 8192 : 0); }
    }
    DEV u32x4 ld(unsigned rb, int kb) const { return ld128(rw, l16, rb + ko[kb]); }
    DEV unsigned so(unsigned rb, int kb) const { unsigned v = rb + ko[kb]; asm volatile("" : "+s"(v)); return v; }
    DEV u32x4 lds_(unsigned s) const { u32x4 v = ld128(rw, l16, s); __builtin_amdgcn_sched_barrier(0); return v; }
    DEV LAS char* at(int kb) const { unsigned a; asm volatile("v_add_u32 %0, %1, %2" : "=v"(a) : "s"(ko[kb] + lbase), "v"(l16)); return (LAS char*)(uintptr_t)a; }
    DEV void share(unsigned so, int kb) const {
        asm volatile("s_mov_b32 m0, %3\n\ts_nop 0\n\tbuffer_load_dwordx4 %0, %1, %2 offen sc1 lds" :: "v"(l16), "s"(rw), "s"(so), "s"(ko[kb] + lbase) : "memory");
    }
    DEV void lda(u32x4& a, unsigned so) const { asm volatile("buffer_load_dwordx4 %0, %1, %2, %3 offen sc1" : "=v"(a) : "v"(l16), "s"(rw), "s"(so)); }
    DEV u32x4 get(int kb) const { return lds128(at(kb)); }
    static DEV unsigned long long bad(u32x4 a, u32x4 b, u32x4 c, u32x4 d) {
        unsigned long long m = __builtin_amdgcn_ballot_w64(max(max(mx4(a), mx4(b)), max(mx4(c), mx4(d))) == SENT); asm volatile("" : "+s"(m)); return m;
    }
};
template <int N> DEV void vmw(u32x4& a) { asm volatile("s_waitcnt vmcnt(%1)" : "+v"(a) : "n"(N)); }
DEV unsigned long long nan2(float a, float b) { unsigned long long m; asm volatile("v_cmp_u_f32 %0, %1, %2" : "=s"(m) : "v"(a), "v"(b)); return m; }
DEV float add1(float a, float b) { float r; asm volatile("v_add_f32 %0, %1, %2" : "=v"(r) : "v"(a), "v"(b)); return r; }
DEV unsigned pk1(float a, float b) { unsigned r; asm volatile("v_cvt_pk_bf16_f32 %0, %1, %2" : "=v"(r) : "v"(a), "v"(b)); return r; }
DEV unsigned track(unsigned est, unsigned m, int s) { return m > est ? min(m, est + (s < 8 ? 8u : 3u)) : est - 1u - ((est - m) >> 3); }
struct AFrag {
    LAS char *p0, *p1;
    DEV void init(LAS char* img, int lane) {
        const int ga = lane >> 4, base = ga * 256 + (lane & 15) * 8, sw = (ga & 1) * 128;
        p0 = opq(img + base + sw); p1 = opq(img + base + (128 ^ sw));
    }
    DEV u32x4 get(int off) const { return cat2(trd(p0 + off), trd(p1 + off)); }
    static DEV int wofs(int lane, int odd) { const int g = lane >> 4, n = lane & 15; return (((2 * (n >> 2) + (n & 1)) * 32) ^ (odd * 128)) + g * 8 + ((n >> 1) & 1) * 2; }
};
DEV unsigned fold_pk(f32x4 c, float& f0, float& f1) {
    unsigned r;
    asm volatile("v_add_f32 %1, %3, %4\n\tv_add_f32 %2, %5, %6\n\tv_cvt_pk_bf16_f32 %0, %1, %2" : "=v"(r), "=&v"(f0), "=&v"(f1) : "v"(c[0]), "v"(c[1]), "v"(c[2]), "v"(c[3]));
    return r;
}
DEV void st2x16(LAS char* p0, LAS char* p1, int off, unsigned v) { *(LAS unsigned short*)(p0 + off) = (unsigned short)v; *(LAS unsigned short*)(p1 + off) = (unsigned short)(v >> 16); }

constexpr float SCALE_L2E = 0.07216878364870322f * 1.4426950408889634f;

DEV void role_S(const Params& P, unsigned char* ws, int h, LAS char* lds, int wave, int lane, Spin& sp) {
    constexpr int L_QK = 8192, L_FL = 14336, L_W = 16384, L_WS = 36864, L_KX = L_W + L_WS, L_QX = L_W + 3 * L_WS;
    const rsrc_t rw = mk_rsrc(ws, (unsigned)WS_END);
    const int rot = MemStream::rot_of(h, wave);
    u32x8 w[6][8];
    {
        const char* src = (const char*)(wave < 2 ? P.wq : P.wk) + h * 384 + (wave & 1) * 192;
        stream_w<6, 16, 20, 6, 4, false>(src, 12288u, lds, L_W + wave * L_WS, lane,
            [&](int t, int k, u32x4 f) {
                if (k & 1) { w[t][k >> 1] = cat4(lo4(w[t][k >> 1]), f); if ((k >> 1) * 6 + t < 32) pin_a(w[t][k >> 1]); } else w[t][k >> 1] = cat4(f, f); }, rot);
        vmwait<0>();
    }
    MARK(2); FLUSH(128);
    bar();
    const unsigned par = load_par(rw, lane, sp);
    const int idx = (lane & 1) ? 0xEEEE : 0x4444;
    const int g = lane >> 4, n = lane & 15;
    const unsigned cq = (unsigned)(O_CTRL + 4 * (W_QKPROG + 32 * (((wave < 2 ? 384 : 0) + h * 12 + (lane & 1) * 11) >> 4)));
    unsigned have = 0;
    auto wait_qk = [&](int s) {
        if (have > (unsigned)s) return;
        for (unsigned c = 0;;) {
            const unsigned v = ld32(rw, cq, 0);
            have = min((unsigned)__builtin_amdgcn_readlane(v, 0), (unsigned)__builtin_amdgcn_readlane(v, 1));
            if (have > (unsigned)s || sp.fail(c, 0x200 + s)) break;
            __builtin_amdgcn_s_sleep(2);
        }
    };
    Clock ck; ck.org = sp.org;
    MemStream ms; ms.init(rw, lds, lane, rot, wave == 1);
    volatile LAS unsigned* lfail = (volatile LAS unsigned*)(lds + L_FL);
    if (wave == 0) *lfail = ~0u;
    WRd<6, false> wr; wr.init(lds + L_W + wave * L_WS + 12288, lane);
    AFrag af; af.init(lds + L_QK, lane);
    LAS char* we = opq(lds + L_QK + wave * 1536 + AFrag::wofs(lane, 0));
    LAS char* wo = opq(lds + L_QK + wave * 1536 + AFrag::wofs(lane, 1));
    const unsigned psel = (n & 1) ? 0x07060302u : 0x01000504u, psel2 = (n & 2) ? 0x07060302u : 0x01000504u;
    unsigned so1, so2;
    if (wave == 0) {
        const int i = 2 * g + (n & 1), half = (n >> 1) & 1;
        so1 = amap(R_P) + (h >> 4) * 16384 + (h & 15) * 512 + ((n >> 3) * 16 + 2 * i + half) * 16 + ((n >> 2) & 1) * 8;
        so2 = amap(R_P + 16384) + (h >> 1) * 256 + (2 * (2 * g + half) + ((n >> 2) & 1)) * 16 + (h & 1) * 8 + (n >> 3) * 4;
    } else {
        const unsigned b = amap(R_PX) + (wave & 1) * amap(PX_B);
        so1 = (wave & 1) ? b + (h >> 3) * 16384 + (h & 7) * 1024 + lane * 16 : b + (h >> 4) * 16384 + ((h >> 1) & 7) * 1024 + lane * 16 + (h & 1) * 8;
        so2 = b + 65536 + h * 256 + n * 16 + g * 4;
    }
    unsigned xo[3];
#pragma unroll
    for (int j = 0; j < 3; ++j) { const unsigned o = (wave < 2 ? R_KX : R_QX) + h * QX_B + j * 4096; xo[j] = (((o >> 13) << 14) | (o & 8191u)) + (wave < 2 ? 0 : (wave & 1) * 1024); }
    auto stage_k = [&](int s) __attribute__((always_inline)) {
        wait_qk(s);
#pragma unroll
        for (int j = 0; j < 3; ++j) glds_dev<4, 1024>(ms.l16, ws + (page(par, s, 1) + xo[j]), (unsigned)(uintptr_t)lds + L_KX + (s & 1) * L_WS + j * 4096);
    };
    auto stage_q = [&](int s) __attribute__((always_inline)) {
        wait_qk(s);
#pragma unroll
        for (int j = 0; j < 3; ++j) glds_dev<2, 2048>(ms.l16, ws + (page(par, s, 1) + xo[j]), (unsigned)(uintptr_t)lds + L_QX + (wave & 1) * 1024 + j * 4096);
    };
    auto lwq = [&](int j, int q) {
        const int t = j % 6, kb = 1 - j / 6;
        return trd(wr.at((2 * kb + (q >> 1)) * 6144 + (q & 1) * 1536, t));
    };
    unsigned dur = 146, tn = 0;
    if (wave == 0) __builtin_amdgcn_s_setprio(3);
    if (wave == 1) stage_k(0); else if (wave >= 2) stage_q(0);
    vmwait<0>();
    bar();
    if (wave == 1) stage_k(1);
    for (int s = 0; s < NSEG; ++s) {
        const unsigned rb = rec(par, s);
        f32x4 acc[6];
        u32x2 st[12][4], nf; u32x4 a[10], kxf[12];
        LAS const char* kx = opq(lds + L_KX + (s & 1) * L_WS + lane * 16);
        unsigned long long bad = 0;
        auto prep = [&]() __attribute__((always_inline)) {
#pragma unroll
            for (int t = 0; t < 6; ++t) zero_acc(acc[t]);
#pragma unroll
            for (int i = 0; i < 12; ++i) st[i >> 2][i & 3] = lwq(i >> 2, i & 3);
        };
        float fa[6], fb[6]; unsigned fp[6];
        auto fin = [&](int t, int step) __attribute__((always_inline)) {
            if (step == 0) fa[t] = add1(acc[t][0], acc[t][1]);
            if (step == 1) fb[t] = add1(acc[t][2], acc[t][3]);
            if (step == 2) fp[t] = pk1(fa[t], fb[t]);
            if (step == 3) *(LAS unsigned short*)(((t & 1) ? wo : we) + t * 256) = (unsigned short)fp[t];
            if (step == 4) *(LAS unsigned short*)(((t & 1) ? wo : we) + t * 256 + 4) = (unsigned short)(fp[t] >> 16);
        };
        unsigned long long bad2 = 0;
        unsigned qp;
        LAS char* ad[6];
        auto slot = [&](auto I) __attribute__((always_inline)) {
            constexpr int i = decltype(I)::value % 64;
            constexpr bool fast = decltype(I)::value < 64;
            if constexpr (fast) {
                if constexpr (i == 0) vmw<6>(a[0]);
                if constexpr (i == 6) vmw<5>(a[9]);
                if constexpr (i == 7) vmw<4>(a[1]);
                if constexpr (i == 8) vmw<3>(a[2]);
            }
            if constexpr (i == 15) asm volatile("s_waitcnt vmcnt(0)\n\ts_barrier" ::: "memory");
            if constexpr (i == 15) { MARK3(0); }
            if constexpr (i == 36) { MARK3(5); }
            constexpr bool isl = i >= 6 && i <= 50 && (i & 3) == 2;
            constexpr int nl = i < 6 ? 0 : i > 50 ? 12 : (i - 6 + 3) / 4;
            if constexpr (isl) {
                smf_v(acc[nl % 6], a[9 - nl / 6], cat4(cat2(st[nl][0], st[nl][1]), cat2(st[nl][2], st[nl][3])), idx);
            } else {
                constexpr int r = i - nl;
                constexpr int kb = r < 7 ? r / 6 : r == 7 ? 2 : r < 13 ? 1 : r < 30 ? r / 6 : 5 + ((r - 30) % 6) / 2;
                constexpr int t = r < 7 ? r % 6 : r == 7 ? 0 : r < 13 ? r - 7 : r < 30 ? r % 6 : 2 * ((r - 30) / 6) + (r & 1);
                if constexpr (kb * 6 + t < 32) smf_a(acc[t], a[kb], w[t][kb], idx); else smf_v(acc[t], a[kb], w[t][kb], idx);
            }
            if constexpr (i == 0) { MARK2(2); }
            if constexpr (i == 12) bad = nan2(acc[0][0], acc[0][1]);
            if constexpr (i == 13) bad2 = nan2(acc[0][2], acc[0][3]);
            if constexpr (i == 15) a[3] = lds128(ad[0]);
            if constexpr (i == 18) nf = ld64(rw, ms.vn, rb + amap(R_NF));
            if constexpr (i == 25) qp = ld32(rw, cq, 0);
            if constexpr (i == 20) a[8] = lds128(ad[1]);
            if constexpr (i == 22) a[4] = lds128(ad[2]);
            if constexpr (i == 28) a[5] = lds128(ad[3]);
            if constexpr (i == 31) a[6] = lds128(ad[4]);
            if constexpr (i == 34) a[7] = lds128(ad[5]);
            if constexpr (i >= 7 && i < 43) st[(i - 7) / 4 + 3][(i - 7) & 3] = lwq((i - 7) / 4 + 3, (i - 7) & 3);
            if constexpr (i >= 43 && i < 51) kxf[i - 43] = lds128(kx + (i - 43) * 1024);
            if constexpr (i >= 55 && i < 59) kxf[i - 47] = lds128(kx + (i - 47) * 1024);
            if constexpr (i >= 49 && i < 52) { fin(0, i - 49); } if constexpr (i >= 52 && i < 55) { fin(1, i - 52); if constexpr (i < 54) fin(0, i - 49); }
            if constexpr (i >= 55 && i < 57) fin(1, i - 52);
            if constexpr (i >= 57) { fin(2, i - 57); fin(3, i - 57); }
        };
        prep();
        MARK(0); SETV(7, 0);
        ad[0] = ms.at(3); ad[1] = ms.at(8); ad[2] = ms.at(4); ad[3] = ms.at(5); ad[4] = ms.at(6); ad[5] = ms.at(7);
        const unsigned o0 = ms.so(rb, 0), o9 = ms.so(rb, 9), o1 = ms.so(rb, 1), o2 = ms.so(rb, 2);
        const unsigned tg = ck.wait_fine(tn);
        ms.lda(a[0], o0); ms.lda(a[9], o9); ms.lda(a[1], o1); ms.lda(a[2], o2);
        ms.share(o0, 0); ms.share(o1, 1); ms.share(o2, 2);
        SETV(1, tg + ck.org);
        sfor<0, 15>(slot);
        if (__builtin_expect((bad | bad2) != 0, 0)) {
            asm volatile("s_nop 7\n\ts_waitcnt vmcnt(0)" ::: "memory");
            *lfail = (unsigned)s; SETV(7, 1);
            for (unsigned c = 0;;) {
                a[0] = ms.ld(rb, 0); a[9] = ms.ld(rb, 9); a[1] = ms.ld(rb, 1); a[2] = ms.ld(rb, 2);
                if (MemStream::bad(a[0], a[9], a[1], a[2]) == 0 || sp.fail(c, 0x100 + s)) break;
            }
            ms.share(o0, 0); ms.share(o1, 1); ms.share(o2, 2);
            prep();
            sfor<64, 79>(slot);
            asm volatile("s_nop 7" : "+v"(acc[0]), "+v"(acc[1]), "+v"(acc[2]), "+v"(acc[3]), "+v"(acc[4]), "+v"(acc[5]));
        }
        MARK3(6);
        sfor<15, 60>(slot);
        fin(2, 3); fin(2, 4); fin(3, 3); fin(3, 4);
        asm volatile("s_nop 0");
        fin(4, 0); fin(4, 1); fin(5, 0); fin(5, 1); fin(4, 2); fin(5, 2); fin(4, 3); fin(4, 4); fin(5, 3); fin(5, 4);
        f32x4 am, a0, a1;
        MARK2(3);
        bar();
        MARK2(4);
        u32x4 qa[3], ka[3];
#pragma unroll
        for (int kb = 0; kb < 3; ++kb) { qa[kb] = af.get(kb * 1024); __builtin_amdgcn_sched_barrier(0); ka[kb] = af.get(3072 + kb * 1024); __builtin_amdgcn_sched_barrier(0); }
        asm volatile("" ::: "memory");
        if (wave >= 2) {
            const int Tq = wave & 1;
            have = min((unsigned)__builtin_amdgcn_readlane(qp, 0), (unsigned)__builtin_amdgcn_readlane(qp, 1));
            u32x4 qx[6];
#pragma unroll
            for (int ep = 0; ep < 2; ++ep) qx[ep] = lds128(lds + L_QX + lane * 16 + Tq * 1024 + ep * 2048);
            zero_acc(am);
#pragma unroll
            for (int ep = 0; ep < 6; ++ep) {
                if (ep < 4) qx[ep + 2] = lds128(lds + L_QX + lane * 16 + Tq * 1024 + (ep + 2) * 2048);
                mf_vv(a0, kxf[2 * ep], qx[ep], ep == 0); mf_vv(a1, kxf[2 * ep + 1], qx[ep], ep == 0);
                if (ep & 1) smf_v(am, ka[ep >> 1], cat4(qx[ep - 1], qx[ep]), idx);
            }
            mf_done(am, a0, a1);
            float v[10];
            constexpr float SC = 0.07216878364870322f, L2E = 1.4426950408889634f;
#pragma unroll
            for (int r = 0; r < 4; ++r) {
                v[r] = (4 * g + r <= 16 * Tq + n) ? a0[r] * SC : -1e9f;
                v[4 + r] = (16 + 4 * g + r <= 16 * Tq + n) ? a1[r] * SC : -1e9f;
            }
            v[8] = (am[0] + am[1]) * SC; v[9] = (am[2] + am[3]) * SC;
            float mx = v[0];
#pragma unroll
            for (int r = 1; r < 10; ++r) mx = fmaxf(mx, v[r]);
            mx = x16(mx, true); mx = x32(mx, true);
            float sum = 0.f;
#pragma unroll
            for (int r = 0; r < 10; ++r) { v[r] = __builtin_amdgcn_exp2f((v[r] - mx) * L2E); sum += v[r]; }
            sum = x16(sum, false); sum = x32(sum, false);
            const float inv = __builtin_amdgcn_rcpf(sum);
            u32x4 ox = {pka(v[0] * inv, v[1] * inv), pka(v[2] * inv, v[3] * inv), pka(v[4] * inv, v[5] * inv), pka(v[6] * inv, v[7] * inv)};
            unsigned om = pka(v[8] * inv, v[9] * inv);
            asm volatile("s_barrier" : "+v"(ox), "+v"(om) :: "memory");
            if (Tq) st128(ox, rw, so1, rb); else st64(u32x2{ox.x, ox.y}, rw, so1, rb);
            st32(om, rw, so2, rb);
            if (s + 1 < NSEG) stage_q(s + 1);
            if (s + KARM < NSEG) { const unsigned ra = rec(par, s + KARM); if (Tq) st128(SENT4, rw, so1, ra); else st64(SENT2, rw, so1, ra); st32(sent1(), rw, so2, ra); }
        } else if (wave == 1) {
            have = min((unsigned)__builtin_amdgcn_readlane(qp, 0), (unsigned)__builtin_amdgcn_readlane(qp, 1));
            if (s + 2 < NSEG) stage_k(s + 2);
            if (s == 0) nf = first_notes(rw, ms.vn, rb + amap(R_NF), sp);
            { const u32x2 pl = read_notes(nf); lfail[2] = pl.x; lfail[3] = pl.y; }
            asm volatile("s_waitcnt lgkmcnt(0)\n\ts_barrier" ::: "memory");
        } else {
            if (s < NSEG - 1) {
                a0 = f32x4{zero1(), zero1(), zero1(), zero1()}; a1 = f32x4{zero1(), zero1(), zero1(), zero1()};
                asm volatile("s_nop 0" : "+v"(a0), "+v"(a1));
                auto sx = [&](int kb, int T) __attribute__((always_inline)) { smf_v(T ? a1 : a0, qa[kb], cat4(kxf[4 * kb + T], kxf[4 * kb + 2 + T]), idx); };
                sx(0, 0); mf_vv(am, qa[0], ka[0], true); sx(0, 1); mf_vv(am, qa[1], ka[1], false); sx(1, 0); mf_vv(am, qa[2], ka[2], false);
                sx(1, 1); sx(2, 0); sx(2, 1);
                float v[6];
                asm volatile("s_nop 4\n\tv_add_f32_dpp %0, %3, %2 quad_perm:[1,1,3,3] row_mask:0xf bank_mask:0xf\n\tv_add_f32_dpp %1, %5, %4 quad_perm:[1,1,3,3] row_mask:0xf bank_mask:0xf"
                             : "=&v"(v[2]), "=&v"(v[5]) : "v"(am[0]), "v"(am[1]), "v"(am[2]), "v"(am[3]));
                asm volatile("v_add_f32 %0, %6, %7\n\tv_add_f32 %1, %8, %9\n\t"
                             "v_mov_b32_dpp %4, %4 quad_perm:[0,0,2,2] row_mask:0xf bank_mask:0xf\n\tv_mov_b32_dpp %5, %5 quad_perm:[0,0,2,2] row_mask:0xf bank_mask:0xf\n\t"
                             "v_add_f32 %2, %10, %11\n\tv_add_f32 %3, %12, %13"
                             : "=&v"(v[0]), "=&v"(v[3]), "=&v"(v[1]), "=&v"(v[4]), "+v"(v[2]), "+v"(v[5])
                             : "v"(a0[0]), "v"(a0[1]), "v"(a0[2]), "v"(a0[3]), "v"(a1[0]), "v"(a1[1]), "v"(a1[2]), "v"(a1[3]));
                float ma = fmax3(v[0], v[1], v[2]), mb = fmax3(v[3], v[4], v[5]);
                row_max2(ma, mb);
                ma *= SCALE_L2E; mb *= SCALE_L2E;
#pragma unroll
                for (int r = 0; r < 6; ++r) v[r] = __builtin_amdgcn_exp2f(__builtin_fmaf(v[r], SCALE_L2E, r < 3 ? -ma : -mb));
                float sa = __builtin_fmaf(v[2], 0.5f, v[0] + v[1]), sb = __builtin_fmaf(v[5], 0.5f, v[3] + v[4]);
                row_sum2(sa, sb);
                sa = __builtin_amdgcn_rcpf(sa); sb = __builtin_amdgcn_rcpf(sb);
                unsigned p0 = pka(v[0] * sa, v[3] * sb), p1 = pka(v[1] * sa, v[4] * sb), p2 = pka(v[2] * sa, v[5] * sb);
                asm volatile("s_nop 0" : "+v"(p0), "+v"(p1), "+v"(p2));
                st64(u32x2{pair_cols(p0, psel), pair_cols(p1, psel)}, rw, so1, rb);
                st32(__builtin_amdgcn_perm(p2, __builtin_amdgcn_update_dpp(0u, p2, 0x4E, 0xf, 0xf, true), psel2), rw, so2, rb);
                if (lane == 0) st32(dur, rw, h * 128, rb + amap(R_NS));
                tn = ck.now();
                lfail[1] = tn;
                asm volatile("s_waitcnt lgkmcnt(0)\n\ts_barrier" ::: "memory");
                if (s > 0 && __builtin_amdgcn_readfirstlane(*lfail) != s) dur = track(dur, tn - tg, s);
                SETV(5, tn + ck.org); SETV(6, tg + dur + ck.org);
                if (s + KARM < NSEG) { const unsigned ra = rec(par, s + KARM); st64(SENT2, rw, so1, ra); st32(sent1(), rw, so2, ra); if (lane == 0) st32(sent1(), rw, h * 128, ra + amap(R_NS)); }
            } else asm volatile("s_barrier" ::: "memory");
        }
        tn = mem_time(__builtin_amdgcn_readfirstlane(lfail[2]), __builtin_amdgcn_readfirstlane(lfail[3]), __builtin_amdgcn_readfirstlane(lfail[1]));
        FLUSH(s);
    }
    vmwait<0>();
}

constexpr int L_WO = 81920;
template <class SV, class SO>
DEV void load_vo(const Params& P, int h, LAS char* lds, int wave, int lane, Spin& sp, SV&& sv, SO&& so_, int rot = 0) {
    auto krow = [&](int k) { int kk = k + 2 * rot; if (kk >= 20) kk -= 20; return (unsigned)kk * 32u * 12288u; };
    WSrc<3> wv; wv.init((const char*)P.wv + h * 384 + wave * 96, lane, 12288u);
    const unsigned lo = (unsigned)(uintptr_t)lds + L_WO + wave * 20480, lv = (unsigned)(uintptr_t)lds + wave * 20480;
#pragma unroll
    for (int k = 0; k < 6; ++k) wv.issue(krow(k), lv + k * 3072);
    WSrc<10> wo; wo.init((const char*)P.wo + (size_t)h * E_ * 1280 + wave * 320, lane, 1280u);
    wo.issue(0, lo); wo.issue(32 * 1280, lo + 10240);
    WRd<3, false> rv; rv.init(lds + wave * 20480, lane);
    WRd<10, false> ro; ro.init(lds + L_WO + wave * 20480, lane);
#pragma unroll
    for (int k = 0; k < 20; ++k) {
        if (k == 1) { MARK(3); } if (k == 6) { MARK(4); } if (k == 12) { MARK(5); }
        if (k == 6 || k == 12) {
            const int ko = k / 3 - 2;
            vmwait<18>();
#pragma unroll
            for (int j = 0; j < 2; ++j) {
                u32x4 fo[10];
#pragma unroll
                for (int t = 0; t < 10; ++t) fo[t] = ro.get(j * 10240, t);
#pragma unroll
                for (int t = 0; t < 10; ++t) so_(t, ko + j, fo[t]);
            }
            lgkm0();
            wo.issue((ko + 2) * 32 * 1280, lo); wo.issue((ko + 3) * 32 * 1280, lo + 10240);
        }
        vmwait_n(k < 15 ? 35 : k < 18 ? 20 + (19 - k) * 3 : (19 - k) * 3);
        u32x4 fr[3];
#pragma unroll
        for (int t = 0; t < 3; ++t) fr[t] = rv.get((k % 6) * 3072, t);
#pragma unroll
        for (int t = 0; t < 3; ++t) sv(t, k, fr[t]);
        if (k + 6 < 20) { lgkm0(); wv.issue(krow(k + 6), lv + (k % 6) * 3072); }
    }
    vmwait<0>();
    MARK(2); FLUSH(128);
    bar();
}

template <class SV, class SO>
DEV void load_vo_rows(const Params& P, int h, LAS char* lds, int wave, int lane, Spin& sp, SV&& sv, SO&& so_) {
    WSrc<3> wv;
    wv.r = mk_rsrc((const char*)P.wv + h * 384 - 4096, 0xFFFFFFFFu);
#pragma unroll
    for (int i = 0; i < 3; ++i) {
        const int Pc = 64 * (3 * wave + i) + lane, rho = Pc / 24, cs = Pc % 24, tile = (cs >> 1) ^ img_rot<12>(rho);
        wv.voff[i] = (unsigned)swap23(rho) * 12288u + (unsigned)(2 * tile + (cs & 1)) * 16u + 4096u - i * 1024u;
    }
    const unsigned lo = (unsigned)(uintptr_t)lds + L_WO + wave * 20480, lv = (unsigned)(uintptr_t)lds + wave * 3072;
#pragma unroll
    for (int k = 0; k < 6; ++k) wv.issue(k * 32u * 12288u, lv + k * 12288);
    WSrc<10> wo; wo.init((const char*)P.wo + (size_t)h * E_ * 1280 + wave * 320, lane, 1280u);
    wo.issue(0, lo); wo.issue(32 * 1280, lo + 10240);
    LAS char* rv[3];
    { const int g = lane >> 4, q = (lane >> 2) & 3, p = lane & 3, rho = q | ((g & 1) << 2) | ((g >> 1) << 4);
#pragma unroll
      for (int t = 0; t < 3; ++t) rv[t] = opq(lds + rho * 384 + ((3 * wave + t) ^ img_rot<12>(rho)) * 32 + 8 * p); }
    WRd<10, false> ro; ro.init(lds + L_WO + wave * 20480, lane);
#pragma unroll
    for (int k = 0; k < 20; ++k) {
        if (k == 1) { MARK(3); } if (k == 6) { MARK(4); } if (k == 12) { MARK(5); }
        if (k == 6 || k == 12) {
            const int ko = k / 3 - 2;
            vmwait_n(k == 6 ? 15 : 18);
#pragma unroll
            for (int j = 0; j < 2; ++j) {
                u32x4 fo[10];
#pragma unroll
                for (int t = 0; t < 10; ++t) fo[t] = ro.get(j * 10240, t);
#pragma unroll
                for (int t = 0; t < 10; ++t) so_(t, ko + j, fo[t]);
            }
            lgkm0();
            wo.issue((ko + 2) * 32 * 1280, lo); wo.issue((ko + 3) * 32 * 1280, lo + 10240);
        }
        vmwait_n(k == 0 ? 35 : k == 11 ? 12 : k < 16 ? 32 : k == 16 ? 29 : (19 - k) * 3);
        bar();
        if (k >= 1 && k + 5 < 20) wv.issue((k + 5) * 32u * 12288u, lv + ((k + 5) % 6) * 12288);
        u32x4 fr[3];
#pragma unroll
        for (int t = 0; t < 3; ++t) fr[t] = cat2(trd(rv[t] + (k % 6) * 12288), trd(rv[t] + (k % 6) * 12288 + 3072));
#pragma unroll
        for (int t = 0; t < 3; ++t) sv(t, k, fr[t]);
    }
    vmwait<0>();
    MARK(2); FLUSH(128);
    bar();
}

DEV void role_UV(const Params& P, unsigned char* ws, int h, LAS char* lds, int wave, int lane, Spin& sp) {
    const rsrc_t rw = mk_rsrc(ws, (unsigned)WS_END);
    const int rot = MemStream::rot_of(h + 5, wave);
    u32x8 wv[3][10], wo[10][2];
    load_vo(P, h, lds, wave, lane, sp,
        [&](int t, int k, u32x4 f) { if (k & 1) { wv[t][k >> 1] = cat4(lo4(wv[t][k >> 1]), f); pin_a(wv[t][k >> 1]); } else wv[t][k >> 1] = cat4(f, f); },
        [&](int t, int k, u32x4 f) { if (k & 1) { wo[t][k >> 1] = cat4(lo4(wo[t][k >> 1]), f); if (k == 1 && t < 2) pin_a(wo[t][k >> 1]); } else wo[t][k >> 1] = cat4(f, f); }, rot);
    const unsigned par = load_par(rw, lane, sp);
    const int idx = (lane & 1) ? 0xEEEE : 0x4444;
    const int g = lane >> 4, n = lane & 15;
    MemStream ms; ms.init(rw, lds, lane, rot, wave == 0);
    volatile LAS unsigned* lfail = (volatile LAS unsigned*)(lds + 14336);
    if (wave == 0) *lfail = ~0u;
    bar();
    AFrag af; af.init(lds + 8192, lane);
    WRd<10, false> wr; wr.init(lds + L_WO + wave * 20480, lane);
    const unsigned uo = amap(R_UB) + (wave * 10 + (g & 1)) * 16384 + h * 256 + n * 16 + (g >> 1) * 8;
    LAS char* vw0 = opq(lds + 8192 + wave * 768 + AFrag::wofs(lane, wave & 1));
    LAS char* vw1 = opq(lds + 8192 + wave * 768 + AFrag::wofs(lane, ~wave & 1));
    auto lwq = [&](int t, int q) { return trd(wr.at((q >> 1) * 10240 + (q & 1) * 2560, t)); };
    Clock ck; ck.org = sp.org;
    unsigned dur = 135, tn = 0;
    for (int s = 0; s < NSEG; ++s) {
        const unsigned rb = rec(par, s);
        f32x4 acc[3], c[10];
        u32x2 st[10][4]; u32x4 a[10];
#pragma unroll
        for (int i = 0; i < 12; ++i) st[i >> 2][i & 3] = lwq(i >> 2, i & 3);
#pragma unroll
        for (int t = 0; t < 3; ++t) zero_acc(acc[t]);
#pragma unroll
        for (int t = 0; t < 4; ++t) zero_acc(c[t]);
        MARK(0); SETV(7, 0);
        const unsigned o0 = ms.so(rb, 0), o1 = ms.so(rb, 1), o2 = ms.so(rb, 2), o3 = ms.so(rb, 3);
        const unsigned tg = ck.wait_fine(tn);
        ms.lda(a[0], o0); ms.lda(a[1], o1); ms.lda(a[2], o2); ms.lda(a[3], o3);
        ms.share(o0, 0); ms.share(o1, 1); ms.share(o2, 2);
        SETV(1, tg + ck.org);
        unsigned long long bad = 0, bad2 = 0;
        auto slot = [&](auto I) __attribute__((always_inline)) {
            constexpr int i = decltype(I)::value % 64;
            constexpr bool fast = decltype(I)::value < 64;
            constexpr int kb = i < 4 ? i : i < 12 ? (i - 4) / 2 : i / 3, t = i < 4 ? 0 : i < 12 ? 1 + (i & 1) : i % 3;
            if constexpr (fast && i < 4) vmw<6 - i>(a[i]);
            if constexpr (i == 10) {
                asm volatile("s_waitcnt vmcnt(0)\n\ts_barrier" ::: "memory");
#pragma unroll
                for (int k = 4; k < 7; ++k) a[k] = ms.get(k);
            }
            smf_a(acc[t], a[kb], wv[t][kb], idx);
            if constexpr (i == 0) { MARK2(2); }
            if constexpr (i == 7) { asm volatile("s_nop 3"); bad = nan2(acc[0][0], acc[0][1]); bad2 = nan2(acc[0][2], acc[0][3]); }
            if constexpr (i >= 10 && i < 13) a[i - 3] = ms.get(i - 3);
            if constexpr (i >= 14 && i < 26) { constexpr int q = i - 14; c[4 + q / 4][q & 3] = zero1(); }
        };
        sfor<0, 10>(slot);
        if (__builtin_expect((bad | bad2) != 0, 0)) {
            asm volatile("s_nop 7\n\ts_waitcnt vmcnt(0)" ::: "memory");
            *lfail = (unsigned)s; SETV(7, 1);
            for (unsigned c = 0;;) {
#pragma unroll
                for (int k = 0; k < 4; ++k) a[k] = ms.ld(rb, k);
                if (MemStream::bad(a[0], a[1], a[2], a[3]) == 0 || sp.fail(c, 0x100 + s)) break;
            }
            ms.share(o0, 0); ms.share(o1, 1); ms.share(o2, 2);
#pragma unroll
            for (int t = 0; t < 3; ++t) zero_acc(acc[t]);
            sfor<64, 74>(slot);
            mf_done(acc[0], acc[1], acc[2]);
        }
        sfor<10, 30>(slot);
        asm volatile("s_nop 7" : "+v"(acc[0]), "+v"(acc[1]), "+v"(acc[2]), "+v"(c[4]), "+v"(c[5]), "+v"(c[6]));
#pragma unroll
        for (int t = 0; t < 3; ++t) { LAS char* p = t == 1 ? vw1 : vw0; float f0, f1; st2x16(p, p + 4, t * 256, fold_pk(acc[t], f0, f1)); }
#pragma unroll
        for (int t = 7; t < 10; ++t) { c[t] = f32x4{zero1(), zero1(), zero1(), zero1()}; pin_v(c[t]); }
        MARK2(3);
        bar();
        MARK2(4);
        u32x4 va[3];
#pragma unroll
        for (int kb = 0; kb < 3; ++kb) va[kb] = af.get(kb * 1024);
        float fa[10], fb[10]; unsigned o[10];
        sfor<0, 39>([&](auto I) __attribute__((always_inline)) {
            constexpr int i = decltype(I)::value, tp = i / 6, k = (i % 6) >> 1, t = 2 * tp + (i & 1);
            if constexpr (i < 30) {
                if constexpr (k == 0) { if constexpr (t < 2) smf_a(c[t], va[0], wo[t][0], idx); else smf_v(c[t], va[0], wo[t][0], idx); }
                else if constexpr (k == 1) smf_v(c[t], va[1], wo[t][1], idx);
                else smf_v(c[t], va[2], cat4(cat2(st[t][0], st[t][1]), cat2(st[t][2], st[t][3])), idx);
                constexpr int q0 = i < 3 ? 0 : ((i - 3) * 4) / 3, q1 = i < 3 ? 0 : ((i - 2) * 4) / 3;
                sfor<q0, q1>([&](auto Q) __attribute__((always_inline)) { constexpr int q = 12 + decltype(Q)::value; if constexpr (q < 40) st[q >> 2][q & 3] = lwq(q >> 2, q & 3); });
            }
            if constexpr (i >= 9) {
                constexpr int x = 2 * ((i - 9) / 6), y = x + 1, j = (i - 9) % 6;
                if constexpr (i == 33) asm volatile("s_nop 0");
                if constexpr (j == 0) { fa[x] = add1(c[x][0], c[x][1]); fb[x] = add1(c[x][2], c[x][3]); }
                if constexpr (j == 1) { fa[y] = add1(c[y][0], c[y][1]); fb[y] = add1(c[y][2], c[y][3]); }
                if constexpr (j == 2) { o[x] = pk1(fa[x], fb[x]); o[y] = pk1(fa[y], fb[y]); }
                if constexpr (j == 3) asm volatile("v_min_u32 %0, -2, %0\n\tv_min_u32 %1, -2, %1" : "+v"(o[x]), "+v"(o[y]));
                if constexpr (j == 4) asm volatile("s_nop 1\n\tv_permlane16_swap_b32 %0, %1" : "+v"(o[x]), "+v"(o[y]));
                if constexpr (j == 5) st64(u32x2{o[x], o[y]}, rw, uo, rb + x * 16384);
            }
        });
        if (wave == 0 && lane == 0) st32(dur, rw, (32 + h) * 128, rb + amap(R_NS));
        tn = ck.now();
        if (s > 0 && __builtin_amdgcn_readfirstlane(*lfail) != s) dur = track(dur, tn - tg, s);
        SETV(5, tn + ck.org);
        if (wave == 0) { const u32x2 pl = read_notes(s ? ld64(rw, ms.vn, rb + amap(R_NF)) : first_notes(rw, ms.vn, rb + amap(R_NF), sp)); lfail[1] = mem_time(pl.x, pl.y, tn); }
        bar();
        tn = __builtin_amdgcn_readfirstlane(lfail[1]);
        MARKC(6);
        if (s + KARM < NSEG) {
            const unsigned ra = rec(par, s + KARM);
#pragma unroll
            for (int tp = 0; tp < 5; ++tp) st64(SENT2, rw, uo, ra + tp * 32768);
            if (wave == 0 && lane == 0) st32(sent1(), rw, (32 + h) * 128, ra + amap(R_NS));
        }
        FLUSH(s);
    }
}

DEV void role_VC(const Params& P, unsigned char* ws, int h, int odd, LAS char* lds, int wave, int lane, Spin& sp) {
    constexpr int L_V = 40960;
    const rsrc_t rw = mk_rsrc(ws, (unsigned)WS_END);
    u32x4 wv[3][20], wo[10][4];
    load_vo_rows(P, h, lds, wave, lane, sp, [&](int t, int k, u32x4 f) { wv[t][k] = f; pin_a(wv[t][k]); }, [&](int t, int k, u32x4 f) { wo[t][k] = f; if (t < 2 && k < 2) pin_a(wo[t][k]); });
    const int g = lane >> 4, n = lane & 15;
    const char* xf = (const char*)ws + O_XF;
    WRd<10, false> wr; wr.init(lds + L_WO + wave * 20480, lane);
    LAS char* l16 = opq(lds + lane * 16);
    auto issue_x = [&](int s) {
#pragma unroll
        for (int i = 0; i < 10; ++i) glds<0>(lane * 16, xf + (size_t)s * 40960 + (wave * 10 + i) * 1024, (unsigned)(uintptr_t)lds + (wave * 10 + i) * 1024);
    };
    const unsigned par = load_par(rw, lane, sp);
    issue_x(odd);
    unsigned seen = 0;
    for (int s = odd; s < NSEG; s += 2) {
        MARK(0);
        vmwait<0>();
        bar();
        if (s >= 2 && wave == 0 && lane == 0) st32((unsigned)(s - 1), rw, (unsigned)(O_CTRL + 4 * (W_VCPROG + 32 * odd + h)), 0);
        MARK(1);
        f32x4 acc[3][2];
        u32x4 xb[40];
#pragma unroll
        for (int i = 0; i < 4; ++i) xb[i] = lds128(l16 + (i & 1) * 20480 + (i >> 1) * 1024);
#pragma unroll
        for (int i = 0; i < 40; ++i) {
#pragma unroll
            for (int t = 0; t < 3; ++t) {
                mf_av(acc[t][i & 1], wv[t][i >> 1], xb[i], i < 2);
                if (t == 0 && i + 4 < 40) xb[i + 4] = lds128(l16 + (i & 1) * 20480 + ((i + 4) >> 1) * 1024);
            }
        }
        mf_done(acc[0][0], acc[1][0], acc[2][0]); mf_done(acc[0][1], acc[1][1], acc[2][1]);
#pragma unroll
        for (int t = 0; t < 3; ++t)
#pragma unroll
            for (int T = 0; T < 2; ++T) {
                const int et = wave * 3 + t;
                const int off = (et >> 1) * 2048 + T * 1024 + ((2 * (et & 1) + (g >> 1)) * 16 + n) * 16 + (g & 1) * 8;
                *(LAS u32x2*)(lds + L_V + off) = u32x2{pk(acc[t][T][0], acc[t][T][1]), pk(acc[t][T][2], acc[t][T][3])};
            }
        MARK(2);
        wait_slot(rw, s, seen, h & 7, sp);
        bar();
        MARK(3);
        u32x4 va[6][2], st[10][2];
        f32x4 c[10][2];
#pragma unroll
        for (int k = 0; k < 6; ++k)
#pragma unroll
            for (int T = 0; T < 2; ++T) va[k][T] = lds128(l16 + L_V + k * 2048 + T * 1024);
        st[0][0] = wr.get(0, 0); st[0][1] = wr.get(10240, 0);
        const char* xn = xf + (size_t)(s + 2 < NSEG ? s + 2 : s) * 40960 + wave * 10240;
        const unsigned xd = (unsigned)(uintptr_t)lds + wave * 10240;
        auto fin = [&](int t) {
            st128(u32x4{pk(c[t][0][0], c[t][0][1]), pk(c[t][0][2], c[t][0][3]), pk(c[t][1][0], c[t][1][1]), pk(c[t][1][2], c[t][1][3])}, rw,
                  (h >> 3) * 16384 + (h & 7) * 1024 + lane * 16, (page(par, s, 2 + (wave >> 1)) ^ (CMIX ? ((h >> 3) & 1) << 13 : 0)) + ((wave & 1) * 10 + t) * 65536);
        };
#pragma unroll
        for (int t = 0; t < 10; ++t) {
#pragma unroll
            for (int k = 0; k < 6; ++k) {
                const u32x4 b = k < 4 ? wo[t][k & 3] : st[t][k & 1];
                if (t < 2 && k < 2) { mf_va(c[t][0], va[k][0], b, k == 0); mf_va(c[t][1], va[k][1], b, k == 0); }
                else { mf_vv(c[t][0], va[k][0], b, k == 0); mf_vv(c[t][1], va[k][1], b, k == 0); }
                if (k < 2 && t + 1 < 10) st[t + 1][k] = wr.get(k * 10240, t + 1);
                if (k == 5 && t < 5) { glds<0>(lane * 16, xn + 2 * t * 1024, xd + 2 * t * 1024); }
                if (k == 2 && t < 5) { glds<1024>(lane * 16, xn + 2 * t * 1024, xd + 2 * t * 1024); }
                if (k == 3 && t >= 1) { pin_v(c[t - 1][0]); pin_v(c[t - 1][1]); fin(t - 1); }
            }
        }
        mf_done(c[9][0], c[9][1]);
        fin(9);
        MARK(4); FLUSH(s);
    }
    vmwait<0>();
    bar();
    if (wave == 0 && lane == 0) st32((unsigned)NSEG, rw, (unsigned)(O_CTRL + 4 * (W_VCPROG + 32 * odd + h)), 0);
}

DEV void role_QK(const Params& P, unsigned char* ws, int cu, LAS char* lds, int wave, int lane, Spin& sp) {
    const rsrc_t rw = mk_rsrc(ws, (unsigned)WS_END);
    u32x4 w[4][20];
    const int sel = cu >= 24, c0 = (cu - 24 * sel) * 256 + wave * 64;
    stream_w<4, 20, 20, 10, 0, true>((const char*)(sel ? P.wk : P.wq) + c0 * 2, 12288u, lds, wave * 40960, lane,
                                 [&](int t, int k, u32x4 f) { w[t][k] = f; if (k < 16) pin_a(w[t][k]); });
    vmwait<0>();
    bar();
    const char* xf = (const char*)ws + O_XF;
    auto issue_x = [&](int s) {
#pragma unroll
        for (int i = 0; i < 10; ++i) glds<0>(lane * 16, xf + (size_t)s * 40960 + (wave * 10 + i) * 1024, (unsigned)(uintptr_t)lds + (s & 1) * 40960 + (wave * 10 + i) * 1024);
    };
    const unsigned par = load_par(rw, lane, sp);
    issue_x(0);
    const unsigned ol = (sel ? R_KX : R_QX) + (c0 >> 5) * 2048, ob = ((ol >> 13) << 14) | (ol & 8191u);
    unsigned seen = 0;
    for (int s = 0; s < NSEG; ++s) {
        MARK(0);
        vmwait<0>();
        bar();
        MARK(1);
        if (s >= 1 && wave == 0 && lane == 0) st32((unsigned)s, rw, (unsigned)(O_CTRL + 4 * (W_QKPROG + 32 * cu)), 0);
        wait_slot(rw, s, seen, cu & 7, sp);
        LAS char* l16 = opq(lds + (s & 1) * 40960 + lane * 16);
        f32x4 acc[4][2];
        u32x4 xb[40];
#pragma unroll
        for (int i = 0; i < 4; ++i) xb[i] = lds128(l16 + (i & 1) * 20480 + (i >> 1) * 1024);
        const char* xn = xf + (size_t)(s + 1 < NSEG ? s + 1 : s) * 40960 + wave * 10240;
        const unsigned xd = (unsigned)(uintptr_t)lds + (~s & 1) * 40960 + wave * 10240;
#pragma unroll
        for (int i = 0; i < 40; ++i)
#pragma unroll
            for (int t = 0; t < 4; ++t) {
                if (t == 2 && i % 3 == 0 && i < 30) glds<0>(lane * 16, xn + (i / 3) * 1024, xd + (i / 3) * 1024);
                if (i < 32) mf_av(acc[t][i & 1], w[t][i >> 1], xb[i], i < 2); else mf_vv(acc[t][i & 1], w[t][i >> 1], xb[i], false);
                if (t == 0 && i + 4 < 40) xb[i + 4] = lds128(l16 + (i & 1) * 20480 + ((i + 4) >> 1) * 1024);
            }
        mf_done(acc[0][0], acc[1][0], acc[2][0], acc[3][0]); mf_done(acc[0][1], acc[1][1], acc[2][1], acc[3][1]);
#pragma unroll
        for (int pr = 0; pr < 2; ++pr)
#pragma unroll
            for (int T = 0; T < 2; ++T) {
                const f32x4 a = acc[2 * pr][T], b = acc[2 * pr + 1][T];
                st128(u32x4{pk(a[0], a[1]), pk(a[2], a[3]), pk(b[0], b[1]), pk(b[2], b[3])}, rw, pr * 2048 + T * 1024 + lane * 16, page(par, s, 1) + ob);
            }
        MARK(2); MARKC(6); FLUSH(s);
    }
    vmwait<0>();
    bar();
    if (wave == 0 && lane == 0) st32((unsigned)NSEG, rw, (unsigned)(O_CTRL + 4 * (W_QKPROG + 32 * cu)), 0);
}

DEV void role_F(const Params& P, unsigned char* ws, int f, LAS char* lds, int wave, int lane, Spin& sp) {
    constexpr int L_FL = 131072, L_U = L_FL + 1024;
    const rsrc_t rw = mk_rsrc(ws, (unsigned)WS_END);
    volatile LAS unsigned* fl = (volatile LAS unsigned*)(lds + L_FL);
    if (wave == 0 && lane < 8) fl[lane] = lane == 5 ? ~0u : 0u;
    bar();
    const unsigned par = load_par(rw, lane, sp);
    const int g = lane >> 4, n = lane & 15;
    const unsigned l16 = lane * 16;
    const unsigned xr = ((f % 10) * 3) & 15;
    auto lwait = [&](int i, int need, unsigned code) {
        for (unsigned c = 0, k = 0; (int)__builtin_amdgcn_readfirstlane(fl[i]) < need;) { if ((++k & 63u) == 0u && sp.fail(c, code)) break; __builtin_amdgcn_s_sleep(LSLEEP); }
        asm volatile("" ::: "memory");
    };
    auto lset = [&](int i, int v) { asm volatile("s_waitcnt lgkmcnt(0)" ::: "memory"); if (lane == 0) fl[i] = (unsigned)v; };
    if (wave == 3) {
        unsigned have0 = 0, have1 = 0;
        const unsigned vf = min(lane, 39) * 128;
        for (int s = 0; s < NSEG; ++s) {
            MARK(0);
            unsigned hv = (s & 1) ? have1 : have0;
            for (unsigned c = 0; hv <= (unsigned)s;) {
                const unsigned v = ld32(rw, (lane & 31) * 4, (unsigned)(O_CTRL + 4 * (W_VCPROG + 32 * (s & 1))));
                if (!any_lane(v <= (unsigned)s)) {
                    hv = ~wave_max(~v);
                    break;
                }
                if (sp.fail(c, 0x300 + s)) break;
                __builtin_amdgcn_s_sleep(2);
            }
            if (s & 1) have1 = hv; else have0 = hv;
            lwait(2, s - 3, 0x341); lwait(3, s - 3, 0x342); lwait(1, s - 1, 0x340);
            MARK(1);
            const unsigned cpg = page(par, s, f < 20 ? 2 : 3) + (f % 20) * 65536;
            const unsigned no = rec(par, s - 1) + amap(R_NF);
            u32x2 nf;
            __builtin_amdgcn_s_sleep(4);
            asm volatile("buffer_load_dwordx2 %0, %1, %2, %3 offen sc1" : "=v"(nf) : "v"(vf), "s"(rw), "s"(no));
#pragma unroll
            for (int i = 0; i < 16; ++i) glds_dev<2, 1024>(l16, ws + ((cpg ^ (CMIX ? (((i ^ xr) >> 2) & 1) << 13 : 0)) + ((i ^ xr) >> 2) * 16384 + ((i ^ xr) & 3) * 2048), (unsigned)(uintptr_t)lds + (s & 3) * 32768 + i * 2048);
            asm volatile("s_waitcnt vmcnt(32)" : "+v"(nf));
            if (s >= 1) {
                for (unsigned c = 0; any_lane(max(nf.x, nf.y) == SENT);) { if (sp.fail(c, 0x390)) break; nf = ld64(rw, vf, no); }
                const u32x2 pl = read_notes(nf);
                if (lane == 0) { fl[6] = pl.x; fl[7] = pl.y; }
                lset(5, s - 1);
            }
            vmwait<0>();
            lset(0, s + 1);
            MARK(2); FLUSH(s);
        }
    } else if (wave == 0) {
        const int idx = (lane & 1) ? 0xEEEE : 0x4444;
        const unsigned mo = amap(R_MEM + f * 256) + (2 * (2 * g + (n & 1)) + ((n >> 1) & 1)) * 16 + (n >> 2) * 4;
        const unsigned psel = (n & 1) ? 0x07060302u : 0x01000504u;
        unsigned pv[4], ps[4];
#pragma unroll
        for (int j = 0; j < 4; ++j) { pv[j] = l16 + ((j ^ xr) & 3) * 1024; const unsigned k = j ^ (xr >> 2); ps[j] = amap(R_P) + (k & 1) * 4096 + (k >> 1) * 16384; }
        Clock ck; ck.org = sp.org;
        unsigned dur = 68, tp = 0, plan = 0, dS = 146, dU = 135;
        unsigned boost = 0, lead = 10;
        auto use_notes = [&](int s) __attribute__((always_inline)) {
            if (__builtin_amdgcn_readfirstlane(fl[5]) == s && s > 0) { tp = __builtin_amdgcn_readfirstlane(fl[6]); plan = __builtin_amdgcn_readfirstlane(fl[7]); }
        };
        __builtin_amdgcn_s_setprio(3);
        for (int s = 0; s < NSEG - 1; ++s) {
            MARK(0);
            lwait(0, s + 1, 0x343);
            if (s == 0) { tp = ck.now() + 20u; plan = tp + dur + 30u + DELTA_B; if (lane == 0) st64(u32x2{tp, dur + 30u}, rw, f * 128, rec(par, 0) + amap(R_NF)); }
            LAS char* cb = opq(lds + (s & 3) * 32768 + l16);
            u32x8 bc[16];
#pragma unroll
            for (int t = 0; t < 16; ++t) bc[t] = cat4(lds128(cb + 2 * t * 1024), lds128(cb + (2 * t + 1) * 1024));
#pragma unroll
            for (int t = 0; t < 16; ++t) pin_a(bc[t]);
            const unsigned rb = rec(par, s), rn = rec(par, s + 1), ub = rb + amap(R_UB) + f * 16384, pb = rb + amap(R_P);
            unsigned so4[4];
#pragma unroll
            for (int j = 0; j < 4; ++j) { so4[j] = rb + ps[j]; asm volatile("" : "+s"(so4[j])); }
            ck.wait_until(tp - lead - 20u);
            use_notes(s);
            const unsigned tu = tp - lead;
            float r0, r1;
            u32x4 u[8], px[16], pm[4];
            f32x4 d; zero_acc(d);
            MARK2(1);
            ck.wait_fine(tu);
#pragma unroll
            for (int i = 0; i < 8; ++i) u[i] = ld128(rw, l16 + (i & 3) * 1024, ub + (i >> 2) * 4096);
            const unsigned tg = ck.wait_fine(tp);
            SETV(3, tg + ck.org); SETV(4, tp + ck.org); SETV(5, plan - DELTA_B + ck.org); SETV(6, tu + ck.org);
            bool failed = false;
            for (unsigned c = 0;;) {
#pragma unroll
                for (int i = 0; i < 16; ++i) px[i] = ld128(rw, pv[i & 3], so4[i >> 2]);
#pragma unroll
                for (int i = 0; i < 4; ++i) pm[i] = ld128(rw, l16 + i * 1024, pb + 32768);
#pragma unroll
                for (int i = 0; i < 16; ++i) smf_a(d, px[i], bc[i], idx);
#pragma unroll
                for (int i = 0; i < 4; ++i) smf_v(d, pm[i], cat4(u[2 * i], u[2 * i + 1]), idx);
                mf_done(d);
                r0 = d[0] + d[1]; r1 = d[2] + d[3];
                if (__builtin_expect(!any_lane(__builtin_isunordered(r0, r1)), 1)) { SETV(7, c); break; }
                if (c > 0) {
                    unsigned sg = 0; bool miss = false;
#pragma unroll
                    for (int i = 0; i < 8; ++i) miss |= mx4(u[i]) == SENT;
#pragma unroll
                    for (int i = 0; i < 16; ++i) sg |= px[i].x | px[i].y | px[i].z | px[i].w;
#pragma unroll
                    for (int i = 0; i < 4; ++i) sg |= pm[i].x | pm[i].y | pm[i].z | pm[i].w;
                    if (!any_lane(miss | ((sg & 0x80008000u) != 0u))) break;
                }
                if (sp.fail(c, 0x400 + s)) break;
                failed = true;
                zero_acc(d);
#pragma unroll
                for (int i = 0; i < 8; ++i) u[i] = ld128(rw, l16 + (i & 3) * 1024, ub + (i >> 2) * 4096);
            }
            st32(nosent(pair_cols(pk(r0, r1), psel)), rw, mo, rn);
            const unsigned ns = ld32(rw, lane * 128, rb + amap(R_NS));
            const unsigned n5 = fl[5], n7 = fl[7];
            const unsigned tpub = ck.now();
            if (__builtin_amdgcn_readfirstlane(n5) == s) plan = __builtin_amdgcn_readfirstlane(n7);
            const unsigned prop = (tpub + VIS <= plan ? plan : tpub + LATE) + max(dS, dU + 8u) + DELTA_A + (failed ? boost : 0u) + (s < 2 ? EARLY : 0u);
            boost = failed ? min(boost + 40u, 400u) : 0u;
            if (lane == 0) { st64(u32x2{prop, dur + (s < 2 ? EARLY / 3 : 0u)}, rw, f * 128, rn + amap(R_NF)); fl[1] = (unsigned)(s + 1); }
            if (!failed && tg - tp < 15u) dur = track(dur, tpub - tp, s);
            SETV(2, tpub + ck.org);
            lwait(2, s - 1, 0x346); lwait(3, s - 1, 0x347);
#pragma unroll
            for (int i = 0; i < 8; ++i) *(LAS u32x4*)(lds + L_U + (s & 1) * 8192 + i * 1024 + l16) = u[i];
            lset(4, s + 1);
            tp = prop; plan = prop + dur + DELTA_B; lead = max(dS, dU + 8u) - dU;
            if (!any_lane(ns == SENT)) { const unsigned m = half_max(min(ns, 400u)); dS = __builtin_amdgcn_readlane(m, 0); dU = __builtin_amdgcn_readlane(m, 32); }
            if (f == 0 && lane < 8) st32((unsigned)(s + 1), rw, lane * 128, (unsigned)(O_CTRL + 4 * W_PROG));
            if (s + KARM < NSEG) { const unsigned ra = rec(par, s + KARM); st32(sent1(), rw, mo, ra); if (lane == 0) st64(SENT2, rw, f * 128, ra + amap(R_NF)); }
            FLUSH(s);
        }
        {
            const unsigned ub = rec(par, NSEG - 1) + amap(R_UB) + f * 16384;
            u32x4 u[8];
            ck.wait_until(tp);
            lset(1, NSEG);
            for (unsigned c = 0;;) {
                unsigned m = 0;
#pragma unroll
                for (int i = 0; i < 8; ++i) u[i] = ld128(rw, l16 + (i & 3) * 1024, ub + (i >> 2) * 4096);
#pragma unroll
                for (int i = 0; i < 8; ++i) m = max(m, mx4(u[i]));
                if (!any_lane(m == SENT) || sp.fail(c, 0x4FF)) break;
            }
            lwait(2, NSEG - 2, 0x346); lwait(3, NSEG - 2, 0x347);
#pragma unroll
            for (int i = 0; i < 8; ++i) *(LAS u32x4*)(lds + L_U + ((NSEG - 1) & 1) * 8192 + i * 1024 + l16) = u[i];
            lset(4, NSEG);
        }
    } else {
        auto xrows = [&](auto TQ) __attribute__((always_inline)) {
            constexpr int Tq = decltype(TQ)::value, NX = Tq ? 32 : 16;
            const rsrc_t rx = mk_rsrc(P.x, S_ * D_ * 2);
            const unsigned ho = (f >> 1) * 1024 + ((2 * (f & 1) + (g >> 1)) * 16 + n) * 16 + (g & 1) * 8;
            for (int s = 0; s < NSEG; ++s) {
                MARK(0);
                lwait(0, s + 1, 0x345);
                LAS char* cb = opq(lds + (s & 3) * 32768 + l16);
                u32x4 ca[NX];
#pragma unroll
                for (int i = 0; i < NX; ++i) ca[i] = Tq ? lds128(cb + i * 1024) : cat2(*(LAS const u32x2*)(cb + i * 2048), *(LAS const u32x2*)(cb + i * 2048 + 1024));
#pragma unroll
                for (int i = 0; i < NX; ++i) pin_a(ca[i]);
                const unsigned pb = rec(par, s) + amap(R_PX) + Tq * amap(PX_B);
                lwait(1, s + 1, 0x344);
                MARK2(1);
                f32x4 d; u32x2 xv;
                for (unsigned c = 0;;) {
                    u32x4 u[8], px[NX + 8];
#pragma unroll
                    for (int i = 0; i < NX; ++i) {
                        if (Tq) { const unsigned k = (i >> 1) ^ xr; px[i] = ld128(rw, l16 + (i & 1) * 1024, pb + (k & 3) * 2048 + (k >> 2) * 16384); }
                        else { const unsigned k = i ^ xr; px[i] = ld128(rw, l16, pb + (k & 7) * 1024 + (k >> 3) * 16384); }
                    }
#pragma unroll
                    for (int i = 0; i < 8; ++i) px[NX + i] = ld128(rw, l16 + (i & 3) * 1024, pb + 65536 + (i >> 2) * 4096);
                    xv = __builtin_amdgcn_raw_buffer_load_b64(rx, ((32 * s + 16 * Tq + n) * D_ + 16 * f + 4 * g) * 2, 0, 0);
                    lwait(4, s + 1, 0x348);
#pragma unroll
                    for (int i = 0; i < 8; ++i) u[i] = lds128(lds + L_U + (s & 1) * 8192 + i * 1024 + l16);
#pragma unroll
                    for (int i = 0; i < NX; ++i) mf_av(d, ca[i], px[i], i == 0);
#pragma unroll
                    for (int i = 0; i < 8; ++i) mf_vv(d, u[i], px[NX + i], false);
                    mf_done(d);
                    if (!any_lane(__builtin_isunordered(d[0] + d[1], d[2] + d[3]))) { SETV(7, c); break; }
                    bool miss = false;
#pragma unroll
                    for (int i = 0; i < NX + 8; ++i) miss |= mx4(px[i]) == SENT;
                    if (!any_lane(miss)) break;
                    if (sp.fail(c, 0x500 + s)) break;
                }
                MARK(2);
                lset(2 + Tq, s + 1);
                const unsigned a01 = pk(d[0], d[1]), a23 = pk(d[2], d[3]);
                st64(u32x2{pks(bflo(xv.x) + bflo(a01), bfhi(xv.x) + bfhi(a01)), pks(bflo(xv.y) + bflo(a23), bfhi(xv.y) + bfhi(a23))}, rw, ho, (unsigned)(O_HF + (size_t)(2 * s + Tq) * XF_B));
                if (2 * (s + KARM) < UTAIL) st64(SENT2, rw, ho, (unsigned)(O_HF + (size_t)(2 * (s + KARM) + Tq) * XF_B));
                MARK(3); FLUSH(s);
            }
        };
        if (wave == 1) xrows(IC<0>{}); else xrows(IC<1>{});
    }
}

DEV float gelu_t(float x) {
    const float t = x * __builtin_fmaf(x * x, -0.10294324f, -2.30220820f);
    return x * __builtin_amdgcn_rcpf(1.f + __builtin_amdgcn_exp2f(t));
}

template <int NK> DEV bool any_sent(const u32x4 (&x)[NK]) {
    unsigned m = 0;
#pragma unroll
    for (int k = 0; k < NK; ++k) m = max(m, mx4(x[k]));
    return any_lane(m == SENT);
}
template <int U0> DEV int next_unit(int u) { return u < U0 ? u + 1 : u + 4; }
template <int MODE>
DEV void role_W(const Params& P, unsigned char* ws, int cb, int rep, LAS char* lds, int wave, int lane, Spin& sp) {
    constexpr int U0 = MODE == 1 ? U0_1 : MODE == 2 ? U0_2 : U0_3;
    const rsrc_t rw = mk_rsrc(ws, (unsigned)WS_END);
    constexpr unsigned stride = MODE == 3 ? F_ * 2 : D_ * 2;
    const char* wsrc = (const char*)(MODE == 1 ? P.w1 : MODE == 2 ? P.w2 : P.w3) + (size_t)wave * 160 * stride + cb * 256;
    u32x4 w[8][5];
    stream_w<8, 5, 5, 5, 0, true>(wsrc, stride, lds, wave * 40960, lane, [&](int t, int k, u32x4 f) { w[t][k] = f; pin_a(w[t][k]); }, 0, 2 * wave + 2);
    vmwait<0>();
    bar();
    const int g = lane >> 4, n = lane & 15;
    const unsigned l16 = lane * 16;
    if (rep == 0) load_par<false>(rw, lane, sp);
    constexpr size_t O_IN = MODE == 1 ? O_HF : MODE == 2 ? O_GF : O_X1F;
    LAS char* wr[3];
#pragma unroll
    for (int j = 0; j < 3; ++j) wr[j] = opq(lds + ((wave + 1 + j) & 3) * 6144 + j * 2048 + l16);
    LAS char* rd = opq(lds + wave * 6144 + l16);
    LAS char* sw = opq(lds + 24576 + wave * 512 + lane * 8);
    LAS char* sr = opq(lds + 24576 + ((n >> 2) * 16 + n) * 8);
    u32x4 ones = {0x3F803F80u, 0x3F803F80u, 0x3F803F80u, 0x3F803F80u}; pin_v(ones);
    auto fetch1 = [&](int u, int k) __attribute__((always_inline)) { return ld128(rw, l16, (unsigned)(O_IN + (size_t)u * XF_B) + (wave * 5 + k) * 1024); };
    auto probe = [&](int u) __attribute__((always_inline)) { return ld32(rw, 1008, (unsigned)(O_IN + (size_t)u * XF_B) + (wave * 5 + 4) * 1024); };
    auto product = [&](const u32x4 (&xb)[5], auto&& side, int buf, f32x4& o0, f32x4& o1, float& S1, float& S2) __attribute__((always_inline)) {
        f32x4 acc[8], s1, s2;
        if (MODE != 2) {
#pragma unroll
            for (int k = 0; k < 5; ++k) { mf_vv(s1, ones, xb[k], k == 0); mf_vv(s2, xb[k], xb[k], k == 0); }
        }
        sfor<0, 40>([&](auto I) __attribute__((always_inline)) {
            constexpr int i = decltype(I)::value, t = 2 * (i / 10) + (i & 1), k = (i % 10) >> 1;
            mf_av(acc[t], w[t][k], xb[k], k == 0);
            if constexpr (i % 4 == 0) side(IC<i / 4>{});
            if constexpr (i >= 17 && (i % 10 == 7 || i % 10 == 8)) { constexpr int tw = 2 * (i / 10 - 1) + (i % 10 - 7); pin_v(acc[tw]); *(LAS f32x4*)(wr[tw >> 1] + buf * 28672 + (tw & 1) * 1024) = acc[tw]; }
            if constexpr (MODE != 2 && i == 20) {
                asm volatile("" : "+v"(s1), "+v"(s2));
                const float d = (n & 2) ? ((n & 1) ? s2[3] : s2[2]) : ((n & 1) ? s2[1] : s2[0]);
                *(LAS f32x2*)(sw + buf * 28672) = f32x2{s1[0], d};
            }
        });
        bar();
        f32x4 p[6]; f32x2 q[4];
#pragma unroll
        for (int j = 0; j < 6; ++j) p[j] = *(LAS const f32x4*)(rd + buf * 28672 + j * 1024);
        if (MODE != 2) {
#pragma unroll
            for (int j = 0; j < 4; ++j) q[j] = *(LAS const f32x2*)(sr + buf * 28672 + j * 512);
        }
        mf_done(acc[6], acc[7]);
        o0 = (acc[6] + p[0]) + (p[2] + p[4]); o1 = (acc[7] + p[1]) + (p[3] + p[5]);
        if (MODE != 2) { S1 = (q[0].x + q[1].x) + (q[2].x + q[3].x); S2 = (q[0].y + q[1].y) + (q[2].y + q[3].y); }
    };
    f32x4 cs0 = {}, cs1 = {};
    if (MODE != 2) {
        u32x4 o4[5] = {ones, ones, ones, ones, ones};
        asm volatile("s_nop 1" : "+v"(o4[0]), "+v"(o4[1]), "+v"(o4[2]), "+v"(o4[3]), "+v"(o4[4]));
        float a, b; product(o4, [](auto) {}, 1, cs0, cs1, a, b);
    }
    const int kso = 4 * cb + wave;
    const int grp = kso / 5, sh = grp == 0 ? 1 : grp == 1 ? -1 : grp == 2 ? 2 : -2;
    if (MODE == 1 && rep == 0) {
        const int td = sh > 0 ? n : S_ - 1 - n;
        if (n < (sh > 0 ? sh : -sh)) st128(u32x4{0u, 0u, 0u, 0u}, rw, (td >> 4) * (unsigned)XF_B + kso * 1024 + (g * 16 + (td & 15)) * 16, (unsigned)O_GF);
        vmwait<0>();
    }
    u32x4 xa[5], xc[5], hn; u32x2 sn;
    bool miss = true, ahead = false;
    auto extra = [&](int u) __attribute__((always_inline)) { if (MODE == 2) { hn = ld128(rw, l16, (unsigned)(O_HF + (size_t)u * XF_B) + kso * 1024); sn = ld64(rw, (16 * u + n) * 8, (unsigned)O_ST); } };
    auto unit = [&](int u, int buf, u32x4 (&xb)[5], u32x4 (&nx)[5]) __attribute__((always_inline)) {
        MARK(0);
        const int tok = 16 * u + n, un = next_unit<U0>(u), u2 = next_unit<U0>(un);
        for (unsigned c = 0; miss;) {
            while (__builtin_amdgcn_readfirstlane(probe(u)) == SENT) { if (sp.fail(c, 0x600 + MODE * 0x100 + u)) break; __builtin_amdgcn_s_sleep(PSLEEP); }
            __builtin_amdgcn_s_sleep(PWAIT);
#pragma unroll
            for (int k = 0; k < 5; ++k) xb[k] = fetch1(u, k);
            extra(u);
            miss = any_sent(xb);
            if (miss && sp.fail(c, 0x680 + u)) break;
        }
        MARK(1);
        asm volatile("s_nop 1" : "+v"(xb[0]), "+v"(xb[1]), "+v"(xb[2]), "+v"(xb[3]), "+v"(xb[4]));
        const bool early = ahead;
        unsigned pr = SENT;
        if ((early ? u2 : un) < NUNIT) pr = probe(early ? u2 : un);
        u32x4 hv = hn; u32x2 stt = sn;
        if (MODE == 2) asm volatile("" : "+v"(hv), "+v"(stt));
        f32x4 o0, o1; float S1, S2;
        product(xb, [&](auto J) __attribute__((always_inline)) {
            constexpr int j = decltype(J)::value;
            if constexpr (j < 5) { if (early) nx[j] = fetch1(un, j); }
            if constexpr (j == 5) { if (early) extra(un); }
        }, buf, o0, o1, S1, S2);
        if (!early) {
            ahead = __builtin_amdgcn_readfirstlane(pr) != SENT;
            if (ahead) {
#pragma unroll
                for (int k = 0; k < 5; ++k) nx[k] = fetch1(un, k);
                extra(un);
                pr = SENT;
                if (u2 < NUNIT) pr = probe(u2);
            }
        }
        auto settle = [&]() __attribute__((always_inline)) { __builtin_amdgcn_s_waitcnt(0x0F70); miss = !ahead || any_sent(nx); ahead = ahead && __builtin_amdgcn_readfirstlane(pr) != SENT; if (MODE == 2) asm volatile("" : "+v"(hn), "+v"(sn)); };
        MARK(2);
        if (MODE != 2) {
            const float mu = S1 * (1.f / D_), var = fmaxf(S2 * (1.f / D_) - mu * mu, 0.f);
            float al = __builtin_amdgcn_rsqf(var + 1e-5f);
            if (MODE == 1) {
                if (cb == 0 && wave == 0 && g == 0) st64(u32x2{nosent(f2u(mu)), nosent(f2u(al))}, rw, tok * 8, (unsigned)O_ST);
                al *= __builtin_amdgcn_rsqf(var * al * al + 1e-5f);
            }
            float y[8];
#pragma unroll
            for (int r = 0; r < 4; ++r) { y[r] = gelu_t(al * (o0[r] - mu * cs0[r])); y[4 + r] = gelu_t(al * (o1[r] - mu * cs1[r])); }
            u32x4 o = {pks(y[0], y[1]), pks(y[2], y[3]), pks(y[4], y[5]), pks(y[6], y[7])};
            pin_v(o);
            settle();
            if (MODE == 1) {
                const int td = tok + sh;
                const unsigned vo = (td >> 4) * (int)XF_B + kso * 1024 + (g * 16 + (td & 15)) * 16;
                if (td >= 0 && td < S_) st128(o, rw, vo, (unsigned)O_GF);
                if (td + 32 * KARM < 16 * UTAIL) st128(SENT4, rw, vo + 2 * KARM * (unsigned)XF_B, (unsigned)O_GF);
            } else {
                st128(o, rw, l16, (unsigned)(O_G3F + (size_t)u * G3_B) + kso * 1024);
                if (u + 2 * KARM < UTAIL) st128(SENT4, rw, l16, (unsigned)(O_G3F + (size_t)(u + 2 * KARM) * G3_B) + kso * 1024);
            }
        } else {
            for (unsigned c = 0; any_lane(max(mx4(hv), max(stt.x, stt.y)) == SENT);) {
                if (sp.fail(c, 0xA00 + u)) break;
                hv = ld128(rw, l16, (unsigned)(O_HF + (size_t)u * XF_B) + kso * 1024); stt = ld64(rw, tok * 8, (unsigned)O_ST);
            }
            const float mu = u2f(stt.x), al = u2f(stt.y);
            const u32x4 o = {pks((bflo(hv.x) - mu) * al + o0[0], (bfhi(hv.x) - mu) * al + o0[1]), pks((bflo(hv.y) - mu) * al + o0[2], (bfhi(hv.y) - mu) * al + o0[3]),
                             pks((bflo(hv.z) - mu) * al + o1[0], (bfhi(hv.z) - mu) * al + o1[1]), pks((bflo(hv.w) - mu) * al + o1[2], (bfhi(hv.w) - mu) * al + o1[3])};
            settle();
            st128(o, rw, l16, (unsigned)(O_X1F + (size_t)u * XF_B) + kso * 1024);
            if (u + 2 * KARM < UTAIL) st128(SENT4, rw, l16, (unsigned)(O_X1F + (size_t)(u + 2 * KARM) * XF_B) + kso * 1024);
        }
        MARK(3); FLUSH(u);
    };
    for (int u = rep ? U0 + rep : 0;;) {
        unit(u, 0, xa, xc); u = next_unit<U0>(u); if (u >= NUNIT) break;
        unit(u, 1, xc, xa); u = next_unit<U0>(u); if (u >= NUNIT) break;
    }
}

DEV void role_W4(const Params& P, unsigned char* ws, int cb, int rep, LAS char* lds, int wave, int lane, Spin& sp) {
    constexpr int U0 = U0_4;
    const rsrc_t rw = mk_rsrc(ws, (unsigned)WS_END);
    const rsrc_t ro = mk_rsrc(P.out, S_ * D_ * 2);
    u32x4 w[4][12];
    stream_w<4, 12, 12, 10, 0, false>((const char*)P.w4 + (size_t)wave * 384 * 1280 + cb * 128, 1280u, lds, wave * 40960, lane,
                                  [&](int t, int k, u32x4 f) { w[t][k] = f; pin_a(w[t][k]); }, 0, wave + 1);
    vmwait<0>();
    bar();
    const int g = lane >> 4, n = lane & 15;
    const unsigned l16 = lane * 16;
    if (rep == 0) load_par<false>(rw, lane, sp);
    const int ct = 4 * cb + wave;
    const unsigned po = (ct >> 1) * 1024 + ((2 * (ct & 1) + (g >> 1)) * 16 + n) * 16 + (g & 1) * 8;
    LAS char* wr[3];
#pragma unroll
    for (int j = 0; j < 3; ++j) wr[j] = opq(lds + ((wave + 1 + j) & 3) * 3072 + j * 1024 + l16);
    LAS char* rd = opq(lds + wave * 3072 + l16);
    auto fetch = [&](u32x4 (&x)[12], int u) __attribute__((always_inline)) {
#pragma unroll
        for (int k = 0; k < 12; ++k) x[k] = ld128(rw, l16 + (k & 3) * 1024, (unsigned)(O_G3F + (size_t)u * G3_B) + (wave * 12 + (k & ~3)) * 1024);
    };
    auto probe = [&](int u) __attribute__((always_inline)) { return ld32(rw, 1008, (unsigned)(O_G3F + (size_t)u * G3_B) + (wave * 12 + 11) * 1024); };
    u32x4 xa[12], xc[12]; u32x2 hn, xn;
    bool miss = true, ahead = false;
    auto extra = [&](int u) __attribute__((always_inline)) { hn = ld64(rw, po, (unsigned)(O_HF + (size_t)u * XF_B)); xn = ld64(rw, po, (unsigned)(O_X1F + (size_t)u * XF_B)); };
    auto unit = [&](int u, int buf, u32x4 (&xb)[12], u32x4 (&nx)[12]) __attribute__((always_inline)) {
        MARK(0);
        for (unsigned c = 0; miss;) {
            while (__builtin_amdgcn_readfirstlane(probe(u)) == SENT) { if (sp.fail(c, 0xB00 + u)) break; __builtin_amdgcn_s_sleep(PSLEEP); }
            __builtin_amdgcn_s_sleep(PWAIT);
            fetch(xb, u); extra(u);
            miss = any_sent(xb);
            if (miss && sp.fail(c, 0xB80 + u)) break;
        }
        MARK(1);
#pragma unroll
        for (int k = 0; k < 12; ++k) pin_v(xb[k]);
        asm volatile("s_nop 1");
        u32x2 hv = hn, xv = xn;
        const int un = next_unit<U0>(u), u2 = next_unit<U0>(un);
        unsigned pr = SENT;
        const bool early = ahead;
        if ((early ? u2 : un) < NUNIT) pr = probe(early ? u2 : un);
        asm volatile("" : "+v"(hv), "+v"(xv));
        f32x4 acc[4];
        sfor<0, 48>([&](auto I) __attribute__((always_inline)) {
            constexpr int i = decltype(I)::value, t = i / 12, k = i % 12;
            mf_av(acc[t], w[t][k], xb[k], k == 0);
            if constexpr (i % 3 == 0 && i < 36) { constexpr int kk = i / 3; if (early) nx[kk] = ld128(rw, l16 + (kk & 3) * 1024, (unsigned)(O_G3F + (size_t)un * G3_B) + (wave * 12 + (kk & ~3)) * 1024); }
            if constexpr (i == 35) { if (early) extra(un); }
            if constexpr (i == 36) { if (!early) { ahead = __builtin_amdgcn_readfirstlane(pr) != SENT; if (ahead) { fetch(nx, un); extra(un); pr = SENT; if (u2 < NUNIT) pr = probe(u2); } } }
            if constexpr (t >= 1 && k == 8) { pin_v(acc[t - 1]); *(LAS f32x4*)(wr[t - 1] + buf * 12288) = acc[t - 1]; }
        });
        bar();
        f32x4 p[3];
#pragma unroll
        for (int j = 0; j < 3; ++j) p[j] = *(LAS const f32x4*)(rd + buf * 12288 + j * 1024);
        mf_done(acc[3]);
        const f32x4 o = (acc[3] + p[0]) + (p[1] + p[2]);
        MARK(2);
        for (unsigned c = 0; any_lane(max(max(hv.x, hv.y), max(xv.x, xv.y)) == SENT);) {
            if (sp.fail(c, 0xC00 + u)) break;
            hv = ld64(rw, po, (unsigned)(O_HF + (size_t)u * XF_B)); xv = ld64(rw, po, (unsigned)(O_X1F + (size_t)u * XF_B));
        }
        const u32x2 r = {pk(bflo(hv.x) + (bflo(xv.x) + o[0]), bfhi(hv.x) + (bfhi(xv.x) + o[1])), pk(bflo(hv.y) + (bflo(xv.y) + o[2]), bfhi(hv.y) + (bfhi(xv.y) + o[3]))};
        __builtin_amdgcn_s_waitcnt(0x0F70);
        miss = !ahead || any_sent(nx); ahead = ahead && __builtin_amdgcn_readfirstlane(pr) != SENT;
        asm volatile("" : "+v"(hn), "+v"(xn));
        __builtin_amdgcn_raw_buffer_store_b64(r, ro, ((16 * u + n) * D_ + 16 * ct + 4 * g) * 2, 0, 0);
        MARK(3); FLUSH(u);
    };
    for (int u = rep ? U0 + rep : 0;;) {
        unit(u, 0, xa, xc); u = next_unit<U0>(u); if (u >= NUNIT) break;
        unit(u, 1, xc, xa); u = next_unit<U0>(u); if (u >= NUNIT) break;
    }
}

DEV unsigned char* ws_base(const Params& P) { return P.ws + ((0u - (unsigned)(uintptr_t)P.ws) & ((2u << 20) - 1u)); }

DEV void publish_ready(rsrc_t rw, int i, unsigned near, int tid, Spin& sp) {
    sp.start();
    vmwait<0>();
    bar();
    if (tid == 0) st128(u32x4{sp.tag, near, rawnow() - 3000u, ~sp.tag}, rw, i * 128, (unsigned)(O_CTRL + 4 * W_READY));
}
DEV void init_F(const Params& P, unsigned char* ws, int f, unsigned near, LAS char* lds, int wave, int lane, Spin& sp) {
    const rsrc_t rw = mk_rsrc(ws, (unsigned)WS_END);
    const int tid = wave * 64 + lane;
    LAS float* red = (LAS float*)lds;
    const int r = tid & 15, pc = tid >> 4;
    const rsrc_t rx = mk_rsrc(P.x, S_ * D_ * 2);
    u32x4 v[4][5];
#pragma unroll
    for (int j = 0; j < 4; ++j)
#pragma unroll
        for (int i = 0; i < 5; ++i) v[j][i] = __builtin_amdgcn_raw_buffer_load_b128(rx, (r * D_ + (pc + 16 * i) * 8) * 2, min(f + 40 * j, NUNIT - 1) * (16 * D_ * 2), 0);
    if (f < 3) {
        const int p = f * 256 + tid;
        if (p < 640) {
            const unsigned* src = (const unsigned*)(P.mem0 + ((p >> 1) & 7) * D_ + (p >> 4) * 16 + (p & 1) * 2);
            const u32x4 m = {nosent(src[0]), nosent(src[2]), nosent(src[4]), nosent(src[6])};
            const unsigned o = p * 16, oa = ((o >> 13) << 14) | (o & 8191u);
            st128(m, rw, oa, 0); st128(m, rw, oa, 8192);
        }
    }
    float mu[4];
#pragma unroll
    for (int j = 0; j < 4; ++j) {
        float s1 = 0.f;
#pragma unroll
        for (int i = 0; i < 5; ++i)
#pragma unroll
            for (int k = 0; k < 4; ++k) s1 += bflo(v[j][i][k]) + bfhi(v[j][i][k]);
        s1 = x32(x16(s1, false), false);
        red[(j * 2) * 64 + wave * 16 + r] = s1;
    }
    bar();
#pragma unroll
    for (int j = 0; j < 4; ++j) {
        const LAS float* q = red + (j * 2) * 64 + r;
        mu[j] = ((q[0] + q[16]) + (q[32] + q[48])) * (1.f / D_);
        float s2 = 0.f;
#pragma unroll
        for (int i = 0; i < 5; ++i)
#pragma unroll
            for (int k = 0; k < 4; ++k) { const float a = bflo(v[j][i][k]) - mu[j], c = bfhi(v[j][i][k]) - mu[j]; s2 += a * a + c * c; }
        s2 = x32(x16(s2, false), false);
        red[(j * 2 + 1) * 64 + wave * 16 + r] = s2;
    }
    bar();
#pragma unroll
    for (int j = 0; j < 4; ++j) {
        const LAS float* q = red + (j * 2 + 1) * 64 + r;
        const float rs = __builtin_amdgcn_rsqf(((q[0] + q[16]) + (q[32] + q[48])) * (1.f / D_) + 1e-5f);
        if (f + 40 * j < NUNIT) {
#pragma unroll
            for (int i = 0; i < 5; ++i) {
                u32x4 o;
#pragma unroll
                for (int k = 0; k < 4; ++k) o[k] = pk((bflo(v[j][i][k]) - mu[j]) * rs, (bfhi(v[j][i][k]) - mu[j]) * rs);
                st128(o, rw, (pc + 16 * i) * 256 + r * 16, (unsigned)(O_XF + (size_t)(f + 40 * j) * XF_B));
            }
        }
    }
    publish_ready(rw, f, near, tid, sp);
    bar();
}
DEV void init_arm(unsigned char* ws, int i, int tid, Spin& sp) {
    const rsrc_t rw = mk_rsrc(ws, (unsigned)WS_END);
    const int q = i * 256 + tid;
    constexpr int NT = 40 * 256;
    const u32x4 sv = SENT4;
    if (q < (W_READY - W_QKPROG) / 4) st128(u32x4{0u, 0u, 0u, 0u}, rw, q * 16, (unsigned)(O_CTRL + 4 * W_QKPROG));
    if (q < S_ / 2) st128(sv, rw, q * 16, (unsigned)O_ST);
    constexpr int RU = REC_BLOCKS * 1024;
    for (int r = 0; r < KARM; ++r)
        for (int j = q; j < RU; j += NT) if (r > 0 || j >= 2048) st128(sv, rw, j * 16, (unsigned)r * 4 * PAGE_B);
    constexpr int NA = (2 * KARM + 1) * (int)(XF_B / 16), NG = 2 * KARM * (int)(G3_B / 16);
    for (int j = q; j < NA; j += NT) { st128(sv, rw, j * 16, (unsigned)O_HF); st128(sv, rw, j * 16, (unsigned)O_GF); st128(sv, rw, j * 16, (unsigned)O_X1F); }
    for (int j = q; j < NG; j += NT) st128(sv, rw, j * 16, (unsigned)O_G3F);
    constexpr int TA = (NUNIT - UTAIL) * (int)(XF_B / 16), TG = (NUNIT - UTAIL) * (int)(G3_B / 16);
    for (int j = q; j < TA; j += NT) { st128(sv, rw, j * 16, (unsigned)(O_HF + UTAIL * XF_B)); st128(sv, rw, j * 16, (unsigned)(O_GF + UTAIL * XF_B)); st128(sv, rw, j * 16, (unsigned)(O_X1F + UTAIL * XF_B)); }
    for (int j = q; j < TG; j += NT) st128(sv, rw, j * 16, (unsigned)(O_G3F + UTAIL * G3_B));
    publish_ready(rw, 40 + i, 0u, tid, sp);
}

__global__ void __launch_bounds__(256, 1) k_main(Params P) {
    extern __shared__ __attribute__((aligned(16))) char smem[];
    LAS char* lds = (LAS char*)smem;
    const int tid = threadIdx.x, lane = tid & 63, wave = __builtin_amdgcn_readfirstlane(tid >> 6);
    unsigned char* ws = ws_base(P);
    const unsigned first = (xcc_id() & 7u) * 32u + (blockIdx.x >> 3);
    if (first < 128u && (first & 31u) - 8u < 10u) {
        if (wave == 0) {
            const unsigned f = (first >> 5) * 10u + (first & 31u) - 8u;
            const rsrc_t rw = mk_rsrc(ws, (unsigned)WS_END);
            const unsigned o = (f & 31u) * PAGE_B + PAGE_B - 16384u + (f >> 5) * 2048;
            st32(1u, rw, (lane & 1) * 8192, o);
            vmwait<0>();
            unsigned lat[2] = {~0u, ~0u};
#pragma unroll
            for (int i = 0; i < 4; ++i) {
                const int hf = (i ^ (i >> 1)) & 1;
                unsigned long long t0, t1;
                asm volatile("s_memtime %0\n\ts_waitcnt lgkmcnt(0)" : "=s"(t0) :: "memory");
                st32(1u, rw, 0, o + hf * 8192);
                asm volatile("s_waitcnt vmcnt(0)\n\ts_memtime %0\n\ts_waitcnt lgkmcnt(0)" : "=s"(t1) :: "memory");
                lat[hf] = min(lat[hf], (unsigned)(t1 - t0));
            }
            ((LAS unsigned*)lds)[2] = lat[0]; ((LAS unsigned*)lds)[3] = lat[1];
        }
        __builtin_amdgcn_s_barrier();
    }
    Spin sp;
    {
        const char* code; unsigned len;
        asm volatile("s_getpc_b64 %0\n.Lmain_pc:\n\ts_mov_b32 %1, .Lmain_end-.Lmain_pc" : "=s"(code), "=s"(len));
        sp.cpf = __builtin_amdgcn_raw_buffer_load_b32(mk_rsrc(code, len & ~3u), ((blockIdx.x >> 3) * 256 + tid) * 128, 0, 0);
    }
    gu32* ctrl = (gu32*)(ws + O_CTRL);
    sp.tmo = ctrl + W_TMO; sp.dead = false;
    sp.ev = __hip_atomic_load(ctrl + W_EPOCH, RLX_AGENT);
    if (tid == 0) {
        unsigned slot = first;
        for (int k = 0; k < 256; ++k, slot = (slot + 1u) & 255u)
            if (__hip_atomic_exchange((GAS unsigned long long*)(ctrl + W_CLAIM) + slot * 2u, (sp.ev + 1u) | ((unsigned long long)~(sp.ev + 1u) << 32), RLX_AGENT) != ((sp.ev + 1u) | ((unsigned long long)~(sp.ev + 1u) << 32))) break;
        *(LAS unsigned*)lds = slot;
    }
    __syncthreads();
    const int slot = __builtin_amdgcn_readfirstlane(*(LAS unsigned*)lds);
    const unsigned near = slot == (int)first && __builtin_amdgcn_readfirstlane(((LAS unsigned*)lds)[3] < ((LAS unsigned*)lds)[2]);
    __syncthreads();
    const int xq = slot >> 5, t = slot & 31;
    int mlp = -1, rep = 0;
    if (DLY1 && xq < 4 && (t < 8 || (t >= 18 && t < 26))) { const unsigned t0 = rawnow(); while (rawnow() - t0 < DLY1) __builtin_amdgcn_s_sleep(4); }
    if (xq < 4) {
        if (t < 8) role_S(P, ws, xq * 8 + t, lds, wave, lane, sp);
        else if (t < 18) { init_F(P, ws, xq * 10 + (t - 8), near, lds, wave, lane, sp); role_F(P, ws, xq * 10 + (t - 8), lds, wave, lane, sp); }
        else if (t < 26) role_UV(P, ws, xq * 8 + (t - 18), lds, wave, lane, sp);
        else mlp = t < 26 + MLP0 ? xq * MLP0 + (t - 26) : 32 + xq * 2 + (t - 30);
    } else {
        const int y = xq - 4;
        if (t < 16) { role_VC(P, ws, y * 8 + (t >> 1), t & 1, lds, wave, lane, sp); mlp = (t >> 1) * 4 + y; rep = 1 + (t & 1); }
        else if (t < 28) { role_QK(P, ws, y * 12 + (t - 16), lds, wave, lane, sp); if (t < 24) { mlp = (t - 16) * 4 + y; rep = 3; } }
        else mlp = MLP0 == 4 ? 16 + y * 4 + (t - 28) : t < 30 ? 24 + y * 2 + (t - 28) : 32 + y * 2 + (t - 30);
    }
    if (mlp >= 0) {
        if (rep == 0) {
            const unsigned t0 = rawnow();
            init_arm(ws, mlp, tid, sp);
            while (rawnow() - t0 < DLY2) __builtin_amdgcn_s_sleep(4);
        }
        if (mlp < 5) role_W<1>(P, ws, mlp, rep, lds, wave, lane, sp);
        else if (mlp < 10) role_W<2>(P, ws, mlp - 5, rep, lds, wave, lane, sp);
        else if (mlp < 22) role_W<3>(P, ws, mlp - 10, rep, lds, wave, lane, sp);
        else if (mlp < 32) role_W4(P, ws, mlp - 22, rep, lds, wave, lane, sp);
        if (mlp == 22 && rep == 0 && tid == 0) __hip_atomic_store(ctrl + W_EPOCH, sp.tag, RLX_AGENT);
    }
    MARK(0); MARKC(1); FLUSH(129);
    asm volatile(".Lmain_end:");
}

extern "C" void kernel_launch(void* const* d_in, const int* in_sizes, int n_in, void* d_out, int out_size, void* d_ws, size_t ws_size, hipStream_t stream) {
    Params P;
    P.x = (const unsigned short*)d_in[0]; P.mem0 = (const unsigned short*)d_in[1];
    P.wq = (const unsigned short*)d_in[2]; P.wk = (const unsigned short*)d_in[3]; P.wv = (const unsigned short*)d_in[4]; P.wo = (const unsigned short*)d_in[5];
    P.w1 = (const unsigned short*)d_in[8]; P.w2 = (const unsigned short*)d_in[9]; P.w3 = (const unsigned short*)d_in[10]; P.w4 = (const unsigned short*)d_in[11];
    P.out = (unsigned short*)d_out; P.ws = (unsigned char*)d_ws;
    (void)hipFuncSetAttribute((const void*)k_main, hipFuncAttributeMaxDynamicSharedMemorySize, LDS_BYTES);
    hipLaunchKernelGGL(k_main, dim3(256), dim3(256), LDS_BYTES, stream, P);
}
```

```cpp
#include <hip/hip_runtime.h>
#include <stdint.h>

#define GAS __attribute__((address_space(1)))
#define LAS __attribute__((address_space(3)))
#define RLX_AGENT __ATOMIC_RELAXED, __HIP_MEMORY_SCOPE_AGENT
#define DEV __device__ __forceinline__
typedef __bf16 bf16x2 __attribute__((ext_vector_type(2)));
typedef __bf16 bf16x8 __attribute__((ext_vector_type(8)));
typedef __bf16 bf16x16 __attribute__((ext_vector_type(16)));
typedef short s16x4 __attribute__((ext_vector_type(4)));
typedef float f32x2 __attribute__((ext_vector_type(2)));
typedef float f32x4 __attribute__((ext_vector_type(4)));
typedef unsigned u32x2 __attribute__((ext_vector_type(2)));
typedef unsigned u32x4 __attribute__((ext_vector_type(4)));
typedef unsigned u32x8 __attribute__((ext_vector_type(8)));
typedef GAS unsigned gu32;
typedef __amdgpu_buffer_rsrc_t rsrc_t;

constexpr int S_ = 2048, D_ = 640, H_ = 32, E_ = 192, F_ = 1536, NSEG = 64, NUNIT = 128;
constexpr unsigned SENT = 0xFFFFFFFFu;
constexpr unsigned SPINCAP = 1u << 16;

constexpr unsigned PAGE_B = 2u << 20;
constexpr int NSLOT = 8;
constexpr unsigned R_MEM = 0;
constexpr unsigned R_P = 2 * 8192;
constexpr unsigned R_UB = 5 * 8192;
constexpr unsigned R_PX = 45 * 8192;
constexpr unsigned PX_B = 40960;
constexpr unsigned R_NF = 55 * 8192;
constexpr unsigned R_NS = 56 * 8192;
constexpr int REC_BLOCKS = 57;
#define DELTA_A 13
#define DELTA_B 14
#define VIS 5
#define SLOW 105
#define BULK_FAR 14
#define PSLEEP 2
#define PWAIT 1
#define DLY1 0
#define DLY2 1500
#define DLY3 0
#define NTMASK 0
#define ROTB 0
#define U0_1 116
#define U0_2 108
#define U0_3 106
#define U0_4 96
#define CMIX 1
#define LSLEEP 1
#define MLP0 6
#define EARLY 36u
#define UTAIL_ 96
#define UFREE 118
#define DECAY 2
#define NEARLY 3
#define LATE 40
#define PACE 10
constexpr unsigned amap(unsigned off) { return ((off >> 13) << 14) | (off & 8191u); }
constexpr int UTAIL = UTAIL_;
constexpr int KARM = 4;
constexpr size_t O_CTRL = (size_t)NSLOT * 4 * PAGE_B, CTRL_BYTES = 65536;
constexpr int W_TMO = 0  , W_EPOCH = 64, W_READY = 12288  , W_CLAIM = 15360  , W_QKPROG = 1024  , W_VCPROG = 4096  , W_PROG = 8192  ;
constexpr size_t O_ST = O_CTRL + CTRL_BYTES + 4096;
constexpr size_t O_HF = O_ST + S_ * 8, XF_B = 20480;
constexpr size_t O_GF = O_HF + NUNIT * XF_B;
constexpr size_t O_X1F = O_GF + NUNIT * XF_B;
constexpr size_t O_G3F = O_X1F + NUNIT * XF_B, G3_B = 49152;
constexpr size_t O_XF = O_G3F + NUNIT * G3_B;
constexpr unsigned QX_B = 12288, R_QX = 0, R_KX = H_ * QX_B;
constexpr size_t WS_END = O_XF + NUNIT * XF_B;
static_assert(WS_END < (238u << 20), "workspace");
constexpr int LDS_BYTES = 160 * 1024;

struct Params {
    const unsigned short *x, *mem0, *wq, *wk, *wv, *wo, *w1, *w2, *w3, *w4;
    unsigned short* out;
    unsigned char* ws;
};

DEV unsigned xcc_id() { return (unsigned)__builtin_amdgcn_s_getreg(20 | (3 << 11)) & 0xfu; }
DEV rsrc_t mk_rsrc(const void* p, unsigned bytes) { return __builtin_amdgcn_make_buffer_rsrc((void*)p, 0, bytes, 0x00020000); }
DEV u32x4 ld128(rsrc_t r, unsigned v, unsigned s) { return __builtin_amdgcn_raw_buffer_load_b128(r, v, s, 16); }
DEV u32x2 ld64(rsrc_t r, unsigned v, unsigned s) { return __builtin_amdgcn_raw_buffer_load_b64(r, v, s, 16); }
DEV unsigned ld32(rsrc_t r, unsigned v, unsigned s) { return __builtin_amdgcn_raw_buffer_load_b32(r, v, s, 16); }
DEV void st128(u32x4 d, rsrc_t r, unsigned v, unsigned s) {
    __builtin_amdgcn_raw_buffer_store_b128(d, r, v, s, 16);
    asm volatile("s_nop 1" :: "v"(d) : "memory");
}
DEV void st64(u32x2 d, rsrc_t r, unsigned v, unsigned s) { __builtin_amdgcn_raw_buffer_store_b64(d, r, v, s, 16); }
DEV void st32(unsigned d, rsrc_t r, unsigned v, unsigned s) { __builtin_amdgcn_raw_buffer_store_b32(d, r, v, s, 16); }
DEV void st16(unsigned short d, rsrc_t r, unsigned v, unsigned s) { __builtin_amdgcn_raw_buffer_store_b16(d, r, v, s, 16); }
DEV u32x2 trd(LAS const char* p) { return __builtin_bit_cast(u32x2, __builtin_amdgcn_ds_read_tr16_b64_v4i16((LAS s16x4*)p)); }
DEV u32x4 cat2(u32x2 a, u32x2 b) { return __builtin_shufflevector(a, b, 0, 1, 2, 3); }
DEV u32x8 cat4(u32x4 a, u32x4 b) { return __builtin_shufflevector(a, b, 0, 1, 2, 3, 4, 5, 6, 7); }
DEV u32x4 lo4(u32x8 a) { return __builtin_shufflevector(a, a, 0, 1, 2, 3); }
DEV u32x4 hi4(u32x8 a) { return __builtin_shufflevector(a, a, 4, 5, 6, 7); }
DEV u32x4 lds128(LAS const char* p) { return *(LAS const u32x4*)p; }
DEV unsigned pk(float a, float b) { bf16x2 v = {(__bf16)a, (__bf16)b}; return __builtin_bit_cast(unsigned, v); }
DEV unsigned nosent(unsigned u) { return min(u, SENT - 1u); }
DEV unsigned pks(float a, float b) { return min(pk(a, b), SENT - 1u); }
DEV unsigned pka(float a, float b) { unsigned r; asm("v_cvt_pk_bf16_f32 %0, |%1|, |%2|" : "=v"(r) : "v"(a), "v"(b)); return r; }
DEV float bflo(unsigned u) { return __builtin_bit_cast(float, u << 16); }
DEV float bfhi(unsigned u) { return __builtin_bit_cast(float, u & 0xFFFF0000u); }
DEV unsigned f2u(float f) { return __builtin_bit_cast(unsigned, f); }
DEV float u2f(unsigned u) { return __builtin_bit_cast(float, u); }
DEV unsigned mx4(u32x4 v) { return max(max(v.x, v.y), max(v.z, v.w)); }
DEV bool any_lane(bool c) { return __builtin_amdgcn_ballot_w64(c) != 0; }
DEV void bar() { asm volatile("s_waitcnt lgkmcnt(0)" ::: "memory"); __builtin_amdgcn_s_barrier(); asm volatile("" ::: "memory"); }
DEV void fold32(float& a, float& b) {
    auto r = __builtin_amdgcn_permlane32_swap(f2u(a), f2u(b), false, false); a = u2f(r[0]); b = u2f(r[1]);
}
DEV float x32(float v, bool mx) { auto r = __builtin_amdgcn_permlane32_swap(f2u(v), f2u(v), false, false); return mx ? fmaxf(u2f(r[0]), u2f(r[1])) : u2f(r[0]) + u2f(r[1]); }
DEV float x16(float v, bool mx) { auto r = __builtin_amdgcn_permlane16_swap(f2u(v), f2u(v), false, false); return mx ? fmaxf(u2f(r[0]), u2f(r[1])) : u2f(r[0]) + u2f(r[1]); }
DEV float fmax2(float a, float b) { float r; asm("v_max_f32 %0, %1, %2" : "=v"(r) : "v"(a), "v"(b)); return r; }
DEV float fmax3(float a, float b, float c) { float r; asm("v_max3_f32 %0, %1, %2, %3" : "=v"(r) : "v"(a), "v"(b), "v"(c)); return r; }
#define ROWOP2(op) asm volatile("s_nop 1\n\t" \
    op " %0, %0, %0 row_ror:8 row_mask:0xf bank_mask:0xf\n\t" op " %1, %1, %1 row_ror:8 row_mask:0xf bank_mask:0xf\n\ts_nop 0\n\t" \
    op " %0, %0, %0 row_ror:4 row_mask:0xf bank_mask:0xf\n\t" op " %1, %1, %1 row_ror:4 row_mask:0xf bank_mask:0xf\n\ts_nop 0\n\t" \
    op " %0, %0, %0 row_ror:2 row_mask:0xf bank_mask:0xf\n\t" op " %1, %1, %1 row_ror:2 row_mask:0xf bank_mask:0xf\n\ts_nop 0\n\t" \
    op " %0, %0, %0 row_ror:1 row_mask:0xf bank_mask:0xf\n\t" op " %1, %1, %1 row_ror:1 row_mask:0xf bank_mask:0xf" : "+v"(a), "+v"(b))
DEV void row_max2(float& a, float& b) { ROWOP2("v_max_f32_dpp"); }
DEV void row_sum2(float& a, float& b) { ROWOP2("v_add_f32_dpp"); }
DEV unsigned pair_cols(unsigned p, unsigned psel) { return __builtin_amdgcn_perm(p, __builtin_amdgcn_update_dpp(0u, p, 0xB1, 0xf, 0xf, true), psel); }
DEV float ror8(float v) { return u2f(__builtin_amdgcn_update_dpp(0u, f2u(v), 0x128, 0xf, 0xf, true)); }
template <int OFF> DEV void glds(unsigned voff, const void* sbase, unsigned lds_dst) {
    asm volatile("s_mov_b32 m0, %2\n\ts_nop 0\n\tglobal_load_lds_dwordx4 %0, %1 offset:%3 sc1" :: "v"(voff), "s"(sbase), "s"(lds_dst), "n"(OFF) : "memory");
}
template <int N, int STEP> DEV void glds_dev(unsigned voff, const void* sbase, unsigned lds_dst) {
    static_assert(N == 2 || N == 4, "");
    if (N == 2) asm volatile("s_mov_b32 m0, %2\n\ts_nop 0\n\tglobal_load_lds_dwordx4 %0, %1 sc1\n\tglobal_load_lds_dwordx4 %0, %1 offset:%3 sc1" :: "v"(voff), "s"(sbase), "s"(lds_dst), "n"(STEP) : "memory");
    else asm volatile("s_mov_b32 m0, %2\n\ts_nop 0\n\tglobal_load_lds_dwordx4 %0, %1 sc1\n\tglobal_load_lds_dwordx4 %0, %1 offset:%3 sc1\n\tglobal_load_lds_dwordx4 %0, %1 offset:%4 sc1\n\tglobal_load_lds_dwordx4 %0, %1 offset:%5 sc1"
                      :: "v"(voff), "s"(sbase), "s"(lds_dst), "n"(STEP), "n"(2 * STEP), "n"(3 * STEP) : "memory");
}
template <int N> DEV void vmwait() { asm volatile("s_waitcnt vmcnt(%0)" :: "n"(N) : "memory"); }
template <class T> DEV void pin_a(T& v) { asm volatile("" : "+a"(v)); }
template <class T> DEV void pin_v(T& v) { asm volatile("" : "+v"(v)); }
#define MF_ "v_mfma_f32_16x16x32_bf16 "
DEV void smf_a(f32x4& c, u32x4 a, u32x8 b, int idx) { asm volatile("v_smfmac_f32_16x16x64_bf16 %0, %1, %2, %3" : "+v"(c) : "v"(a), "a"(b), "v"(idx)); }
DEV void smf_v(f32x4& c, u32x4 a, u32x8 b, int idx) { asm volatile("v_smfmac_f32_16x16x64_bf16 %0, %1, %2, %3" : "+v"(c) : "v"(a), "v"(b), "v"(idx)); }
DEV void mf_vv(f32x4& c, u32x4 a, u32x4 b, bool first) { if (first) asm volatile(MF_ "%0, %1, %2, 0" : "=&v"(c) : "v"(a), "v"(b)); else asm volatile(MF_ "%0, %1, %2, %0" : "+v"(c) : "v"(a), "v"(b)); }
DEV void mf_av(f32x4& c, u32x4 a, u32x4 b, bool first) { if (first) asm volatile(MF_ "%0, %1, %2, 0" : "=&v"(c) : "a"(a), "v"(b)); else asm volatile(MF_ "%0, %1, %2, %0" : "+v"(c) : "a"(a), "v"(b)); }
DEV void mf_va(f32x4& c, u32x4 a, u32x4 b, bool first) { if (first) asm volatile(MF_ "%0, %1, %2, 0" : "=&v"(c) : "v"(a), "a"(b)); else asm volatile(MF_ "%0, %1, %2, %0" : "+v"(c) : "v"(a), "a"(b)); }
DEV void mf_done(f32x4& a) { asm volatile("s_nop 7" : "+v"(a)); }
DEV void mf_done(f32x4& a, f32x4& b) { asm volatile("s_nop 7" : "+v"(a), "+v"(b)); }
DEV void mf_done(f32x4& a, f32x4& b, f32x4& c) { asm volatile("s_nop 7" : "+v"(a), "+v"(b), "+v"(c)); }
DEV void mf_done(f32x4& a, f32x4& b, f32x4& c, f32x4& d) { asm volatile("s_nop 7" : "+v"(a), "+v"(b), "+v"(c), "+v"(d)); }
DEV float zero1() { float v; asm volatile("v_mov_b32 %0, 0" : "=v"(v)); return v; }
DEV void zero_acc(f32x4& a) { a = f32x4{zero1(), zero1(), zero1(), zero1()}; asm volatile("s_nop 1" : "+v"(a)); }
DEV LAS char* opq(LAS char* p) { unsigned u = (unsigned)(uintptr_t)p; asm volatile("" : "+v"(u)); return (LAS char*)(uintptr_t)u; }
template <int V> struct IC { static constexpr int value = V; constexpr operator int() const { return V; } };
template <int I, int N, class F> DEV void sfor(F&& f) { if constexpr (I < N) { f(IC<I>{}); sfor<I + 1, N>(f); } }
DEV void lgkm0() { asm volatile("s_waitcnt lgkmcnt(0)" ::: "memory"); __builtin_amdgcn_sched_barrier(0); }

#define MARK(i)
#define MARK2(i)
#define MARK3(i)
#define SETV(i, v)
#define MARKC(i)
#define FLUSH(step)
#define TA(i)
#define TA_DECL
#define TA_FLUSH(step)
struct Spin {
    gu32* tmo; bool dead; unsigned tag;
    unsigned org, ev, cpf;
    DEV void start() { asm volatile("" :: "v"(cpf)); tag = __builtin_amdgcn_readfirstlane(ev) + 1u; }
    DEV bool fail(unsigned& n, unsigned code) {
        asm volatile("" ::: "memory");
        if (dead) return true;
        ++n;
        if (n > SPINCAP) { __hip_atomic_store(tmo + 1, code, RLX_AGENT); __hip_atomic_store(tmo, tag, RLX_AGENT); dead = true; return true; }
        if ((n & 63u) == 0u && (unsigned)__builtin_amdgcn_readfirstlane(__hip_atomic_load(tmo, RLX_AGENT)) == tag) { dead = true; return true; }
        return false;
    }
};

template <int NT> DEV int img_rot(int rho) { return (NT & 1) ? 0 : (NT & 2) ? ((rho >> 2) & 1) : (NT & 4) ? ((rho >> 1) & 3) : (rho & 7); }
DEV int swap23(int r) { return (r & 3) | ((r >> 1) & 4) | ((r << 1) & 8) | (r & 16); }
template <int NT> struct WSrc {
    unsigned voff[NT]; rsrc_t r;
    DEV void init(const char* src, int lane, unsigned stride, int trot = 0) {
        r = mk_rsrc(src - 4096, 0xFFFFFFFFu);
#pragma unroll
        for (int i = 0; i < NT; ++i) {
            const int P = 64 * i + lane, rho = P / (2 * NT), cs = P % (2 * NT);
            const int tile = (((cs >> 1) ^ img_rot<NT>(rho)) + trot) % NT;
            voff[i] = (unsigned)swap23(rho) * stride + (unsigned)(2 * tile + (cs & 1)) * 16u + 4096u - (i & 3) * 1024u;
        }
    }
    DEV void issue(unsigned so, unsigned lds_dst) {
        constexpr bool nt = (NTMASK >> (NT == 6 ? 1 : NT == 10 ? 2 : NT == 3 ? 3 : 0)) & 1;
#define BL_(v, o, p) "buffer_load_dwordx4 " v ", %0, %1 offen offset:" o p " lds\n\t"
#define GRP_(p) \
            if (i + 4 <= NT) asm volatile("s_mov_b32 m0, %2\n\ts_nop 0\n\t" BL_("%3", "0", p) BL_("%4", "1024", p) BL_("%5", "2048", p) BL_("%6", "3072", p) :: "s"(r), "s"(so), "s"(d), "v"(voff[i]), "v"(voff[(i + 1) % NT]), "v"(voff[(i + 2) % NT]), "v"(voff[(i + 3) % NT]) : "memory"); \
            else if (i + 3 == NT) asm volatile("s_mov_b32 m0, %2\n\ts_nop 0\n\t" BL_("%3", "0", p) BL_("%4", "1024", p) BL_("%5", "2048", p) :: "s"(r), "s"(so), "s"(d), "v"(voff[i]), "v"(voff[(i + 1) % NT]), "v"(voff[(i + 2) % NT]) : "memory"); \
            else asm volatile("s_mov_b32 m0, %2\n\ts_nop 0\n\t" BL_("%3", "0", p) BL_("%4", "1024", p) :: "s"(r), "s"(so), "s"(d), "v"(voff[i]), "v"(voff[(i + 1) % NT]) : "memory");
#pragma unroll
        for (int i = 0; i < NT; i += 4) {
            const unsigned d = lds_dst + i * 1024;
            if (nt) { GRP_(" nt") } else { GRP_("") }
        }
#undef GRP_
#undef BL_
    }
};
template <int NT, bool PAIR> struct WRd {
    static constexpr int RR = (NT & 1) ? 1 : (NT & 2) ? 2 : (NT & 4) ? 4 : 8, NB = PAIR ? RR / 2 : RR;
    LAS char* b[NB];
    DEV void init(LAS char* base, int lane) {
        const int g = lane >> 4, q = (lane >> 2) & 3, p = lane & 3, rho = q | ((g & 1) << 2) | ((g >> 1) << 4), rot = img_rot<NT>(rho);
#pragma unroll
        for (int j = 0; j < NB; ++j) b[j] = opq(base + rho * (NT * 32) + (PAIR ? ((2 * j + (p >> 1)) ^ rot) * 32 + 16 * (p & 1) : (j ^ rot) * 32 + 8 * p));
    }
    DEV LAS char* at(int off, int tile) const { return PAIR ? b[(tile >> 1) % NB] + off + (tile / RR) * RR * 32 + 8 * (tile & 1) : b[tile % NB] + off + (tile / RR) * RR * 32; }
    DEV u32x4 get(int off, int tile) const { LAS char* a = at(off, tile); return cat2(trd(a), trd(a + 8 * NT * 32)); }
};
DEV void vmwait_n(int n) {
    switch (n) {
#define VW(n) case n: vmwait<n>(); break;
        VW(0) VW(3) VW(4) VW(6) VW(8) VW(9) VW(10) VW(12) VW(15) VW(16) VW(18) VW(20) VW(24) VW(26) VW(28) VW(29) VW(30) VW(32) VW(35) VW(36) VW(40)
#undef VW
        default: vmwait<0>(); break;
    }
}
template <int NT, int NK, int NKT, int RING, int SROT, bool PAIR, class Sink>
DEV void stream_w(const char* src, unsigned stride, LAS char* lds, unsigned base, int lane, Sink&& sink, int rot = 0, int trot = 0) {
    auto krow = [&](int k) { int kk = k + 2 * rot; if (kk >= NKT) kk -= NKT; return (unsigned)kk * 32u * stride; };
    WSrc<NT> ws; ws.init(src, lane, stride, trot);
    const unsigned lbase = (unsigned)(uintptr_t)lds + base;
#pragma unroll
    for (int k = 0; k < RING && k < NKT; ++k) ws.issue(krow(k), lbase + ((k + SROT) % RING) * NT * 1024);
    WRd<NT, PAIR> rd; rd.init(lds + base, lane);
#pragma unroll
    for (int k = 0; k < NK; ++k) {
        const int issued = (k + RING < NKT) ? k + RING : NKT;
        vmwait_n((issued - k - 1) * NT);
        u32x4 fr[NT];
#pragma unroll
        for (int t = 0; t < NT; ++t) fr[t] = rd.get(((k + SROT) % RING) * NT * 1024, t);
#pragma unroll
        for (int t = 0; t < NT; ++t) sink(t, k, fr[t]);
        if (k + RING < NKT) { lgkm0(); ws.issue(krow(k + RING), lbase + ((k + SROT) % RING) * NT * 1024); }
    }
}

template <int NT, int NS> DEV void repack(LAS char* base, int lane) {
    WRd<NT, false> rd; rd.init(base, lane);
#pragma unroll 1
    for (int k = 0; k < NS; ++k) {
        u32x4 fr[NT];
#pragma unroll
        for (int t = 0; t < NT; ++t) fr[t] = rd.get(k * NT * 1024, t);
        lgkm0();
#pragma unroll
        for (int t = 0; t < NT; ++t) *(LAS u32x4*)(base + (k * NT + t) * 1024 + lane * 16) = fr[t];
    }
}
DEV unsigned page(unsigned par, int s, int p) { const unsigned i = (unsigned)(s & (NSLOT - 1)) * 4u + (unsigned)p; return i * PAGE_B + (((par >> i) & 1u) << 13); }
DEV unsigned rec(unsigned par, int s) { return page(par, s, 0); }
DEV void wait_slot(rsrc_t rw, int s, unsigned& seen, int rep, Spin& sp) {
    for (unsigned c = 0; (int)seen + 6 < s;) {
        seen = __builtin_amdgcn_readfirstlane(ld32(rw, 0, (unsigned)(O_CTRL + 4 * (W_PROG + 32 * rep))));
        if ((int)seen + 6 >= s || sp.fail(c, 0x280 + s)) break;
        __builtin_amdgcn_s_sleep(8);
    }
}
template <bool START = true> DEV unsigned load_par(rsrc_t rw, int lane, Spin& sp) {
    if (START) sp.start();
    u32x4 a, b;
    for (unsigned c = 0;;) {
        a = ld128(rw, lane * 128, (unsigned)(O_CTRL + 4 * W_READY)); b = ld128(rw, (64 + (lane & 15)) * 128, (unsigned)(O_CTRL + 4 * W_READY));
        if (!any_lane(a.x != sp.tag || a.w != ~sp.tag || b.x != sp.tag || b.w != ~sp.tag) || sp.fail(c, 0x40)) break;
        __builtin_amdgcn_s_sleep(16);
    }
    unsigned m = (unsigned)__builtin_amdgcn_ballot_w64(a.y == 1u);
    const int c = __builtin_popcount(m & 0xAAAAAAAAu) - __builtin_popcount(m & 0x55555555u);
    if (c >= 5) m = 0xAAAAAAAAu; else if (c <= -5) m = 0x55555555u;
    sp.org = __builtin_amdgcn_readfirstlane(a.z);
    return m ^ (BULK_FAR * 0x11111111u);
}
DEV unsigned sent1() { unsigned v; asm volatile("v_mov_b32 %0, -1" : "=v"(v)); return v; }
#define SENT4 (u32x4{sent1(), sent1(), sent1(), sent1()})
#define SENT2 (u32x2{sent1(), sent1()})
DEV unsigned rawnow() { unsigned long long v; asm volatile("s_memrealtime %0\n\ts_waitcnt lgkmcnt(0)" : "=s"(v) :: "memory"); return (unsigned)v; }
struct Clock {
    unsigned org;
    DEV unsigned now() const { return rawnow() - org; }
    DEV unsigned wait_until(unsigned T) const {
        unsigned t = rawnow(); T += org;
        if ((int)(T - t) > 1000) T = t + 1000u;
        while ((int)(t - T) < 0) t = rawnow();
        return t - org;
    }
    DEV unsigned wait_fine(unsigned T) const {
        unsigned t = rawnow(); T += org;
        if ((int)(T - t) > 1000) T = t + 1000u;
        if ((int)(t - T) >= -2) return t + 2u - org;
        const unsigned E = T - 4u;
        while ((int)(t - E) < 0) t = rawnow();
        const unsigned rem = T - t;
        if (rem > 2) asm volatile("s_nop 7\n\ts_nop 1");
        if (rem & 1) return T - org;
        asm volatile("s_nop 4");
        return T - org;
    }
};
DEV unsigned umax_dpp(unsigned v, int k) {
    switch (k) { case 1: return max(v, (unsigned)__builtin_amdgcn_update_dpp(0u, v, 0x121, 0xf, 0xf, true)); case 2: return max(v, (unsigned)__builtin_amdgcn_update_dpp(0u, v, 0x122, 0xf, 0xf, true));
                 case 4: return max(v, (unsigned)__builtin_amdgcn_update_dpp(0u, v, 0x124, 0xf, 0xf, true)); default: return max(v, (unsigned)__builtin_amdgcn_update_dpp(0u, v, 0x128, 0xf, 0xf, true)); }
}
DEV unsigned half_max(unsigned v) {
    v = umax_dpp(umax_dpp(umax_dpp(umax_dpp(v, 1), 2), 4), 8);
    auto r = __builtin_amdgcn_permlane16_swap(v, v, false, false);
    return max((unsigned)r[0], (unsigned)r[1]);
}
DEV unsigned wave_max(unsigned v) { const unsigned m = half_max(v); return max((unsigned)__builtin_amdgcn_readlane(m, 0), (unsigned)__builtin_amdgcn_readlane(m, 32)); }
DEV u32x2 read_notes(u32x2 nf) {
    const unsigned tp = wave_max(nf.x == SENT ? 0u : nf.x), df = wave_max(nf.y == SENT ? 0u : min(nf.y, 400u));
    return u32x2{tp, tp + df + DELTA_B};
}
DEV u32x2 first_notes(rsrc_t rw, unsigned vn, unsigned so, Spin& sp) {
    u32x2 nf;
    for (unsigned c = 0;;) { nf = ld64(rw, vn, so); if (!any_lane(max(nf.x, nf.y) == SENT) || sp.fail(c, 0x50)) break; __builtin_amdgcn_s_sleep(2); }
    return nf;
}
DEV unsigned mem_time(unsigned tp, unsigned plan, unsigned tn) { return tn + VIS > tp ? max(plan, tn + SLOW) : plan; }
struct MemStream {
    rsrc_t rw; unsigned l16, vn, lbase, ko[10];
    static DEV int rot_of(int base, int wave) { return (ROTB * base + ((wave * 5 + 1) >> 1)) % 10; }
    DEV void init(rsrc_t r, LAS char* lds, int lane, int rot, bool reader) {
        rw = r; l16 = lane * 16; vn = reader ? min(lane, 39) * 128 : (lane & 15) * 8; lbase = (unsigned)(uintptr_t)lds;
#pragma unroll
        for (int k = 0; k < 10; ++k) { int kk = k + rot; if (kk >= 10) kk -= 10; ko[k] = kk * 1024 + (kk >= 8 ? 8192 : 0); }
    }
    DEV u32x4 ld(unsigned rb, int kb) const { return ld128(rw, l16, rb + ko[kb]); }
    DEV unsigned so(unsigned rb, int kb) const { unsigned v = rb + ko[kb]; asm volatile("" : "+s"(v)); return v; }
    DEV u32x4 lds_(unsigned s) const { u32x4 v = ld128(rw, l16, s); __builtin_amdgcn_sched_barrier(0); return v; }
    DEV LAS char* at(int kb) const { unsigned a; asm volatile("v_add_u32 %0, %1, %2" : "=v"(a) : "s"(ko[kb] + lbase), "v"(l16)); return (LAS char*)(uintptr_t)a; }
    DEV void share(unsigned so, int kb) const {
        asm volatile("s_mov_b32 m0, %3\n\ts_nop 0\n\tbuffer_load_dwordx4 %0, %1, %2 offen sc1 lds" :: "v"(l16), "s"(rw), "s"(so), "s"(ko[kb] + lbase) : "memory");
    }
    DEV void lda(u32x4& a, unsigned so) const { asm volatile("buffer_load_dwordx4 %0, %1, %2, %3 offen sc1" : "=v"(a) : "v"(l16), "s"(rw), "s"(so)); }
    DEV u32x4 get(int kb) const { return lds128(at(kb)); }
    static DEV unsigned long long bad(u32x4 a, u32x4 b, u32x4 c, u32x4 d) {
        unsigned long long m = __builtin_amdgcn_ballot_w64(max(max(mx4(a), mx4(b)), max(mx4(c), mx4(d))) == SENT); asm volatile("" : "+s"(m)); return m;
    }
};
template <int N> DEV void vmw(u32x4& a) { asm volatile("s_waitcnt vmcnt(%1)" : "+v"(a) : "n"(N)); }
DEV unsigned long long nan2(float a, float b) { unsigned long long m; asm volatile("v_cmp_u_f32 %0, %1, %2" : "=s"(m) : "v"(a), "v"(b)); return m; }
DEV float add1(float a, float b) { float r; asm volatile("v_add_f32 %0, %1, %2" : "=v"(r) : "v"(a), "v"(b)); return r; }
DEV unsigned pk1(float a, float b) { unsigned r; asm volatile("v_cvt_pk_bf16_f32 %0, %1, %2" : "=v"(r) : "v"(a), "v"(b)); return r; }
DEV unsigned track(unsigned est, unsigned m, int s) { return m > est ? min(m, est + (s < 8 ? 8u : 3u)) : est - 1u - ((est - m) >> DECAY); }
struct AFrag {
    LAS char *p0, *p1;
    DEV void init(LAS char* img, int lane) {
        const int ga = lane >> 4, base = ga * 256 + (lane & 15) * 8, sw = (ga & 1) * 128;
        p0 = opq(img + base + sw); p1 = opq(img + base + (128 ^ sw));
    }
    DEV u32x4 get(int off) const { return cat2(trd(p0 + off), trd(p1 + off)); }
    static DEV int wofs(int lane, int odd) { const int g = lane >> 4, n = lane & 15; return (((2 * (n >> 2) + (n & 1)) * 32) ^ (odd * 128)) + g * 8 + ((n >> 1) & 1) * 2; }
};
DEV unsigned fold_pk(f32x4 c, float& f0, float& f1) {
    unsigned r;
    asm volatile("v_add_f32 %1, %3, %4\n\tv_add_f32 %2, %5, %6\n\tv_cvt_pk_bf16_f32 %0, %1, %2" : "=v"(r), "=&v"(f0), "=&v"(f1) : "v"(c[0]), "v"(c[1]), "v"(c[2]), "v"(c[3]));
    return r;
}
DEV void st2x16(LAS char* p0, LAS char* p1, int off, unsigned v) { *(LAS unsigned short*)(p0 + off) = (unsigned short)v; *(LAS unsigned short*)(p1 + off) = (unsigned short)(v >> 16); }

constexpr float SCALE_L2E = 0.07216878364870322f * 1.4426950408889634f;

DEV void role_S(const Params& P, unsigned char* ws, int h, LAS char* lds, int wave, int lane, Spin& sp) {
    constexpr int L_QK = 8192, L_FL = 14336, L_W = 16384, L_WS = 36864, L_KX = L_W + L_WS, L_QX = L_W + 3 * L_WS;
    const rsrc_t rw = mk_rsrc(ws, (unsigned)WS_END);
    const int rot = MemStream::rot_of(h, wave);
    u32x8 w[6][8];
    {
        const char* src = (const char*)(wave < 2 ? P.wq : P.wk) + h * 384 + (wave & 1) * 192;
        stream_w<6, 16, 20, 6, 4, false>(src, 12288u, lds, L_W + wave * L_WS, lane,
            [&](int t, int k, u32x4 f) {
                if (k & 1) { w[t][k >> 1] = cat4(lo4(w[t][k >> 1]), f); if ((k >> 1) * 6 + t < 32) pin_a(w[t][k >> 1]); } else w[t][k >> 1] = cat4(f, f); }, rot);
        vmwait<0>();
    }
    MARK(2); FLUSH(128);
    bar();
    const unsigned par = load_par(rw, lane, sp);
    const int idx = (lane & 1) ? 0xEEEE : 0x4444;
    const int g = lane >> 4, n = lane & 15;
    const unsigned cq = (unsigned)(O_CTRL + 4 * (W_QKPROG + 32 * (((wave < 2 ? 384 : 0) + h * 12 + (lane & 1) * 11) >> 4)));
    unsigned have = 0;
    auto wait_qk = [&](int s) {
        if (have > (unsigned)s) return;
        for (unsigned c = 0;;) {
            const unsigned v = ld32(rw, cq, 0);
            have = min((unsigned)__builtin_amdgcn_readlane(v, 0), (unsigned)__builtin_amdgcn_readlane(v, 1));
            if (have > (unsigned)s || sp.fail(c, 0x200 + s)) break;
            __builtin_amdgcn_s_sleep(2);
        }
    };
    Clock ck; ck.org = sp.org;
    MemStream ms; ms.init(rw, lds, lane, rot, wave == 1);
    volatile LAS unsigned* lfail = (volatile LAS unsigned*)(lds + L_FL);
    if (wave == 0) *lfail = ~0u;
    repack<6, 4>(lds + L_W + wave * L_WS + 12288, lane);
    LAS char* wr = opq(lds + L_W + wave * L_WS + 12288 + lane * 16);
    AFrag af; af.init(lds + L_QK, lane);
    LAS char* we = opq(lds + L_QK + wave * 1536 + AFrag::wofs(lane, 0));
    LAS char* wo = opq(lds + L_QK + wave * 1536 + AFrag::wofs(lane, 1));
    const unsigned psel = (n & 1) ? 0x07060302u : 0x01000504u, psel2 = (n & 2) ? 0x07060302u : 0x01000504u;
    unsigned so1, so2;
    if (wave == 0) {
        const int i = 2 * g + (n & 1), half = (n >> 1) & 1;
        so1 = amap(R_P) + (h >> 4) * 16384 + (h & 15) * 512 + ((n >> 3) * 16 + 2 * i + half) * 16 + ((n >> 2) & 1) * 8;
        so2 = amap(R_P + 16384) + (h >> 1) * 256 + (2 * (2 * g + half) + ((n >> 2) & 1)) * 16 + (h & 1) * 8 + (n >> 3) * 4;
    } else {
        const unsigned b = amap(R_PX) + (wave & 1) * amap(PX_B);
        so1 = (wave & 1) ? b + (h >> 3) * 16384 + (h & 7) * 1024 + lane * 16 : b + (h >> 4) * 16384 + ((h >> 1) & 7) * 1024 + lane * 16 + (h & 1) * 8;
        so2 = b + 65536 + h * 256 + n * 16 + g * 4;
    }
    unsigned xo[3];
#pragma unroll
    for (int j = 0; j < 3; ++j) { const unsigned o = (wave < 2 ? R_KX : R_QX) + h * QX_B + j * 4096; xo[j] = (((o >> 13) << 14) | (o & 8191u)) + (wave < 2 ? 0 : (wave & 1) * 1024); }
    auto stage_k = [&](int s) __attribute__((always_inline)) {
        wait_qk(s);
#pragma unroll
        for (int j = 0; j < 3; ++j) glds_dev<4, 1024>(ms.l16, ws + (page(par, s, 1) + xo[j]), (unsigned)(uintptr_t)lds + L_KX + (s & 1) * L_WS + j * 4096);
    };
    auto stage_q = [&](int s) __attribute__((always_inline)) {
        wait_qk(s);
#pragma unroll
        for (int j = 0; j < 3; ++j) glds_dev<2, 2048>(ms.l16, ws + (page(par, s, 1) + xo[j]), (unsigned)(uintptr_t)lds + L_QX + (wave & 1) * 1024 + j * 4096);
    };
    auto lwh = [&](int j, int q) { return lds128(wr + (2 * (1 - j / 6) + q) * 6144 + (j % 6) * 1024); };
    unsigned dur = 139, tn = 0;
    if (wave == 0) __builtin_amdgcn_s_setprio(3);
    if (wave == 1) stage_k(0); else if (wave >= 2) stage_q(0);
    vmwait<0>();
    bar();
    if (wave == 1) stage_k(1);
    for (int s = 0; s < NSEG; ++s) {
        const unsigned rb = rec(par, s);
        f32x4 acc[6];
        u32x2 nf; u32x4 st[12][2], a[10], kxf[12];
        LAS const char* kx = opq(lds + L_KX + (s & 1) * L_WS + lane * 16);
        unsigned long long bad = 0;
        auto prep = [&]() __attribute__((always_inline)) {
#pragma unroll
            for (int t = 0; t < 6; ++t) zero_acc(acc[t]);
#pragma unroll
            for (int i = 0; i < 6; ++i) st[i >> 1][i & 1] = lwh(i >> 1, i & 1);
        };
        float fa[6], fb[6]; unsigned fp[6];
        auto fin = [&](int t, int step) __attribute__((always_inline)) {
            if (step == 0) fa[t] = add1(acc[t][0], acc[t][1]);
            if (step == 1) fb[t] = add1(acc[t][2], acc[t][3]);
            if (step == 2) fp[t] = pk1(fa[t], fb[t]);
            if (step == 3) *(LAS unsigned short*)(((t & 1) ? wo : we) + t * 256) = (unsigned short)fp[t];
            if (step == 4) *(LAS unsigned short*)(((t & 1) ? wo : we) + t * 256 + 4) = (unsigned short)(fp[t] >> 16);
        };
        unsigned long long bad2 = 0;
        unsigned qp;
        LAS char* ad[9];
        TA_DECL;
        auto slot = [&](auto I) __attribute__((always_inline)) {
            constexpr int i = decltype(I)::value;
            if constexpr (i == 15) asm volatile("s_waitcnt lgkmcnt(4)\n\ts_barrier" ::: "memory");
            if constexpr (i == 15) { MARK3(0); }
            if constexpr (i == 36) { MARK3(5); }
            constexpr bool isl = i >= 6 && i <= 50 && (i & 3) == 2;
            constexpr int nl = i < 6 ? 0 : i > 50 ? 12 : (i - 6 + 3) / 4;
            if constexpr (isl) {
                smf_v(acc[nl % 6], a[9 - nl / 6], cat4(st[nl][0], st[nl][1]), idx);
            } else {
                constexpr int r = i - nl;
                constexpr int kb = r < 7 ? r / 6 : r == 7 ? 2 : r < 13 ? 1 : r < 24 ? r / 6 : r < 40 ? 4 + ((r - 24) % 8) / 2 : 4 + (r & 3);
                constexpr int t = r < 7 ? r % 6 : r == 7 ? 0 : r < 13 ? r - 7 : r < 24 ? r % 6 : r < 40 ? 2 * ((r - 24) / 8) + (r & 1) : 4 + (r - 40) / 4;
                if constexpr (kb * 6 + t < 32) smf_a(acc[t], a[kb], w[t][kb], idx); else smf_v(acc[t], a[kb], w[t][kb], idx);
            }
            if constexpr (i == 0) { MARK2(2); TA(1); }
            if constexpr (i == 14) { TA(2); }
            if constexpr (i == 15) { TA(3); }
            if constexpr (i == 59) { TA(4); }
            if constexpr (i == 0) *(LAS u32x4*)ad[6] = a[0];
            if constexpr (i == 2) *(LAS u32x4*)ad[7] = a[1];
            if constexpr (i == 4) *(LAS u32x4*)ad[8] = a[2];
            if constexpr (i == 13) bad = nan2(acc[0][0], acc[0][1]);
            if constexpr (i == 13) bad2 = nan2(acc[0][2], acc[0][3]);
            if constexpr (i == 16) a[3] = lds128(ad[0]);
            if constexpr (i == 18) nf = ld64(rw, ms.vn, rb + amap(R_NF));
            if constexpr (i == 30) qp = ld32(rw, cq, 0);
            if constexpr (i == 20) a[8] = lds128(ad[1]);
            if constexpr (i == 22) a[4] = lds128(ad[2]);
            if constexpr (i == 24) a[5] = lds128(ad[3]);
            if constexpr (i == 26) a[6] = lds128(ad[4]);
            if constexpr (i == 28) a[7] = lds128(ad[5]);
            if constexpr (i >= 7 && i < 43 && (i & 1)) st[(i - 7) / 4 + 3][((i - 7) >> 1) & 1] = lwh((i - 7) / 4 + 3, ((i - 7) >> 1) & 1);
            if constexpr (i >= 43 && i < 55) kxf[i - 43] = lds128(kx + (i - 43) * 1024);
            if constexpr (i >= 44 && i < 49) fin(0, i - 44);
            if constexpr (i >= 47 && i < 52) fin(1, i - 47);
            if constexpr (i >= 55) { fin(2, i - 55); fin(3, i - 55); }
        };
        prep();
        MARK(0); SETV(7, 0);
        ad[0] = ms.at(3); ad[1] = ms.at(8); ad[2] = ms.at(4); ad[3] = ms.at(5); ad[4] = ms.at(6); ad[5] = ms.at(7); ad[6] = ms.at(0); ad[7] = ms.at(1); ad[8] = ms.at(2);
        const unsigned o0 = ms.so(rb, 0), o9 = ms.so(rb, 9), o1 = ms.so(rb, 1), o2 = ms.so(rb, 2);
        const unsigned tg = ck.wait_fine(tn);
        TA(0);
        a[0] = ms.lds_(o0); a[1] = ms.lds_(o1); a[2] = ms.lds_(o2); a[9] = ms.lds_(o9);
        SETV(1, tg + ck.org);
        sfor<0, 15>(slot);
        if (__builtin_expect((bad | bad2) != 0, 0)) {
            asm volatile("s_nop 7\n\ts_waitcnt vmcnt(0)" ::: "memory");
            *lfail = (unsigned)s; SETV(7, 1);
            for (unsigned c = 0;;) {
                a[0] = ms.ld(rb, 0); a[9] = ms.ld(rb, 9); a[1] = ms.ld(rb, 1); a[2] = ms.ld(rb, 2);
                if (MemStream::bad(a[0], a[9], a[1], a[2]) == 0 || sp.fail(c, 0x100 + s)) break;
            }
            prep();
            sfor<0, 15>(slot);
            asm volatile("s_nop 7" : "+v"(acc[0]), "+v"(acc[1]), "+v"(acc[2]), "+v"(acc[3]), "+v"(acc[4]), "+v"(acc[5]));
        }
        MARK3(6);
        sfor<15, 60>(slot);
        fin(4, 0); fin(4, 1); fin(4, 2); fin(4, 3); fin(4, 4);
        asm volatile("s_nop 0");
        fin(5, 0); fin(5, 1); fin(5, 2); fin(5, 3); fin(5, 4);
        f32x4 am, a0, a1;
        MARK2(3);
        bar();
        MARK2(4); TA(5); TA(6); TA(7);
        u32x4 qa[3], ka[3];
        if (wave >= 2) {
            const int Tq = wave & 1;
            have = min((unsigned)__builtin_amdgcn_readlane(qp, 0), (unsigned)__builtin_amdgcn_readlane(qp, 1));
            u32x4 qx[6];
            asm volatile("s_nop 7\n\ts_nop 7" ::: "memory");
#pragma unroll
            for (int kb = 0; kb < 3; ++kb) ka[kb] = af.get(3072 + kb * 1024);
#pragma unroll
            for (int ep = 0; ep < 2; ++ep) qx[ep] = lds128(lds + L_QX + lane * 16 + Tq * 1024 + ep * 2048);
            zero_acc(am);
#pragma unroll
            for (int ep = 0; ep < 6; ++ep) {
                if (ep < 4) qx[ep + 2] = lds128(lds + L_QX + lane * 16 + Tq * 1024 + (ep + 2) * 2048);
                mf_vv(a0, kxf[2 * ep], qx[ep], ep == 0); mf_vv(a1, kxf[2 * ep + 1], qx[ep], ep == 0);
                if (ep & 1) smf_v(am, ka[ep >> 1], cat4(qx[ep - 1], qx[ep]), idx);
            }
            mf_done(am, a0, a1);
            float v[10];
            constexpr float SC = 0.07216878364870322f, L2E = 1.4426950408889634f;
#pragma unroll
            for (int r = 0; r < 4; ++r) {
                v[r] = (4 * g + r <= 16 * Tq + n) ? a0[r] * SC : -1e9f;
                v[4 + r] = (16 + 4 * g + r <= 16 * Tq + n) ? a1[r] * SC : -1e9f;
            }
            v[8] = (am[0] + am[1]) * SC; v[9] = (am[2] + am[3]) * SC;
            float mx = v[0];
#pragma unroll
            for (int r = 1; r < 10; ++r) mx = fmaxf(mx, v[r]);
            mx = x16(mx, true); mx = x32(mx, true);
            float sum = 0.f;
#pragma unroll
            for (int r = 0; r < 10; ++r) { v[r] = __builtin_amdgcn_exp2f((v[r] - mx) * L2E); sum += v[r]; }
            sum = x16(sum, false); sum = x32(sum, false);
            const float inv = __builtin_amdgcn_rcpf(sum);
            u32x4 ox = {pka(v[0] * inv, v[1] * inv), pka(v[2] * inv, v[3] * inv), pka(v[4] * inv, v[5] * inv), pka(v[6] * inv, v[7] * inv)};
            unsigned om = pka(v[8] * inv, v[9] * inv);
            asm volatile("s_barrier" : "+v"(ox), "+v"(om) :: "memory");
            if (Tq) st128(ox, rw, so1, rb); else st64(u32x2{ox.x, ox.y}, rw, so1, rb);
            st32(om, rw, so2, rb);
            if (s + 1 < NSEG) stage_q(s + 1);
            if (s + KARM < NSEG) { const unsigned ra = rec(par, s + KARM); if (Tq) st128(SENT4, rw, so1, ra); else st64(SENT2, rw, so1, ra); st32(sent1(), rw, so2, ra); }
        } else if (wave == 1) {
            have = min((unsigned)__builtin_amdgcn_readlane(qp, 0), (unsigned)__builtin_amdgcn_readlane(qp, 1));
            if (s + 2 < NSEG) stage_k(s + 2);
            if (s == 0) nf = first_notes(rw, ms.vn, rb + amap(R_NF), sp);
            { const u32x2 pl = read_notes(nf); lfail[2] = pl.x; lfail[3] = pl.y; }
            asm volatile("s_waitcnt lgkmcnt(0)\n\ts_barrier" ::: "memory");
        } else {
            if (s < NSEG - 1) {
#pragma unroll
                for (int kb = 0; kb < 3; ++kb) { qa[kb] = af.get(kb * 1024); __builtin_amdgcn_sched_barrier(0); ka[kb] = af.get(3072 + kb * 1024); __builtin_amdgcn_sched_barrier(0); }
                a0 = f32x4{zero1(), zero1(), zero1(), zero1()}; a1 = f32x4{zero1(), zero1(), zero1(), zero1()};
                asm volatile("s_nop 0" : "+v"(a0), "+v"(a1));
                auto sx = [&](int kb, int T) __attribute__((always_inline)) { smf_v(T ? a1 : a0, qa[kb], cat4(kxf[4 * kb + T], kxf[4 * kb + 2 + T]), idx); };
                sx(0, 0); mf_vv(am, qa[0], ka[0], true); sx(0, 1); mf_vv(am, qa[1], ka[1], false); sx(1, 0); mf_vv(am, qa[2], ka[2], false);
                sx(1, 1); sx(2, 0); sx(2, 1);
                TA(6);
                float v[6];
                float ma, mb; u32x2 p01; unsigned p0, p1, p2;
#define DP " row_mask:0xf bank_mask:0xf\n\t"
#define RED(op, r) op " %6, %6, %6 row_ror:" r DP op " %7, %7, %7 row_ror:" r DP
                asm volatile("s_nop 4\n\t"
                    "v_add_f32_dpp %2, %12, %11 quad_perm:[1,1,3,3]" DP "v_add_f32_dpp %5, %14, %13 quad_perm:[1,1,3,3]" DP
                    "v_add_f32 %0, %15, %16\n\tv_add_f32 %3, %17, %18\n\t"
                    "v_mov_b32_dpp %2, %2 quad_perm:[0,0,2,2]" DP "v_mov_b32_dpp %5, %5 quad_perm:[0,0,2,2]" DP
                    "v_add_f32 %1, %19, %20\n\tv_add_f32 %4, %21, %22\n\t"
                    "v_max3_f32 %6, %0, %1, %2\n\tv_max3_f32 %7, %3, %4, %5\n\tv_mul_f32 %0, %23, %0\n\t"
                    RED("v_max_f32_dpp", "8") "v_mul_f32 %3, %23, %3\n\t" RED("v_max_f32_dpp", "4") "v_mul_f32 %1, %23, %1\n\t"
                    RED("v_max_f32_dpp", "2") "v_mul_f32 %4, %23, %4\n\t" RED("v_max_f32_dpp", "1")
                    "v_fma_f32 %0, -%6, %23, %0\n\tv_fma_f32 %3, -%7, %23, %3\n\tv_fma_f32 %1, -%6, %23, %1\n\tv_fma_f32 %4, -%7, %23, %4\n\t"
                    "v_exp_f32 %0, %0\n\tv_sub_f32 %2, %2, %6\n\tv_exp_f32 %3, %3\n\tv_sub_f32 %5, %5, %7\n\tv_exp_f32 %1, %1\n\tv_mul_f32 %2, %23, %2\n\tv_exp_f32 %4, %4\n\tv_mul_f32 %5, %23, %5\n\t"
                    "v_exp_f32 %2, %2\n\tv_add_f32 %6, %0, %1\n\tv_exp_f32 %5, %5\n\tv_add_f32 %7, %3, %4\n\t"
                    "v_fmac_f32 %6, 0.5, %2\n\tv_fmac_f32 %7, 0.5, %5\n\ts_nop 0\n\t"
                    RED("v_add_f32_dpp", "8") "s_nop 0\n\t" RED("v_add_f32_dpp", "4") "s_nop 0\n\t" RED("v_add_f32_dpp", "2") "s_nop 0\n\t" RED("v_add_f32_dpp", "1")
                    "v_rcp_f32 %6, %6\n\tv_rcp_f32 %7, %7\n\t"
                    "v_mul_f32 %0, %0, %6\n\tv_mul_f32 %3, %3, %7\n\tv_mul_f32 %1, %1, %6\n\tv_mul_f32 %4, %4, %7\n\t"
                    "v_cvt_pk_bf16_f32 %8, |%0|, |%3|\n\tv_cvt_pk_bf16_f32 %9, |%1|, |%4|\n\tv_mul_f32 %2, %2, %6\n\t"
                    "v_mov_b32_dpp %0, %8 quad_perm:[1,0,3,2]" DP "v_mov_b32_dpp %1, %9 quad_perm:[1,0,3,2]" DP "v_mul_f32 %5, %5, %7\n\t"
                    "v_cvt_pk_bf16_f32 %10, |%2|, |%5|\n\tv_perm_b32 %8, %8, %0, %24\n\tv_perm_b32 %9, %9, %1, %24"
                    : "=&v"(v[0]), "=&v"(v[1]), "=&v"(v[2]), "=&v"(v[3]), "=&v"(v[4]), "=&v"(v[5]), "=&v"(ma), "=&v"(mb), "=&v"(p0), "=&v"(p1), "=&v"(p2)
                    : "v"(am[0]), "v"(am[1]), "v"(am[2]), "v"(am[3]), "v"(a0[0]), "v"(a0[1]), "v"(a0[2]), "v"(a0[3]), "v"(a1[0]), "v"(a1[1]), "v"(a1[2]), "v"(a1[3]), "s"(SCALE_L2E), "v"(psel));
#undef RED
#undef DP
                p01 = u32x2{p0, p1};
                st64(p01, rw, so1, rb);
                st32(__builtin_amdgcn_perm(p2, __builtin_amdgcn_update_dpp(0u, p2, 0x4E, 0xf, 0xf, true), psel2), rw, so2, rb);
                TA(7);
                tn = ck.now();
                if (lane == 0) st32(dur, rw, h * 128, rb + amap(R_NS));
                lfail[1] = tn;
                asm volatile("s_waitcnt lgkmcnt(0)\n\ts_barrier" ::: "memory");
                if (s > 0 && __builtin_amdgcn_readfirstlane(*lfail) != s) dur = track(dur, tn - tg, s);
                SETV(5, tn + ck.org); SETV(6, tg + dur + ck.org);
                if (s + KARM < NSEG) { const unsigned ra = rec(par, s + KARM); st64(SENT2, rw, so1, ra); st32(sent1(), rw, so2, ra); if (lane == 0) st32(sent1(), rw, h * 128, ra + amap(R_NS)); }
            } else asm volatile("s_barrier" ::: "memory");
        }
        tn = mem_time(__builtin_amdgcn_readfirstlane(lfail[2]), __builtin_amdgcn_readfirstlane(lfail[3]), __builtin_amdgcn_readfirstlane(lfail[1]));
        FLUSH(s); TA_FLUSH(s);
    }
    vmwait<0>();
}

constexpr int L_WO = 81920;
template <class SV, class SO>
DEV void load_vo(const Params& P, int h, LAS char* lds, int wave, int lane, Spin& sp, SV&& sv, SO&& so_, int rot = 0) {
    auto krow = [&](int k) { int kk = k + 2 * rot; if (kk >= 20) kk -= 20; return (unsigned)kk * 32u * 12288u; };
    WSrc<3> wv; wv.init((const char*)P.wv + h * 384 + wave * 96, lane, 12288u);
    const unsigned lo = (unsigned)(uintptr_t)lds + L_WO + wave * 20480, lv = (unsigned)(uintptr_t)lds + wave * 20480;
#pragma unroll
    for (int k = 0; k < 6; ++k) wv.issue(krow(k), lv + k * 3072);
    WSrc<10> wo; wo.init((const char*)P.wo + (size_t)h * E_ * 1280 + wave * 320, lane, 1280u);
    wo.issue(0, lo); wo.issue(32 * 1280, lo + 10240);
    WRd<3, false> rv; rv.init(lds + wave * 20480, lane);
    WRd<10, false> ro; ro.init(lds + L_WO + wave * 20480, lane);
#pragma unroll
    for (int k = 0; k < 20; ++k) {
        if (k == 1) { MARK(3); } if (k == 6) { MARK(4); } if (k == 12) { MARK(5); }
        if (k == 6 || k == 12) {
            const int ko = k / 3 - 2;
            vmwait<18>();
#pragma unroll
            for (int j = 0; j < 2; ++j) {
                u32x4 fo[10];
#pragma unroll
                for (int t = 0; t < 10; ++t) fo[t] = ro.get(j * 10240, t);
#pragma unroll
                for (int t = 0; t < 10; ++t) so_(t, ko + j, fo[t]);
            }
            lgkm0();
            wo.issue((ko + 2) * 32 * 1280, lo); wo.issue((ko + 3) * 32 * 1280, lo + 10240);
        }
        vmwait_n(k < 15 ? 35 : k < 18 ? 20 + (19 - k) * 3 : (19 - k) * 3);
        u32x4 fr[3];
#pragma unroll
        for (int t = 0; t < 3; ++t) fr[t] = rv.get((k % 6) * 3072, t);
#pragma unroll
        for (int t = 0; t < 3; ++t) sv(t, k, fr[t]);
        if (k + 6 < 20) { lgkm0(); wv.issue(krow(k + 6), lv + (k % 6) * 3072); }
    }
    vmwait<0>();
    MARK(2); FLUSH(128);
    bar();
}

template <class SV, class SO>
DEV void load_vo_rows(const Params& P, int h, LAS char* lds, int wave, int lane, Spin& sp, SV&& sv, SO&& so_) {
    WSrc<3> wv;
    wv.r = mk_rsrc((const char*)P.wv + h * 384 - 4096, 0xFFFFFFFFu);
#pragma unroll
    for (int i = 0; i < 3; ++i) {
        const int Pc = 64 * (3 * wave + i) + lane, rho = Pc / 24, cs = Pc % 24, tile = (cs >> 1) ^ img_rot<12>(rho);
        wv.voff[i] = (unsigned)swap23(rho) * 12288u + (unsigned)(2 * tile + (cs & 1)) * 16u + 4096u - i * 1024u;
    }
    const unsigned lo = (unsigned)(uintptr_t)lds + L_WO + wave * 20480, lv = (unsigned)(uintptr_t)lds + wave * 3072;
#pragma unroll
    for (int k = 0; k < 6; ++k) wv.issue(k * 32u * 12288u, lv + k * 12288);
    WSrc<10> wo; wo.init((const char*)P.wo + (size_t)h * E_ * 1280 + wave * 320, lane, 1280u);
    wo.issue(0, lo); wo.issue(32 * 1280, lo + 10240);
    LAS char* rv[3];
    { const int g = lane >> 4, q = (lane >> 2) & 3, p = lane & 3, rho = q | ((g & 1) << 2) | ((g >> 1) << 4);
#pragma unroll
      for (int t = 0; t < 3; ++t) rv[t] = opq(lds + rho * 384 + ((3 * wave + t) ^ img_rot<12>(rho)) * 32 + 8 * p); }
    WRd<10, false> ro; ro.init(lds + L_WO + wave * 20480, lane);
#pragma unroll
    for (int k = 0; k < 20; ++k) {
        if (k == 1) { MARK(3); } if (k == 6) { MARK(4); } if (k == 12) { MARK(5); }
        if (k == 6 || k == 12) {
            const int ko = k / 3 - 2;
            vmwait_n(k == 6 ? 15 : 18);
#pragma unroll
            for (int j = 0; j < 2; ++j) {
                u32x4 fo[10];
#pragma unroll
                for (int t = 0; t < 10; ++t) fo[t] = ro.get(j * 10240, t);
#pragma unroll
                for (int t = 0; t < 10; ++t) so_(t, ko + j, fo[t]);
            }
            lgkm0();
            wo.issue((ko + 2) * 32 * 1280, lo); wo.issue((ko + 3) * 32 * 1280, lo + 10240);
        }
        vmwait_n(k == 0 ? 35 : k == 11 ? 12 : k < 16 ? 32 : k == 16 ? 29 : (19 - k) * 3);
        bar();
        if (k >= 1 && k + 5 < 20) wv.issue((k + 5) * 32u * 12288u, lv + ((k + 5) % 6) * 12288);
        u32x4 fr[3];
#pragma unroll
        for (int t = 0; t < 3; ++t) fr[t] = cat2(trd(rv[t] + (k % 6) * 12288), trd(rv[t] + (k % 6) * 12288 + 3072));
#pragma unroll
        for (int t = 0; t < 3; ++t) sv(t, k, fr[t]);
    }
    vmwait<0>();
    MARK(2); FLUSH(128);
    bar();
}

DEV void role_UV(const Params& P, unsigned char* ws, int h, LAS char* lds, int wave, int lane, Spin& sp) {
    const rsrc_t rw = mk_rsrc(ws, (unsigned)WS_END);
    const int rot = MemStream::rot_of(h + 5, wave);
    u32x8 wv[3][10], wo[10][2];
    load_vo(P, h, lds, wave, lane, sp,
        [&](int t, int k, u32x4 f) { if (k & 1) { wv[t][k >> 1] = cat4(lo4(wv[t][k >> 1]), f); pin_a(wv[t][k >> 1]); } else wv[t][k >> 1] = cat4(f, f); },
        [&](int t, int k, u32x4 f) { if (k & 1) { wo[t][k >> 1] = cat4(lo4(wo[t][k >> 1]), f); if (k == 1 && t < 2) pin_a(wo[t][k >> 1]); } else wo[t][k >> 1] = cat4(f, f); }, rot);
    const unsigned par = load_par(rw, lane, sp);
    const int idx = (lane & 1) ? 0xEEEE : 0x4444;
    const int g = lane >> 4, n = lane & 15;
    MemStream ms; ms.init(rw, lds, lane, rot, wave == 0);
    volatile LAS unsigned* lfail = (volatile LAS unsigned*)(lds + 14336);
    if (wave == 0) *lfail = ~0u;
    bar();
    AFrag af; af.init(lds + 8192, lane);
    repack<10, 2>(lds + L_WO + wave * 20480, lane);
    LAS char* wr = opq(lds + L_WO + wave * 20480 + lane * 16);
    const unsigned uo = amap(R_UB) + (wave * 10 + (g & 1)) * 16384 + h * 256 + n * 16 + (g >> 1) * 8;
    LAS char* vw0 = opq(lds + 8192 + wave * 768 + AFrag::wofs(lane, wave & 1));
    LAS char* vw1 = opq(lds + 8192 + wave * 768 + AFrag::wofs(lane, ~wave & 1));
    auto lwh = [&](int t, int q) { return lds128(wr + q * 10240 + t * 1024); };
    Clock ck; ck.org = sp.org;
    unsigned dur = 126, tn = 0;
    for (int s = 0; s < NSEG; ++s) {
        const unsigned rb = rec(par, s);
        f32x4 acc[3], c[10];
        u32x4 st[10][2], a[10];
#pragma unroll
        for (int i = 0; i < 6; ++i) st[i >> 1][i & 1] = lwh(i >> 1, i & 1);
#pragma unroll
        for (int t = 0; t < 3; ++t) zero_acc(acc[t]);
#pragma unroll
        for (int t = 0; t < 4; ++t) zero_acc(c[t]);
        MARK(0); SETV(7, 0);
        const unsigned o0 = ms.so(rb, 0), o1 = ms.so(rb, 1), o2 = ms.so(rb, 2), o3 = ms.so(rb, 3);
        LAS char* ad[10];
#pragma unroll
        for (int k = 0; k < 10; ++k) if (k != 3) ad[k] = ms.at(k);
        const unsigned tg = ck.wait_fine(tn);
        a[0] = ms.lds_(o0); a[1] = ms.lds_(o1); a[2] = ms.lds_(o2); a[3] = ms.lds_(o3);
        SETV(1, tg + ck.org);
        unsigned long long bad = 0, bad2 = 0;
        auto slot = [&](auto I) __attribute__((always_inline)) {
            constexpr int i = decltype(I)::value;
            constexpr int kb = i < 4 ? i : i < 12 ? (i - 4) / 2 : i / 3, t = i < 4 ? 0 : i < 12 ? 1 + (i & 1) : i % 3;
            if constexpr (i == 9) { asm volatile("s_waitcnt lgkmcnt(0)\n\ts_barrier" ::: "memory"); a[4] = lds128(ad[4]); a[5] = lds128(ad[5]); }
            smf_a(acc[t], a[kb], wv[t][kb], idx);
            if constexpr (i == 0) { MARK2(2); }
            if constexpr (i < 3) *(LAS u32x4*)ad[i] = a[i];
            if constexpr (i == 6) { asm volatile("s_nop 4"); bad = nan2(acc[0][0], acc[0][1]); bad2 = nan2(acc[0][2], acc[0][3]); }
            if constexpr (i >= 9 && i <= 18 && i % 3 == 0) a[3 + i / 3] = lds128(ad[3 + i / 3]);
            if constexpr (i >= 14 && i < 26) { constexpr int q = i - 14; c[4 + q / 4][q & 3] = zero1(); }
        };
        sfor<0, 9>(slot);
        if (__builtin_expect((bad | bad2) != 0, 0)) {
            asm volatile("s_nop 7\n\ts_waitcnt vmcnt(0)" ::: "memory");
            *lfail = (unsigned)s; SETV(7, 1);
            for (unsigned c = 0;;) {
#pragma unroll
                for (int k = 0; k < 4; ++k) a[k] = ms.ld(rb, k);
                if (MemStream::bad(a[0], a[1], a[2], a[3]) == 0 || sp.fail(c, 0x100 + s)) break;
            }
#pragma unroll
            for (int t = 0; t < 3; ++t) zero_acc(acc[t]);
            sfor<0, 9>(slot);
            mf_done(acc[0], acc[1], acc[2]);
        }
        sfor<9, 30>(slot);
        asm volatile("s_nop 7" : "+v"(acc[0]), "+v"(acc[1]), "+v"(acc[2]), "+v"(c[4]), "+v"(c[5]), "+v"(c[6]));
#pragma unroll
        for (int t = 0; t < 3; ++t) { LAS char* p = t == 1 ? vw1 : vw0; float f0, f1; st2x16(p, p + 4, t * 256, fold_pk(acc[t], f0, f1)); }
#pragma unroll
        for (int t = 7; t < 10; ++t) { c[t] = f32x4{zero1(), zero1(), zero1(), zero1()}; pin_v(c[t]); }
        MARK2(3);
        bar();
        MARK2(4);
        u32x4 va[3];
#pragma unroll
        for (int kb = 0; kb < 3; ++kb) va[kb] = af.get(kb * 1024);
        float fa[10], fb[10]; unsigned o[10];
        sfor<0, 39>([&](auto I) __attribute__((always_inline)) {
            constexpr int i = decltype(I)::value, tp = i / 6, k = (i % 6) >> 1, t = 2 * tp + (i & 1);
            if constexpr (i < 30) {
                if constexpr (k == 0) { if constexpr (t < 2) smf_a(c[t], va[0], wo[t][0], idx); else smf_v(c[t], va[0], wo[t][0], idx); }
                else if constexpr (k == 1) smf_v(c[t], va[1], wo[t][1], idx);
                else smf_v(c[t], va[2], cat4(st[t][0], st[t][1]), idx);
                if constexpr (i >= 3 && i < 24 && i % 3 < 2) st[i / 3 + 2][i % 3] = lwh(i / 3 + 2, i % 3);
            }
            if constexpr (i >= 9) {
                constexpr int x = 2 * ((i - 9) / 6), y = x + 1, j = (i - 9) % 6;
                if constexpr (i == 33) asm volatile("s_nop 0");
                if constexpr (j == 0) { fa[x] = add1(c[x][0], c[x][1]); fb[x] = add1(c[x][2], c[x][3]); }
                if constexpr (j == 1) { fa[y] = add1(c[y][0], c[y][1]); fb[y] = add1(c[y][2], c[y][3]); }
                if constexpr (j == 2) { o[x] = pk1(fa[x], fb[x]); o[y] = pk1(fa[y], fb[y]); }
                if constexpr (j == 3) asm volatile("v_min_u32 %0, -2, %0\n\tv_min_u32 %1, -2, %1" : "+v"(o[x]), "+v"(o[y]));
                if constexpr (j == 4) asm volatile("s_nop 1\n\tv_permlane16_swap_b32 %0, %1" : "+v"(o[x]), "+v"(o[y]));
                if constexpr (j == 5) st64(u32x2{o[x], o[y]}, rw, uo, rb + x * 16384);
            }
        });
        tn = ck.now();
        if (wave == 0 && lane == 0) st32(dur, rw, (32 + h) * 128, rb + amap(R_NS));
        if (s > 0 && __builtin_amdgcn_readfirstlane(*lfail) != s) dur = track(dur, tn - tg, s);
        SETV(5, tn + ck.org);
        if (wave == 0) { const u32x2 pl = read_notes(s ? ld64(rw, ms.vn, rb + amap(R_NF)) : first_notes(rw, ms.vn, rb + amap(R_NF), sp)); lfail[1] = mem_time(pl.x, pl.y, tn); }
        bar();
        tn = __builtin_amdgcn_readfirstlane(lfail[1]);
        MARKC(6);
        if (s + KARM < NSEG) {
            const unsigned ra = rec(par, s + KARM);
#pragma unroll
            for (int tp = 0; tp < 5; ++tp) st64(SENT2, rw, uo, ra + tp * 32768);
            if (wave == 0 && lane == 0) st32(sent1(), rw, (32 + h) * 128, ra + amap(R_NS));
        }
        FLUSH(s);
    }
}

DEV void role_VC(const Params& P, unsigned char* ws, int h, int odd, LAS char* lds, int wave, int lane, Spin& sp) {
    constexpr int L_V = 40960;
    const rsrc_t rw = mk_rsrc(ws, (unsigned)WS_END);
    u32x4 wv[3][20], wo[10][4];
    load_vo_rows(P, h, lds, wave, lane, sp, [&](int t, int k, u32x4 f) { wv[t][k] = f; pin_a(wv[t][k]); }, [&](int t, int k, u32x4 f) { wo[t][k] = f; if (t < 2 && k < 2) pin_a(wo[t][k]); });
    const int g = lane >> 4, n = lane & 15;
    const char* xf = (const char*)ws + O_XF;
    repack<10, 2>(lds + L_WO + wave * 20480, lane);
    LAS char* wr = opq(lds + L_WO + wave * 20480 + lane * 16);
    LAS char* l16 = opq(lds + lane * 16);
    auto issue_x = [&](int s) {
#pragma unroll
        for (int i = 0; i < 10; ++i) glds<0>(lane * 16, xf + (size_t)s * 40960 + (wave * 10 + i) * 1024, (unsigned)(uintptr_t)lds + (wave * 10 + i) * 1024);
    };
    const unsigned par = load_par(rw, lane, sp);
    issue_x(odd);
    unsigned seen = 0;
    for (int s = odd; s < NSEG; s += 2) {
        MARK(0);
        vmwait<0>();
        bar();
        if (s >= 2 && wave == 0 && lane == 0) st32((unsigned)(s - 1), rw, (unsigned)(O_CTRL + 4 * (W_VCPROG + 32 * odd + h)), 0);
        MARK(1);
        f32x4 acc[3][2];
        u32x4 xb[40];
#pragma unroll
        for (int i = 0; i < 4; ++i) xb[i] = lds128(l16 + (i & 1) * 20480 + (i >> 1) * 1024);
#pragma unroll
        for (int i = 0; i < 40; ++i) {
#pragma unroll
            for (int t = 0; t < 3; ++t) {
                mf_av(acc[t][i & 1], wv[t][i >> 1], xb[i], i < 2);
                if (t == 0 && i + 4 < 40) xb[i + 4] = lds128(l16 + (i & 1) * 20480 + ((i + 4) >> 1) * 1024);
            }
        }
        mf_done(acc[0][0], acc[1][0], acc[2][0]); mf_done(acc[0][1], acc[1][1], acc[2][1]);
#pragma unroll
        for (int t = 0; t < 3; ++t)
#pragma unroll
            for (int T = 0; T < 2; ++T) {
                const int et = wave * 3 + t;
                const int off = (et >> 1) * 2048 + T * 1024 + ((2 * (et & 1) + (g >> 1)) * 16 + n) * 16 + (g & 1) * 8;
                *(LAS u32x2*)(lds + L_V + off) = u32x2{pk(acc[t][T][0], acc[t][T][1]), pk(acc[t][T][2], acc[t][T][3])};
            }
        MARK(2);
        wait_slot(rw, s, seen, h & 7, sp);
        bar();
        MARK(3);
        u32x4 va[6][2], st[10][2];
        f32x4 c[10][2];
#pragma unroll
        for (int k = 0; k < 6; ++k)
#pragma unroll
            for (int T = 0; T < 2; ++T) va[k][T] = lds128(l16 + L_V + k * 2048 + T * 1024);
        st[0][0] = lds128(wr); st[0][1] = lds128(wr + 10240);
        const char* xn = xf + (size_t)(s + 2 < NSEG ? s + 2 : s) * 40960 + wave * 10240;
        const unsigned xd = (unsigned)(uintptr_t)lds + wave * 10240;
        auto fin = [&](int t) {
            st128(u32x4{pk(c[t][0][0], c[t][0][1]), pk(c[t][0][2], c[t][0][3]), pk(c[t][1][0], c[t][1][1]), pk(c[t][1][2], c[t][1][3])}, rw,
                  (h >> 3) * 16384 + (h & 7) * 1024 + lane * 16, (page(par, s, 2 + (wave >> 1)) ^ (CMIX ? ((h >> 3) & 1) << 13 : 0)) + ((wave & 1) * 10 + t) * 65536);
        };
#pragma unroll
        for (int t = 0; t < 10; ++t) {
#pragma unroll
            for (int k = 0; k < 6; ++k) {
                const u32x4 b = k < 4 ? wo[t][k & 3] : st[t][k & 1];
                if (t < 2 && k < 2) { mf_va(c[t][0], va[k][0], b, k == 0); mf_va(c[t][1], va[k][1], b, k == 0); }
                else { mf_vv(c[t][0], va[k][0], b, k == 0); mf_vv(c[t][1], va[k][1], b, k == 0); }
                if (k < 2 && t + 1 < 10) st[t + 1][k] = lds128(wr + k * 10240 + (t + 1) * 1024);
                if (k == 5 && t < 5) { glds<0>(lane * 16, xn + 2 * t * 1024, xd + 2 * t * 1024); }
                if (k == 2 && t < 5) { glds<1024>(lane * 16, xn + 2 * t * 1024, xd + 2 * t * 1024); }
                if (k == 3 && t >= 1) { pin_v(c[t - 1][0]); pin_v(c[t - 1][1]); fin(t - 1); }
            }
        }
        mf_done(c[9][0], c[9][1]);
        fin(9);
        MARK(4); FLUSH(s);
    }
    vmwait<0>();
    bar();
    if (wave == 0 && lane == 0) st32((unsigned)NSEG, rw, (unsigned)(O_CTRL + 4 * (W_VCPROG + 32 * odd + h)), 0);
}

DEV void role_QK(const Params& P, unsigned char* ws, int cu, LAS char* lds, int wave, int lane, Spin& sp) {
    const rsrc_t rw = mk_rsrc(ws, (unsigned)WS_END);
    u32x4 w[4][20];
    const int sel = cu >= 24, c0 = (cu - 24 * sel) * 256 + wave * 64;
    stream_w<4, 20, 20, 10, 0, true>((const char*)(sel ? P.wk : P.wq) + c0 * 2, 12288u, lds, wave * 40960, lane,
                                 [&](int t, int k, u32x4 f) { w[t][k] = f; if (k < 16) pin_a(w[t][k]); });
    vmwait<0>();
    bar();
    const char* xf = (const char*)ws + O_XF;
    auto issue_x = [&](int s) {
#pragma unroll
        for (int i = 0; i < 10; ++i) glds<0>(lane * 16, xf + (size_t)s * 40960 + (wave * 10 + i) * 1024, (unsigned)(uintptr_t)lds + (s & 1) * 40960 + (wave * 10 + i) * 1024);
    };
    const unsigned par = load_par(rw, lane, sp);
    issue_x(0);
    const unsigned ol = (sel ? R_KX : R_QX) + (c0 >> 5) * 2048, ob = ((ol >> 13) << 14) | (ol & 8191u);
    unsigned seen = 0;
    for (int s = 0; s < NSEG; ++s) {
        MARK(0);
        vmwait<0>();
        bar();
        MARK(1);
        if (s >= 1 && wave == 0 && lane == 0) st32((unsigned)s, rw, (unsigned)(O_CTRL + 4 * (W_QKPROG + 32 * cu)), 0);
        wait_slot(rw, s, seen, cu & 7, sp);
        LAS char* l16 = opq(lds + (s & 1) * 40960 + lane * 16);
        f32x4 acc[4][2];
        u32x4 xb[40];
#pragma unroll
        for (int i = 0; i < 4; ++i) xb[i] = lds128(l16 + (i & 1) * 20480 + (i >> 1) * 1024);
        const char* xn = xf + (size_t)(s + 1 < NSEG ? s + 1 : s) * 40960 + wave * 10240;
        const unsigned xd = (unsigned)(uintptr_t)lds + (~s & 1) * 40960 + wave * 10240;
#pragma unroll
        for (int i = 0; i < 40; ++i)
#pragma unroll
            for (int t = 0; t < 4; ++t) {
                if (t == 2 && i % 3 == 0 && i < 30) glds<0>(lane * 16, xn + (i / 3) * 1024, xd + (i / 3) * 1024);
                if (i < 32) mf_av(acc[t][i & 1], w[t][i >> 1], xb[i], i < 2); else mf_vv(acc[t][i & 1], w[t][i >> 1], xb[i], false);
                if (t == 0 && i + 4 < 40) xb[i + 4] = lds128(l16 + (i & 1) * 20480 + ((i + 4) >> 1) * 1024);
            }
        mf_done(acc[0][0], acc[1][0], acc[2][0], acc[3][0]); mf_done(acc[0][1], acc[1][1], acc[2][1], acc[3][1]);
#pragma unroll
        for (int pr = 0; pr < 2; ++pr)
#pragma unroll
            for (int T = 0; T < 2; ++T) {
                const f32x4 a = acc[2 * pr][T], b = acc[2 * pr + 1][T];
                st128(u32x4{pk(a[0], a[1]), pk(a[2], a[3]), pk(b[0], b[1]), pk(b[2], b[3])}, rw, pr * 2048 + T * 1024 + lane * 16, page(par, s, 1) + ob);
            }
        MARK(2); MARKC(6); FLUSH(s);
    }
    vmwait<0>();
    bar();
    if (wave == 0 && lane == 0) st32((unsigned)NSEG, rw, (unsigned)(O_CTRL + 4 * (W_QKPROG + 32 * cu)), 0);
}

DEV void role_F(const Params& P, unsigned char* ws, int f, LAS char* lds, int wave, int lane, Spin& sp) {
    constexpr int L_FL = 131072, L_U = L_FL + 1024;
    const rsrc_t rw = mk_rsrc(ws, (unsigned)WS_END);
    volatile LAS unsigned* fl = (volatile LAS unsigned*)(lds + L_FL);
    if (wave == 0 && lane < 8) fl[lane] = lane == 5 ? ~0u : 0u;
    bar();
    const unsigned par = load_par(rw, lane, sp);
    const int g = lane >> 4, n = lane & 15;
    const unsigned l16 = lane * 16;
    const unsigned xr = ((f % 10) * 3) & 15;
    auto lwait = [&](int i, int need, unsigned code) {
        for (unsigned c = 0, k = 0; (int)__builtin_amdgcn_readfirstlane(fl[i]) < need;) { if ((++k & 63u) == 0u && sp.fail(c, code)) break; __builtin_amdgcn_s_sleep(LSLEEP); }
        asm volatile("" ::: "memory");
    };
    auto lset = [&](int i, int v) { asm volatile("s_waitcnt lgkmcnt(0)" ::: "memory"); if (lane == 0) fl[i] = (unsigned)v; };
    if (wave == 3) {
        unsigned have0 = 0, have1 = 0;
        const unsigned vf = min(lane, 39) * 128;
        for (int s = 0; s < NSEG; ++s) {
            MARK(0);
            unsigned hv = (s & 1) ? have1 : have0;
            for (unsigned c = 0; hv <= (unsigned)s;) {
                const unsigned v = ld32(rw, (lane & 31) * 4, (unsigned)(O_CTRL + 4 * (W_VCPROG + 32 * (s & 1))));
                if (!any_lane(v <= (unsigned)s)) {
                    hv = ~wave_max(~v);
                    break;
                }
                if (sp.fail(c, 0x300 + s)) break;
                __builtin_amdgcn_s_sleep(2);
            }
            if (s & 1) have1 = hv; else have0 = hv;
            lwait(2, s - 3, 0x341); lwait(3, s - 3, 0x342); lwait(1, s - 1, 0x340);
            MARK(1);
            const unsigned cpg = page(par, s, f < 20 ? 2 : 3) + (f % 20) * 65536;
            const unsigned no = rec(par, s - 1) + amap(R_NF);
            u32x2 nf;
            __builtin_amdgcn_s_sleep(4);
            asm volatile("buffer_load_dwordx2 %0, %1, %2, %3 offen sc1" : "=v"(nf) : "v"(vf), "s"(rw), "s"(no));
#pragma unroll
            for (int i = 0; i < 16; ++i) glds_dev<2, 1024>(l16, ws + ((cpg ^ (CMIX ? (((i ^ xr) >> 2) & 1) << 13 : 0)) + ((i ^ xr) >> 2) * 16384 + ((i ^ xr) & 3) * 2048), (unsigned)(uintptr_t)lds + (s & 3) * 32768 + i * 2048);
            asm volatile("s_waitcnt vmcnt(32)" : "+v"(nf));
            if (s >= 1) {
                for (unsigned c = 0; any_lane(max(nf.x, nf.y) == SENT);) { if (sp.fail(c, 0x390)) break; nf = ld64(rw, vf, no); }
                const u32x2 pl = read_notes(nf);
                if (lane == 0) { fl[6] = pl.x; fl[7] = pl.y; }
                lset(5, s - 1);
            }
            vmwait<0>();
            lset(0, s + 1);
            MARK(2); FLUSH(s);
        }
    } else if (wave == 0) {
        const int idx = (lane & 1) ? 0xEEEE : 0x4444;
        const unsigned mo = amap(R_MEM + f * 256) + (2 * (2 * g + (n & 1)) + ((n >> 1) & 1)) * 16 + (n >> 2) * 4;
        const unsigned psel = (n & 1) ? 0x07060302u : 0x01000504u;
        unsigned pv[4], ps[4];
#pragma unroll
        for (int j = 0; j < 4; ++j) { pv[j] = l16 + ((j ^ xr) & 3) * 1024; const unsigned k = j ^ (xr >> 2); ps[j] = amap(R_P) + (k & 1) * 4096 + (k >> 1) * 16384; }
        Clock ck; ck.org = sp.org;
        unsigned dur = 64, tp = 0, plan = 0, dS = 139, dU = 126;
        unsigned boost = 0, lead = 10;
        __builtin_amdgcn_s_setprio(3);
        for (int s = 0; s < NSEG - 1; ++s) {
            MARK(0);
            lwait(0, s + 1, 0x343);
            if (s == 0) { tp = ck.now() + 20u; plan = tp + dur + 30u + DELTA_B; if (lane == 0) st64(u32x2{tp, dur + 30u}, rw, f * 128, rec(par, 0) + amap(R_NF)); }
            LAS char* cb = opq(lds + (s & 3) * 32768 + l16);
            u32x8 bc[16];
#pragma unroll
            for (int t = 0; t < 16; ++t) bc[t] = cat4(lds128(cb + 2 * t * 1024), lds128(cb + (2 * t + 1) * 1024));
#pragma unroll
            for (int t = 0; t < 16; ++t) pin_a(bc[t]);
            const unsigned rb = rec(par, s), rn = rec(par, s + 1), ub = rb + amap(R_UB) + f * 16384, pb = rb + amap(R_P);
            unsigned so4[4];
#pragma unroll
            for (int j = 0; j < 4; ++j) { so4[j] = rb + ps[j]; asm volatile("" : "+s"(so4[j])); }
            if (s > 0) { lwait(5, s, 0x349); tp = __builtin_amdgcn_readfirstlane(fl[6]); plan = __builtin_amdgcn_readfirstlane(fl[7]); }
            const unsigned tu = tp - lead;
            unsigned res, rns = rn; asm volatile("" : "+s"(rns));
            TA_DECL;
            u32x4 u[8], px[16], pm[4];
            f32x4 d; zero_acc(d);
            MARK2(1);
            ck.wait_fine(tu);
#pragma unroll
            for (int i = 0; i < 8; ++i) u[i] = ld128(rw, l16 + (i & 3) * 1024, ub + (i >> 2) * 4096);
            const unsigned tg = ck.wait_fine(tp);
            SETV(3, tg + ck.org); SETV(4, tp + ck.org); SETV(5, plan - DELTA_B + ck.org); SETV(6, tu + ck.org);
            bool failed = false;
            TA(0); TA(6); TA(7);
            for (unsigned c = 0;;) {
#pragma unroll
                for (int i = 0; i < 16; ++i) px[i] = ld128(rw, pv[i & 3], so4[i >> 2]);
#pragma unroll
                for (int i = 0; i < 4; ++i) pm[i] = ld128(rw, l16 + i * 1024, pb + 32768);
                TA(1);
#pragma unroll
                for (int i = 0; i < 16; ++i) { smf_a(d, px[i], bc[i], idx); if (i == 0) { TA(2); } if (i == 15) { TA(3); } }
#pragma unroll
                for (int i = 0; i < 4; ++i) smf_v(d, pm[i], cat4(u[2 * i], u[2 * i + 1]), idx);
                TA(4);
                unsigned long long bad; float r0, r1;
                asm volatile("s_nop 7\n\tv_add_f32 %2, %4, %5\n\tv_add_f32 %3, %6, %7\n\tv_cvt_pk_bf16_f32 %0, %2, %3\n\tv_cmp_u_f32 %1, %2, %3\n\ts_nop 0\n\t"
                             "v_mov_b32_dpp %2, %0 quad_perm:[1,0,3,2] row_mask:0xf bank_mask:0xf\n\tv_perm_b32 %0, %0, %2, %8\n\tv_min_u32 %0, -2, %0"
                             : "=&v"(res), "=&s"(bad), "=&v"(r0), "=&v"(r1) : "v"(d[0]), "v"(d[1]), "v"(d[2]), "v"(d[3]), "v"(psel));
                if (__builtin_expect(bad == 0, 1)) { SETV(7, c); break; }
                if (c > 0) {
                    unsigned sg = 0; bool miss = false;
#pragma unroll
                    for (int i = 0; i < 8; ++i) miss |= mx4(u[i]) == SENT;
#pragma unroll
                    for (int i = 0; i < 16; ++i) sg |= px[i].x | px[i].y | px[i].z | px[i].w;
#pragma unroll
                    for (int i = 0; i < 4; ++i) sg |= pm[i].x | pm[i].y | pm[i].z | pm[i].w;
                    if (!any_lane(miss | ((sg & 0x80008000u) != 0u))) break;
                }
                if (sp.fail(c, 0x400 + s)) break;
                failed = true;
                zero_acc(d);
#pragma unroll
                for (int i = 0; i < 8; ++i) u[i] = ld128(rw, l16 + (i & 3) * 1024, ub + (i >> 2) * 4096);
            }
            st32(res, rw, mo, rns);
            TA(5);
            const unsigned tpub = ck.now();
            const unsigned ns = ld32(rw, lane * 128, rb + amap(R_NS));
            const unsigned prop = (tpub + VIS <= plan ? plan : tpub + LATE) + max(dS, dU + 8u) + DELTA_A + (failed ? boost : 0u) + (s < NEARLY ? EARLY : 0u);
            boost = failed ? min(boost + 40u, 400u) : 0u;
            if (lane == 0) { st64(u32x2{prop, dur + (s < NEARLY ? EARLY / 2 : 0u)}, rw, f * 128, rn + amap(R_NF)); fl[1] = (unsigned)(s + 1); }
            if (!failed && tg - tp < 15u) dur = track(dur, tpub - tp, s);
            SETV(2, tpub + ck.org);
            lwait(2, s - 1, 0x346); lwait(3, s - 1, 0x347);
#pragma unroll
            for (int i = 0; i < 8; ++i) *(LAS u32x4*)(lds + L_U + (s & 1) * 8192 + i * 1024 + l16) = u[i];
            lset(4, s + 1);
            tp = prop; plan = prop + dur + DELTA_B; lead = max(dS, dU + 8u) - dU;
            if (!any_lane(ns == SENT)) { const unsigned m = half_max(min(ns, 400u)); dS = __builtin_amdgcn_readlane(m, 0); dU = __builtin_amdgcn_readlane(m, 32); }
            if (f == 0 && lane < 8) st32((unsigned)(s + 1), rw, lane * 128, (unsigned)(O_CTRL + 4 * W_PROG));
            if (s + KARM < NSEG) { const unsigned ra = rec(par, s + KARM); st32(sent1(), rw, mo, ra); if (lane == 0) st64(SENT2, rw, f * 128, ra + amap(R_NF)); }
            FLUSH(s); TA_FLUSH(s);
        }
        {
            const unsigned ub = rec(par, NSEG - 1) + amap(R_UB) + f * 16384;
            u32x4 u[8];
            ck.wait_until(tp);
            lset(1, NSEG);
            for (unsigned c = 0;;) {
                unsigned m = 0;
#pragma unroll
                for (int i = 0; i < 8; ++i) u[i] = ld128(rw, l16 + (i & 3) * 1024, ub + (i >> 2) * 4096);
#pragma unroll
                for (int i = 0; i < 8; ++i) m = max(m, mx4(u[i]));
                if (!any_lane(m == SENT) || sp.fail(c, 0x4FF)) break;
            }
            lwait(2, NSEG - 2, 0x346); lwait(3, NSEG - 2, 0x347);
#pragma unroll
            for (int i = 0; i < 8; ++i) *(LAS u32x4*)(lds + L_U + ((NSEG - 1) & 1) * 8192 + i * 1024 + l16) = u[i];
            lset(4, NSEG);
        }
    } else {
        auto xrows = [&](auto TQ) __attribute__((always_inline)) {
            constexpr int Tq = decltype(TQ)::value, NX = Tq ? 32 : 16;
            const rsrc_t rx = mk_rsrc(P.x, S_ * D_ * 2);
            const unsigned ho = (f >> 1) * 1024 + ((2 * (f & 1) + (g >> 1)) * 16 + n) * 16 + (g & 1) * 8;
            for (int s = 0; s < NSEG; ++s) {
                MARK(0);
                lwait(0, s + 1, 0x345);
                LAS char* cb = opq(lds + (s & 3) * 32768 + l16);
                u32x4 ca[NX];
#pragma unroll
                for (int i = 0; i < NX; ++i) ca[i] = Tq ? lds128(cb + i * 1024) : cat2(*(LAS const u32x2*)(cb + i * 2048), *(LAS const u32x2*)(cb + i * 2048 + 1024));
#pragma unroll
                for (int i = 0; i < NX; ++i) pin_a(ca[i]);
                const unsigned pb = rec(par, s) + amap(R_PX) + Tq * amap(PX_B);
                lwait(1, s + 1, 0x344);
                MARK2(1);
                f32x4 d; u32x2 xv;
                for (unsigned c = 0;;) {
                    u32x4 u[8], px[NX + 8];
#pragma unroll
                    for (int i = 0; i < NX; ++i) {
                        if (Tq) { const unsigned k = (i >> 1) ^ xr; px[i] = ld128(rw, l16 + (i & 1) * 1024, pb + (k & 3) * 2048 + (k >> 2) * 16384); }
                        else { const unsigned k = i ^ xr; px[i] = ld128(rw, l16, pb + (k & 7) * 1024 + (k >> 3) * 16384); }
                    }
#pragma unroll
                    for (int i = 0; i < 8; ++i) px[NX + i] = ld128(rw, l16 + (i & 3) * 1024, pb + 65536 + (i >> 2) * 4096);
                    xv = __builtin_amdgcn_raw_buffer_load_b64(rx, ((32 * s + 16 * Tq + n) * D_ + 16 * f + 4 * g) * 2, 0, 0);
                    lwait(4, s + 1, 0x348);
#pragma unroll
                    for (int i = 0; i < 8; ++i) u[i] = lds128(lds + L_U + (s & 1) * 8192 + i * 1024 + l16);
#pragma unroll
                    for (int i = 0; i < NX; ++i) mf_av(d, ca[i], px[i], i == 0);
#pragma unroll
                    for (int i = 0; i < 8; ++i) mf_vv(d, u[i], px[NX + i], false);
                    mf_done(d);
                    if (!any_lane(__builtin_isunordered(d[0] + d[1], d[2] + d[3]))) { SETV(7, c); break; }
                    bool miss = false;
#pragma unroll
                    for (int i = 0; i < NX + 8; ++i) miss |= mx4(px[i]) == SENT;
                    if (!any_lane(miss)) break;
                    if (sp.fail(c, 0x500 + s)) break;
                }
                MARK(2);
                lset(2 + Tq, s + 1);
                const unsigned a01 = pk(d[0], d[1]), a23 = pk(d[2], d[3]);
                st64(u32x2{pks(bflo(xv.x) + bflo(a01), bfhi(xv.x) + bfhi(a01)), pks(bflo(xv.y) + bflo(a23), bfhi(xv.y) + bfhi(a23))}, rw, ho, (unsigned)(O_HF + (size_t)(2 * s + Tq) * XF_B));
                if (2 * (s + KARM) < UTAIL) st64(SENT2, rw, ho, (unsigned)(O_HF + (size_t)(2 * (s + KARM) + Tq) * XF_B));
                MARK(3); FLUSH(s);
            }
        };
        if (wave == 1) xrows(IC<0>{}); else xrows(IC<1>{});
    }
}

DEV float gelu_t(float x) {
    const float t = x * __builtin_fmaf(x * x, -0.10294324f, -2.30220820f);
    return x * __builtin_amdgcn_rcpf(1.f + __builtin_amdgcn_exp2f(t));
}

template <int NK> DEV bool any_sent(const u32x4 (&x)[NK]) {
    unsigned m = 0;
#pragma unroll
    for (int k = 0; k < NK; ++k) m = max(m, mx4(x[k]));
    return any_lane(m == SENT);
}
template <int U0, int NSET> DEV int next_unit(int u) { return u < U0 - 1 ? u + 1 : u >= U0 ? u + NSET : NSET == 4 ? U0 + 2 : NUNIT; }
template <int U0, int NSET> DEV int first_unit(int rep) { return rep == 0 ? 0 : rep < 3 ? U0 + rep - 1 : U0 + NSET - 1; }
template <int MODE>
DEV void role_W(const Params& P, unsigned char* ws, int cb, int rep, LAS char* lds, int wave, int lane, Spin& sp) {
    constexpr int U0 = MODE == 1 ? U0_1 : MODE == 2 ? U0_2 : U0_3, NSET = 4;
    const rsrc_t rw = mk_rsrc(ws, (unsigned)WS_END);
    constexpr unsigned stride = MODE == 3 ? F_ * 2 : D_ * 2;
    const char* wsrc = (const char*)(MODE == 1 ? P.w1 : MODE == 2 ? P.w2 : P.w3) + (size_t)wave * 160 * stride + cb * 256;
    u32x4 w[8][5];
    stream_w<8, 5, 5, 5, 0, true>(wsrc, stride, lds, wave * 40960, lane, [&](int t, int k, u32x4 f) { w[t][k] = f; pin_a(w[t][k]); }, 0, 2 * wave + 2);
    vmwait<0>();
    bar();
    const int g = lane >> 4, n = lane & 15;
    const unsigned l16 = lane * 16;
    if (rep == 0) load_par<false>(rw, lane, sp);
    constexpr size_t O_IN = MODE == 1 ? O_HF : MODE == 2 ? O_GF : O_X1F;
    LAS char* wr[3];
#pragma unroll
    for (int j = 0; j < 3; ++j) wr[j] = opq(lds + ((wave + 1 + j) & 3) * 6144 + j * 2048 + l16);
    LAS char* rd = opq(lds + wave * 6144 + l16);
    LAS char* sw = opq(lds + 24576 + wave * 512 + lane * 8);
    LAS char* sr = opq(lds + 24576 + ((n >> 2) * 16 + n) * 8);
    u32x4 ones = {0x3F803F80u, 0x3F803F80u, 0x3F803F80u, 0x3F803F80u}; pin_v(ones);
    auto fetch1 = [&](int u, int k) __attribute__((always_inline)) { return ld128(rw, l16, (unsigned)(O_IN + (size_t)u * XF_B) + (wave * 5 + k) * 1024); };
    auto probe = [&](int u) __attribute__((always_inline)) { return ld32(rw, 1008, (unsigned)(O_IN + (size_t)u * XF_B) + (wave * 5 + 4) * 1024); };
    auto product = [&](const u32x4 (&xb)[5], auto&& side, int buf, f32x4& o0, f32x4& o1, float& S1, float& S2) __attribute__((always_inline)) {
        f32x4 acc[8], s1, s2;
        if (MODE != 2) {
#pragma unroll
            for (int k = 0; k < 5; ++k) { mf_vv(s1, ones, xb[k], k == 0); mf_vv(s2, xb[k], xb[k], k == 0); }
        }
        sfor<0, 40>([&](auto I) __attribute__((always_inline)) {
            constexpr int i = decltype(I)::value, t = 2 * (i / 10) + (i & 1), k = (i % 10) >> 1;
            mf_av(acc[t], w[t][k], xb[k], k == 0);
            if constexpr (i % 4 == 0) side(IC<i / 4>{});
            if constexpr (i >= 17 && (i % 10 == 7 || i % 10 == 8)) { constexpr int tw = 2 * (i / 10 - 1) + (i % 10 - 7); pin_v(acc[tw]); *(LAS f32x4*)(wr[tw >> 1] + buf * 28672 + (tw & 1) * 1024) = acc[tw]; }
            if constexpr (MODE != 2 && i == 20) {
                asm volatile("" : "+v"(s1), "+v"(s2));
                const float d = (n & 2) ? ((n & 1) ? s2[3] : s2[2]) : ((n & 1) ? s2[1] : s2[0]);
                *(LAS f32x2*)(sw + buf * 28672) = f32x2{s1[0], d};
            }
        });
        bar();
        f32x4 p[6]; f32x2 q[4];
#pragma unroll
        for (int j = 0; j < 6; ++j) p[j] = *(LAS const f32x4*)(rd + buf * 28672 + j * 1024);
        if (MODE != 2) {
#pragma unroll
            for (int j = 0; j < 4; ++j) q[j] = *(LAS const f32x2*)(sr + buf * 28672 + j * 512);
        }
        mf_done(acc[6], acc[7]);
        o0 = (acc[6] + p[0]) + (p[2] + p[4]); o1 = (acc[7] + p[1]) + (p[3] + p[5]);
        if (MODE != 2) { S1 = (q[0].x + q[1].x) + (q[2].x + q[3].x); S2 = (q[0].y + q[1].y) + (q[2].y + q[3].y); }
    };
    f32x4 cs0 = {}, cs1 = {};
    if (MODE != 2) {
        u32x4 o4[5] = {ones, ones, ones, ones, ones};
        asm volatile("s_nop 1" : "+v"(o4[0]), "+v"(o4[1]), "+v"(o4[2]), "+v"(o4[3]), "+v"(o4[4]));
        float a, b; product(o4, [](auto) {}, 1, cs0, cs1, a, b);
    }
    const int kso = 4 * cb + wave;
    const int grp = kso / 5, sh = grp == 0 ? 1 : grp == 1 ? -1 : grp == 2 ? 2 : -2;
    if (MODE == 1 && rep == 0) {
        const int td = sh > 0 ? n : S_ - 1 - n;
        if (n < (sh > 0 ? sh : -sh)) st128(u32x4{0u, 0u, 0u, 0u}, rw, (td >> 4) * (unsigned)XF_B + kso * 1024 + (g * 16 + (td & 15)) * 16, (unsigned)O_GF);
        vmwait<0>();
    }
    u32x4 xa[5], xc[5], hn; u32x2 sn;
    bool miss = true, ahead = false, quiet = false;
    auto extra = [&](int u) __attribute__((always_inline)) { if (MODE == 2) { hn = ld128(rw, l16, (unsigned)(O_HF + (size_t)u * XF_B) + kso * 1024); sn = ld64(rw, (16 * u + n) * 8, (unsigned)O_ST); } };
    auto unit = [&](int u, int buf, u32x4 (&xb)[5], u32x4 (&nx)[5]) __attribute__((always_inline)) {
        MARK2(0);
        const int tok = 16 * u + n, un = next_unit<U0, NSET>(u), u2 = next_unit<U0, NSET>(un);
        for (unsigned c = 0; miss;) {
            while (!quiet) {
                const unsigned pv = probe(u), gv = u >= UFREE ? ld32(rw, 0, (unsigned)(O_CTRL + 4 * (W_PROG + 32 * (cb & 7)))) : 0u;
                quiet = __builtin_amdgcn_readfirstlane(gv) >= NSEG - 1;
                if (__builtin_amdgcn_readfirstlane(pv) != SENT || sp.fail(c, 0x600 + MODE * 0x100 + u)) break;
                __builtin_amdgcn_s_sleep(PSLEEP);
            }
            __builtin_amdgcn_s_sleep(PWAIT);
#pragma unroll
            for (int k = 0; k < 5; ++k) xb[k] = fetch1(u, k);
            extra(u);
            miss = any_sent(xb);
            if (miss && sp.fail(c, 0x680 + u)) break;
        }
        MARK2(1);
        asm volatile("s_nop 1" : "+v"(xb[0]), "+v"(xb[1]), "+v"(xb[2]), "+v"(xb[3]), "+v"(xb[4]));
        const bool early = ahead;
        unsigned pr = SENT;
        if ((early ? u2 : un) < NUNIT) pr = probe(early ? u2 : un);
        u32x4 hv = hn; u32x2 stt = sn;
        if (MODE == 2) asm volatile("" : "+v"(hv), "+v"(stt));
        f32x4 o0, o1; float S1, S2;
        product(xb, [&](auto J) __attribute__((always_inline)) {
            constexpr int j = decltype(J)::value;
            if constexpr (j < 5) { if (early) nx[j] = fetch1(un, j); }
            if constexpr (j == 5) { if (early) extra(un); }
        }, buf, o0, o1, S1, S2);
        if (!early) {
            ahead = __builtin_amdgcn_readfirstlane(pr) != SENT;
            if (ahead) {
#pragma unroll
                for (int k = 0; k < 5; ++k) nx[k] = fetch1(un, k);
                extra(un);
                pr = SENT;
                if (u2 < NUNIT) pr = probe(u2);
            }
        }
        auto settle = [&]() __attribute__((always_inline)) { __builtin_amdgcn_s_waitcnt(0x0F70); miss = !ahead || any_sent(nx); ahead = ahead && __builtin_amdgcn_readfirstlane(pr) != SENT; if (MODE == 2) asm volatile("" : "+v"(hn), "+v"(sn)); };
        MARK2(2);
        if (MODE != 2) {
            const float mu = S1 * (1.f / D_), var = fmaxf(S2 * (1.f / D_) - mu * mu, 0.f);
            float al = __builtin_amdgcn_rsqf(var + 1e-5f);
            if (MODE == 1) {
                if (cb == 0 && wave == 0 && g == 0) st64(u32x2{nosent(f2u(mu)), nosent(f2u(al))}, rw, tok * 8, (unsigned)O_ST);
                al *= __builtin_amdgcn_rsqf(var * al * al + 1e-5f);
            }
            float y[8];
#pragma unroll
            for (int r = 0; r < 4; ++r) { y[r] = gelu_t(al * (o0[r] - mu * cs0[r])); y[4 + r] = gelu_t(al * (o1[r] - mu * cs1[r])); }
            u32x4 o = {pks(y[0], y[1]), pks(y[2], y[3]), pks(y[4], y[5]), pks(y[6], y[7])};
            pin_v(o);
            settle();
            if (MODE == 1) {
                const int td = tok + sh;
                const unsigned vo = (td >> 4) * (int)XF_B + kso * 1024 + (g * 16 + (td & 15)) * 16;
                if (td >= 0 && td < S_) st128(o, rw, vo, (unsigned)O_GF);
                if (td + 32 * KARM < 16 * UTAIL) st128(SENT4, rw, vo + 2 * KARM * (unsigned)XF_B, (unsigned)O_GF);
            } else {
                st128(o, rw, l16, (unsigned)(O_G3F + (size_t)u * G3_B) + kso * 1024);
                if (u + 2 * KARM < UTAIL) st128(SENT4, rw, l16, (unsigned)(O_G3F + (size_t)(u + 2 * KARM) * G3_B) + kso * 1024);
            }
        } else {
            for (unsigned c = 0; any_lane(max(mx4(hv), max(stt.x, stt.y)) == SENT);) {
                if (sp.fail(c, 0xA00 + u)) break;
                hv = ld128(rw, l16, (unsigned)(O_HF + (size_t)u * XF_B) + kso * 1024); stt = ld64(rw, tok * 8, (unsigned)O_ST);
            }
            const float mu = u2f(stt.x), al = u2f(stt.y);
            const u32x4 o = {pks((bflo(hv.x) - mu) * al + o0[0], (bfhi(hv.x) - mu) * al + o0[1]), pks((bflo(hv.y) - mu) * al + o0[2], (bfhi(hv.y) - mu) * al + o0[3]),
                             pks((bflo(hv.z) - mu) * al + o1[0], (bfhi(hv.z) - mu) * al + o1[1]), pks((bflo(hv.w) - mu) * al + o1[2], (bfhi(hv.w) - mu) * al + o1[3])};
            settle();
            st128(o, rw, l16, (unsigned)(O_X1F + (size_t)u * XF_B) + kso * 1024);
            if (u + 2 * KARM < UTAIL) st128(SENT4, rw, l16, (unsigned)(O_X1F + (size_t)(u + 2 * KARM) * XF_B) + kso * 1024);
        }
        MARK(3); FLUSH(u);
    };
    for (int u = first_unit<U0, NSET>(rep);;) {
        unit(u, 0, xa, xc); u = next_unit<U0, NSET>(u); if (u >= NUNIT) break;
        unit(u, 1, xc, xa); u = next_unit<U0, NSET>(u); if (u >= NUNIT) break;
    }
}

DEV void role_W4(const Params& P, unsigned char* ws, int cb, int rep, LAS char* lds, int wave, int lane, Spin& sp) {
    constexpr int U0 = U0_4, NSET = 3;
    const rsrc_t rw = mk_rsrc(ws, (unsigned)WS_END);
    const rsrc_t ro = mk_rsrc(P.out, S_ * D_ * 2);
    u32x4 w[4][12];
    stream_w<4, 12, 12, 10, 0, false>((const char*)P.w4 + (size_t)wave * 384 * 1280 + cb * 128, 1280u, lds, wave * 40960, lane,
                                  [&](int t, int k, u32x4 f) { w[t][k] = f; pin_a(w[t][k]); }, 0, wave + 1);
    vmwait<0>();
    bar();
    const int g = lane >> 4, n = lane & 15;
    const unsigned l16 = lane * 16;
    if (rep == 0) load_par<false>(rw, lane, sp);
    const int ct = 4 * cb + wave;
    const unsigned po = (ct >> 1) * 1024 + ((2 * (ct & 1) + (g >> 1)) * 16 + n) * 16 + (g & 1) * 8;
    LAS char* wr[3];
#pragma unroll
    for (int j = 0; j < 3; ++j) wr[j] = opq(lds + ((wave + 1 + j) & 3) * 3072 + j * 1024 + l16);
    LAS char* rd = opq(lds + wave * 3072 + l16);
    auto fetch = [&](u32x4 (&x)[12], int u) __attribute__((always_inline)) {
#pragma unroll
        for (int k = 0; k < 12; ++k) x[k] = ld128(rw, l16 + (k & 3) * 1024, (unsigned)(O_G3F + (size_t)u * G3_B) + (wave * 12 + (k & ~3)) * 1024);
    };
    auto probe = [&](int u) __attribute__((always_inline)) { return ld32(rw, 1008, (unsigned)(O_G3F + (size_t)u * G3_B) + (wave * 12 + 11) * 1024); };
    u32x4 xa[12], xc[12]; u32x2 hn, xn;
    bool miss = true, ahead = false, quiet = false;
    auto extra = [&](int u) __attribute__((always_inline)) { hn = ld64(rw, po, (unsigned)(O_HF + (size_t)u * XF_B)); xn = ld64(rw, po, (unsigned)(O_X1F + (size_t)u * XF_B)); };
    auto unit = [&](int u, int buf, u32x4 (&xb)[12], u32x4 (&nx)[12]) __attribute__((always_inline)) {
        MARK2(0);
        for (unsigned c = 0; miss;) {
            while (!quiet) {
                const unsigned pv = probe(u), gv = u >= UFREE ? ld32(rw, 0, (unsigned)(O_CTRL + 4 * (W_PROG + 32 * (cb & 7)))) : 0u;
                quiet = __builtin_amdgcn_readfirstlane(gv) >= NSEG - 1;
                if (__builtin_amdgcn_readfirstlane(pv) != SENT || sp.fail(c, 0xB00 + u)) break;
                __builtin_amdgcn_s_sleep(PSLEEP);
            }
            __builtin_amdgcn_s_sleep(PWAIT);
            fetch(xb, u); extra(u);
            miss = any_sent(xb);
            if (miss && sp.fail(c, 0xB80 + u)) break;
        }
        MARK2(1);
#pragma unroll
        for (int k = 0; k < 12; ++k) pin_v(xb[k]);
        asm volatile("s_nop 1");
        u32x2 hv = hn, xv = xn;
        const int un = next_unit<U0, NSET>(u), u2 = next_unit<U0, NSET>(un);
        unsigned pr = SENT;
        const bool early = ahead;
        if ((early ? u2 : un) < NUNIT) pr = probe(early ? u2 : un);
        asm volatile("" : "+v"(hv), "+v"(xv));
        f32x4 acc[4];
        sfor<0, 48>([&](auto I) __attribute__((always_inline)) {
            constexpr int i = decltype(I)::value, t = i / 12, k = i % 12;
            mf_av(acc[t], w[t][k], xb[k], k == 0);
            if constexpr (i % 3 == 0 && i < 36) { constexpr int kk = i / 3; if (early) nx[kk] = ld128(rw, l16 + (kk & 3) * 1024, (unsigned)(O_G3F + (size_t)un * G3_B) + (wave * 12 + (kk & ~3)) * 1024); }
            if constexpr (i == 35) { if (early) extra(un); }
            if constexpr (i == 36) { if (!early) { ahead = __builtin_amdgcn_readfirstlane(pr) != SENT; if (ahead) { fetch(nx, un); extra(un); pr = SENT; if (u2 < NUNIT) pr = probe(u2); } } }
            if constexpr (t >= 1 && k == 8) { pin_v(acc[t - 1]); *(LAS f32x4*)(wr[t - 1] + buf * 12288) = acc[t - 1]; }
        });
        bar();
        f32x4 p[3];
#pragma unroll
        for (int j = 0; j < 3; ++j) p[j] = *(LAS const f32x4*)(rd + buf * 12288 + j * 1024);
        mf_done(acc[3]);
        const f32x4 o = (acc[3] + p[0]) + (p[1] + p[2]);
        MARK2(2);
        for (unsigned c = 0; any_lane(max(max(hv.x, hv.y), max(xv.x, xv.y)) == SENT);) {
            if (sp.fail(c, 0xC00 + u)) break;
            hv = ld64(rw, po, (unsigned)(O_HF + (size_t)u * XF_B)); xv = ld64(rw, po, (unsigned)(O_X1F + (size_t)u * XF_B));
        }
        const u32x2 r = {pk(bflo(hv.x) + (bflo(xv.x) + o[0]), bfhi(hv.x) + (bfhi(xv.x) + o[1])), pk(bflo(hv.y) + (bflo(xv.y) + o[2]), bfhi(hv.y) + (bfhi(xv.y) + o[3]))};
        __builtin_amdgcn_s_waitcnt(0x0F70);
        miss = !ahead || any_sent(nx); ahead = ahead && __builtin_amdgcn_readfirstlane(pr) != SENT;
        asm volatile("" : "+v"(hn), "+v"(xn));
        __builtin_amdgcn_raw_buffer_store_b64(r, ro, ((16 * u + n) * D_ + 16 * ct + 4 * g) * 2, 0, 0);
        MARK(3); FLUSH(u);
    };
    for (int u = first_unit<U0, NSET>(rep);;) {
        unit(u, 0, xa, xc); u = next_unit<U0, NSET>(u); if (u >= NUNIT) break;
        unit(u, 1, xc, xa); u = next_unit<U0, NSET>(u); if (u >= NUNIT) break;
    }
}

DEV unsigned char* ws_base(const Params& P) { return P.ws + ((0u - (unsigned)(uintptr_t)P.ws) & ((2u << 20) - 1u)); }

DEV void publish_ready(rsrc_t rw, int i, unsigned near, int tid, Spin& sp) {
    sp.start();
    vmwait<0>();
    bar();
    if (tid == 0) st128(u32x4{sp.tag, near, rawnow() - 3000u, ~sp.tag}, rw, i * 128, (unsigned)(O_CTRL + 4 * W_READY));
}
DEV void init_F(const Params& P, unsigned char* ws, int f, unsigned near, LAS char* lds, int wave, int lane, Spin& sp) {
    const rsrc_t rw = mk_rsrc(ws, (unsigned)WS_END);
    const int tid = wave * 64 + lane;
    LAS float* red = (LAS float*)lds;
    const int r = tid & 15, pc = tid >> 4;
    const rsrc_t rx = mk_rsrc(P.x, S_ * D_ * 2);
    u32x4 v[4][5];
#pragma unroll
    for (int j = 0; j < 4; ++j)
#pragma unroll
        for (int i = 0; i < 5; ++i) v[j][i] = __builtin_amdgcn_raw_buffer_load_b128(rx, (r * D_ + (pc + 16 * i) * 8) * 2, min(f + 40 * j, NUNIT - 1) * (16 * D_ * 2), 0);
    if (f < 3) {
        const int p = f * 256 + tid;
        if (p < 640) {
            const unsigned* src = (const unsigned*)(P.mem0 + ((p >> 1) & 7) * D_ + (p >> 4) * 16 + (p & 1) * 2);
            const u32x4 m = {nosent(src[0]), nosent(src[2]), nosent(src[4]), nosent(src[6])};
            const unsigned o = p * 16, oa = ((o >> 13) << 14) | (o & 8191u);
            st128(m, rw, oa, 0); st128(m, rw, oa, 8192);
        }
    }
    float mu[4];
#pragma unroll
    for (int j = 0; j < 4; ++j) {
        float s1 = 0.f;
#pragma unroll
        for (int i = 0; i < 5; ++i)
#pragma unroll
            for (int k = 0; k < 4; ++k) s1 += bflo(v[j][i][k]) + bfhi(v[j][i][k]);
        s1 = x32(x16(s1, false), false);
        red[(j * 2) * 64 + wave * 16 + r] = s1;
    }
    bar();
#pragma unroll
    for (int j = 0; j < 4; ++j) {
        const LAS float* q = red + (j * 2) * 64 + r;
        mu[j] = ((q[0] + q[16]) + (q[32] + q[48])) * (1.f / D_);
        float s2 = 0.f;
#pragma unroll
        for (int i = 0; i < 5; ++i)
#pragma unroll
            for (int k = 0; k < 4; ++k) { const float a = bflo(v[j][i][k]) - mu[j], c = bfhi(v[j][i][k]) - mu[j]; s2 += a * a + c * c; }
        s2 = x32(x16(s2, false), false);
        red[(j * 2 + 1) * 64 + wave * 16 + r] = s2;
    }
    bar();
#pragma unroll
    for (int j = 0; j < 4; ++j) {
        const LAS float* q = red + (j * 2 + 1) * 64 + r;
        const float rs = __builtin_amdgcn_rsqf(((q[0] + q[16]) + (q[32] + q[48])) * (1.f / D_) + 1e-5f);
        if (f + 40 * j < NUNIT) {
#pragma unroll
            for (int i = 0; i < 5; ++i) {
                u32x4 o;
#pragma unroll
                for (int k = 0; k < 4; ++k) o[k] = pk((bflo(v[j][i][k]) - mu[j]) * rs, (bfhi(v[j][i][k]) - mu[j]) * rs);
                st128(o, rw, (pc + 16 * i) * 256 + r * 16, (unsigned)(O_XF + (size_t)(f + 40 * j) * XF_B));
            }
        }
    }
    publish_ready(rw, f, near, tid, sp);
    bar();
}
DEV void init_arm(unsigned char* ws, int i, int tid, Spin& sp) {
    const rsrc_t rw = mk_rsrc(ws, (unsigned)WS_END);
    const int q = i * 256 + tid;
    constexpr int NT = 40 * 256;
    const u32x4 sv = SENT4;
    if (q < (W_READY - W_QKPROG) / 4) st128(u32x4{0u, 0u, 0u, 0u}, rw, q * 16, (unsigned)(O_CTRL + 4 * W_QKPROG));
    if (q < S_ / 2) st128(sv, rw, q * 16, (unsigned)O_ST);
    constexpr int RU = REC_BLOCKS * 1024;
    for (int r = 0; r < KARM; ++r)
        for (int j = q; j < RU; j += NT) if (r > 0 || j >= 2048) st128(sv, rw, j * 16, (unsigned)r * 4 * PAGE_B);
    constexpr int NA = (2 * KARM + 1) * (int)(XF_B / 16), NG = 2 * KARM * (int)(G3_B / 16);
    for (int j = q; j < NA; j += NT) { st128(sv, rw, j * 16, (unsigned)O_HF); st128(sv, rw, j * 16, (unsigned)O_GF); st128(sv, rw, j * 16, (unsigned)O_X1F); }
    for (int j = q; j < NG; j += NT) st128(sv, rw, j * 16, (unsigned)O_G3F);
    constexpr int TA = (NUNIT - UTAIL) * (int)(XF_B / 16), TG = (NUNIT - UTAIL) * (int)(G3_B / 16);
    for (int j = q; j < TA; j += NT) { st128(sv, rw, j * 16, (unsigned)(O_HF + UTAIL * XF_B)); st128(sv, rw, j * 16, (unsigned)(O_GF + UTAIL * XF_B)); st128(sv, rw, j * 16, (unsigned)(O_X1F + UTAIL * XF_B)); }
    for (int j = q; j < TG; j += NT) st128(sv, rw, j * 16, (unsigned)(O_G3F + UTAIL * G3_B));
    publish_ready(rw, 40 + i, 0u, tid, sp);
}

__global__ void __launch_bounds__(256, 1) k_main(Params P) {
    extern __shared__ __attribute__((aligned(16))) char smem[];
    LAS char* lds = (LAS char*)smem;
    const int tid = threadIdx.x, lane = tid & 63, wave = __builtin_amdgcn_readfirstlane(tid >> 6);
    unsigned char* ws = ws_base(P);
    const unsigned first = (xcc_id() & 7u) * 32u + (blockIdx.x >> 3);
    if (first < 128u && (first & 31u) - 8u < 10u) {
        if (wave == 0) {
            const unsigned f = (first >> 5) * 10u + (first & 31u) - 8u;
            const rsrc_t rw = mk_rsrc(ws, (unsigned)WS_END);
            const unsigned o = (f & 31u) * PAGE_B + PAGE_B - 16384u + (f >> 5) * 2048;
            st32(1u, rw, (lane & 1) * 8192, o);
            vmwait<0>();
            unsigned lat[2] = {~0u, ~0u};
#pragma unroll
            for (int i = 0; i < 4; ++i) {
                const int hf = (i ^ (i >> 1)) & 1;
                unsigned long long t0, t1;
                asm volatile("s_memtime %0\n\ts_waitcnt lgkmcnt(0)" : "=s"(t0) :: "memory");
                st32(1u, rw, 0, o + hf * 8192);
                asm volatile("s_waitcnt vmcnt(0)\n\ts_memtime %0\n\ts_waitcnt lgkmcnt(0)" : "=s"(t1) :: "memory");
                lat[hf] = min(lat[hf], (unsigned)(t1 - t0));
            }
            ((LAS unsigned*)lds)[2] = lat[0]; ((LAS unsigned*)lds)[3] = lat[1];
        }
        __builtin_amdgcn_s_barrier();
    }
    Spin sp;
    {
        const char* code; unsigned len;
        asm volatile("s_getpc_b64 %0\n.Lmain_pc:\n\ts_mov_b32 %1, .Lmain_end-.Lmain_pc" : "=s"(code), "=s"(len));
        sp.cpf = __builtin_amdgcn_raw_buffer_load_b32(mk_rsrc(code, len & ~3u), ((blockIdx.x >> 3) * 256 + tid) * 128, 0, 0);
    }
    gu32* ctrl = (gu32*)(ws + O_CTRL);
    sp.tmo = ctrl + W_TMO; sp.dead = false;
    sp.ev = __hip_atomic_load(ctrl + W_EPOCH, RLX_AGENT);
    if (tid == 0) {
        unsigned slot = first;
        for (int k = 0; k < 256; ++k, slot = (slot + 1u) & 255u)
            if (__hip_atomic_exchange((GAS unsigned long long*)(ctrl + W_CLAIM) + slot * 2u, (sp.ev + 1u) | ((unsigned long long)~(sp.ev + 1u) << 32), RLX_AGENT) != ((sp.ev + 1u) | ((unsigned long long)~(sp.ev + 1u) << 32))) break;
        *(LAS unsigned*)lds = slot;
    }
    __syncthreads();
    const int slot = __builtin_amdgcn_readfirstlane(*(LAS unsigned*)lds);
    const unsigned near = slot == (int)first && __builtin_amdgcn_readfirstlane(((LAS unsigned*)lds)[3] < ((LAS unsigned*)lds)[2]);
    __syncthreads();
    const int xq = slot >> 5, t = slot & 31;
    int mlp = -1, rep = 0;
    if (DLY1 && xq < 4 && (t < 8 || (t >= 18 && t < 26))) { const unsigned t0 = rawnow(); while (rawnow() - t0 < DLY1) __builtin_amdgcn_s_sleep(4); }
    if (xq < 4) {
        if (t < 8) role_S(P, ws, xq * 8 + t, lds, wave, lane, sp);
        else if (t < 18) { init_F(P, ws, xq * 10 + (t - 8), near, lds, wave, lane, sp); role_F(P, ws, xq * 10 + (t - 8), lds, wave, lane, sp); }
        else if (t < 26) role_UV(P, ws, xq * 8 + (t - 18), lds, wave, lane, sp);
        else mlp = t < 26 + MLP0 ? xq * MLP0 + (t - 26) : 32 + xq * 2 + (t - 30);
    } else {
        const int y = xq - 4;
        if (t < 16) { role_VC(P, ws, y * 8 + (t >> 1), t & 1, lds, wave, lane, sp); mlp = (t >> 1) * 4 + y; rep = 1 + (t & 1); }
        else if (t < 28) { role_QK(P, ws, y * 12 + (t - 16), lds, wave, lane, sp); if (t < 24) { mlp = (t - 16) * 4 + y; rep = 3; } }
        else mlp = MLP0 == 4 ? 16 + y * 4 + (t - 28) : t < 30 ? 24 + y * 2 + (t - 28) : 32 + y * 2 + (t - 30);
    }
    if (mlp >= 0) {
        if (rep == 0) {
            const unsigned t0 = rawnow();
            init_arm(ws, mlp, tid, sp);
            while (rawnow() - t0 < DLY2) __builtin_amdgcn_s_sleep(4);
        }
        if (mlp < 5) role_W<1>(P, ws, mlp, rep, lds, wave, lane, sp);
        else if (mlp < 10) role_W<2>(P, ws, mlp - 5, rep, lds, wave, lane, sp);
        else if (mlp < 22) role_W<3>(P, ws, mlp - 10, rep, lds, wave, lane, sp);
        else if (mlp < 32) role_W4(P, ws, mlp - 22, rep, lds, wave, lane, sp);
        if (mlp == 22 && rep == 0 && tid == 0) __hip_atomic_store(ctrl + W_EPOCH, sp.tag, RLX_AGENT);
    }
    MARK(0); MARKC(1); FLUSH(129);
    asm volatile(".Lmain_end:");
}

extern "C" void kernel_launch(void* const* d_in, const int* in_sizes, int n_in, void* d_out, int out_size, void* d_ws, size_t ws_size, hipStream_t stream) {
    Params P;
    P.x = (const unsigned short*)d_in[0]; P.mem0 = (const unsigned short*)d_in[1];
    P.wq = (const unsigned short*)d_in[2]; P.wk = (const unsigned short*)d_in[3]; P.wv = (const unsigned short*)d_in[4]; P.wo = (const unsigned short*)d_in[5];
    P.w1 = (const unsigned short*)d_in[8]; P.w2 = (const unsigned short*)d_in[9]; P.w3 = (const unsigned short*)d_in[10]; P.w4 = (const unsigned short*)d_in[11];
    P.out = (unsigned short*)d_out; P.ws = (unsigned char*)d_ws;
    (void)hipFuncSetAttribute((const void*)k_main, hipFuncAttributeMaxDynamicSharedMemorySize, LDS_BYTES);
    hipLaunchKernelGGL(k_main, dim3(256), dim3(256), LDS_BYTES, stream, P);
}
```

```cpp
#include <hip/hip_runtime.h>
#include <stdint.h>

#define GAS __attribute__((address_space(1)))
#define LAS __attribute__((address_space(3)))
#define RLX_AGENT __ATOMIC_RELAXED, __HIP_MEMORY_SCOPE_AGENT
#define DEV __device__ __forceinline__
typedef __bf16 bf16x2 __attribute__((ext_vector_type(2)));
typedef __bf16 bf16x8 __attribute__((ext_vector_type(8)));
typedef __bf16 bf16x16 __attribute__((ext_vector_type(16)));
typedef short s16x4 __attribute__((ext_vector_type(4)));
typedef float f32x2 __attribute__((ext_vector_type(2)));
typedef float f32x4 __attribute__((ext_vector_type(4)));
typedef unsigned u32x2 __attribute__((ext_vector_type(2)));
typedef unsigned u32x4 __attribute__((ext_vector_type(4)));
typedef unsigned u32x8 __attribute__((ext_vector_type(8)));
typedef GAS unsigned gu32;
typedef __amdgpu_buffer_rsrc_t rsrc_t;

constexpr int S_ = 2048, D_ = 640, H_ = 32, E_ = 192, F_ = 1536, NSEG = 64, NUNIT = 128;
constexpr unsigned SENT = 0xFFFFFFFFu;
constexpr unsigned SPINCAP = 1u << 18;

constexpr unsigned PAGE_B = 2u << 20;
constexpr int NSLOT = 8;
constexpr unsigned R_MEM = 0;
constexpr unsigned R_P = 2 * 8192;
constexpr unsigned R_UB = 5 * 8192;
constexpr unsigned R_PX = 45 * 8192;
constexpr unsigned PX_B = 40960;
constexpr unsigned R_NF = 55 * 8192;
constexpr unsigned R_NS = 56 * 8192;
constexpr int REC_BLOCKS = 57;
#define DELTA_A 13
#define DELTA_B 14
#define VIS 5
#define SLOW 105
#define BULK_FAR 14
#define PSLEEP 2
#define PWAIT 1
#define DLY1 0
#define DLY2 1500
#define DLY3 0
#define NTMASK 0
#define ROTB 0
#define U0_1 116
#define U0_2 108
#define U0_3 106
#define U0_4 96
#define CMIX 1
#define LSLEEP 1
#define MLP0 6
#define EARLY 36u
#define UTAIL_ 96
#define UFREE 118
#define DECAY 2
#define NEARLY 3
#define NAP 1
#define TOUCH 0
#define QEARLY 0
#define QLAZY 0
#define DPOL " nt"
#define RNT 0
#define LATE 40
#define PACE 10
constexpr unsigned amap(unsigned off) { return ((off >> 13) << 14) | (off & 8191u); }
constexpr int UTAIL = UTAIL_;
constexpr int KARM = 4;
constexpr size_t O_CTRL = (size_t)NSLOT * 4 * PAGE_B, CTRL_BYTES = 65536;
constexpr int W_TMO = 0  , W_EPOCH = 64, W_READY = 12288  , W_CLAIM = 15360  , W_QKPROG = 1024  , W_VCPROG = 4096  , W_PROG = 8192  ;
constexpr size_t O_ST = O_CTRL + CTRL_BYTES + 4096;
constexpr size_t O_HF = O_ST + S_ * 8, XF_B = 20480;
constexpr size_t O_GF = O_HF + NUNIT * XF_B;
constexpr size_t O_X1F = O_GF + NUNIT * XF_B;
constexpr size_t O_G3F = O_X1F + NUNIT * XF_B, G3_B = 49152;
constexpr size_t O_XF = O_G3F + NUNIT * G3_B;
constexpr unsigned QX_B = 12288, R_QX = 0, R_KX = H_ * QX_B;
constexpr size_t WS_END = O_XF + NUNIT * XF_B;
static_assert(WS_END < (238u << 20), "workspace");
constexpr int LDS_BYTES = 160 * 1024;

struct Params;
typedef const __attribute__((address_space(4))) Params KP;
struct Params {
    const unsigned short *x, *mem0, *wq, *wk, *wv, *wo, *w1, *w2, *w3, *w4;
    unsigned short* out;
    unsigned char* ws;
};

DEV unsigned xcc_id() { return (unsigned)__builtin_amdgcn_s_getreg(20 | (3 << 11)) & 0xfu; }
DEV rsrc_t mk_rsrc(const void* p, unsigned bytes) { return __builtin_amdgcn_make_buffer_rsrc((void*)p, 0, bytes, 0x00020000); }
DEV u32x4 ld128(rsrc_t r, unsigned v, unsigned s) { return __builtin_amdgcn_raw_buffer_load_b128(r, v, s, 16); }
template <bool NT> DEV u32x4 ld128p(rsrc_t r, unsigned v, unsigned s) { return __builtin_amdgcn_raw_buffer_load_b128(r, v, s, NT ? 18 : 16); }
DEV u32x2 ld64(rsrc_t r, unsigned v, unsigned s) { return __builtin_amdgcn_raw_buffer_load_b64(r, v, s, 16); }
DEV unsigned ld32(rsrc_t r, unsigned v, unsigned s) { return __builtin_amdgcn_raw_buffer_load_b32(r, v, s, 16); }
DEV void st128(u32x4 d, rsrc_t r, unsigned v, unsigned s) {
    asm("" : "+v"(d));
    __builtin_amdgcn_raw_buffer_store_b128(d, r, v, s, 16);
    asm volatile("s_nop 1" :: "v"(d) : "memory");
}
DEV void st64(u32x2 d, rsrc_t r, unsigned v, unsigned s) { __builtin_amdgcn_raw_buffer_store_b64(d, r, v, s, 16); }
DEV void st32(unsigned d, rsrc_t r, unsigned v, unsigned s) { __builtin_amdgcn_raw_buffer_store_b32(d, r, v, s, 16); }
DEV void st16(unsigned short d, rsrc_t r, unsigned v, unsigned s) { __builtin_amdgcn_raw_buffer_store_b16(d, r, v, s, 16); }
DEV u32x2 trd(LAS const char* p) { return __builtin_bit_cast(u32x2, __builtin_amdgcn_ds_read_tr16_b64_v4i16((LAS s16x4*)p)); }
DEV u32x4 cat2(u32x2 a, u32x2 b) { return __builtin_shufflevector(a, b, 0, 1, 2, 3); }
DEV u32x8 cat4(u32x4 a, u32x4 b) { return __builtin_shufflevector(a, b, 0, 1, 2, 3, 4, 5, 6, 7); }
DEV u32x4 lo4(u32x8 a) { return __builtin_shufflevector(a, a, 0, 1, 2, 3); }
DEV u32x4 hi4(u32x8 a) { return __builtin_shufflevector(a, a, 4, 5, 6, 7); }
DEV u32x4 lds128(LAS const char* p) { return *(LAS const u32x4*)p; }
DEV unsigned pk(float a, float b) { bf16x2 v = {(__bf16)a, (__bf16)b}; return __builtin_bit_cast(unsigned, v); }
DEV unsigned nosent(unsigned u) { return min(u, SENT - 1u); }
DEV unsigned pks(float a, float b) { return min(pk(a, b), SENT - 1u); }
DEV unsigned pka(float a, float b) { unsigned r; asm("v_cvt_pk_bf16_f32 %0, |%1|, |%2|" : "=v"(r) : "v"(a), "v"(b)); return r; }
DEV float bflo(unsigned u) { return __builtin_bit_cast(float, u << 16); }
DEV float bfhi(unsigned u) { return __builtin_bit_cast(float, u & 0xFFFF0000u); }
DEV unsigned f2u(float f) { return __builtin_bit_cast(unsigned, f); }
DEV float u2f(unsigned u) { return __builtin_bit_cast(float, u); }
DEV unsigned mx4(u32x4 v) { return max(max(v.x, v.y), max(v.z, v.w)); }
DEV bool any_lane(bool c) { return __builtin_amdgcn_ballot_w64(c) != 0; }
DEV void bar() { asm volatile("s_waitcnt lgkmcnt(0)" ::: "memory"); __builtin_amdgcn_s_barrier(); asm volatile("" ::: "memory"); }
DEV void fold32(float& a, float& b) {
    auto r = __builtin_amdgcn_permlane32_swap(f2u(a), f2u(b), false, false); a = u2f(r[0]); b = u2f(r[1]);
}
DEV float x32(float v, bool mx) { auto r = __builtin_amdgcn_permlane32_swap(f2u(v), f2u(v), false, false); return mx ? fmaxf(u2f(r[0]), u2f(r[1])) : u2f(r[0]) + u2f(r[1]); }
DEV float x16(float v, bool mx) { auto r = __builtin_amdgcn_permlane16_swap(f2u(v), f2u(v), false, false); return mx ? fmaxf(u2f(r[0]), u2f(r[1])) : u2f(r[0]) + u2f(r[1]); }
DEV float fmax2(float a, float b) { float r; asm("v_max_f32 %0, %1, %2" : "=v"(r) : "v"(a), "v"(b)); return r; }
DEV float fmax3(float a, float b, float c) { float r; asm("v_max3_f32 %0, %1, %2, %3" : "=v"(r) : "v"(a), "v"(b), "v"(c)); return r; }
#define ROWOP2(op) asm volatile("s_nop 1\n\t" \
    op " %0, %0, %0 row_ror:8 row_mask:0xf bank_mask:0xf\n\t" op " %1, %1, %1 row_ror:8 row_mask:0xf bank_mask:0xf\n\ts_nop 0\n\t" \
    op " %0, %0, %0 row_ror:4 row_mask:0xf bank_mask:0xf\n\t" op " %1, %1, %1 row_ror:4 row_mask:0xf bank_mask:0xf\n\ts_nop 0\n\t" \
    op " %0, %0, %0 row_ror:2 row_mask:0xf bank_mask:0xf\n\t" op " %1, %1, %1 row_ror:2 row_mask:0xf bank_mask:0xf\n\ts_nop 0\n\t" \
    op " %0, %0, %0 row_ror:1 row_mask:0xf bank_mask:0xf\n\t" op " %1, %1, %1 row_ror:1 row_mask:0xf bank_mask:0xf" : "+v"(a), "+v"(b))
DEV void row_max2(float& a, float& b) { ROWOP2("v_max_f32_dpp"); }
DEV void row_sum2(float& a, float& b) { ROWOP2("v_add_f32_dpp"); }
DEV unsigned pair_cols(unsigned p, unsigned psel) { return __builtin_amdgcn_perm(p, __builtin_amdgcn_update_dpp(0u, p, 0xB1, 0xf, 0xf, true), psel); }
DEV float ror8(float v) { return u2f(__builtin_amdgcn_update_dpp(0u, f2u(v), 0x128, 0xf, 0xf, true)); }
template <int OFF> DEV void glds(unsigned voff, const void* sbase, unsigned lds_dst) {
    asm volatile("s_mov_b32 m0, %2\n\ts_nop 0\n\tglobal_load_lds_dwordx4 %0, %1 offset:%3 sc1" :: "v"(voff), "s"(sbase), "s"(lds_dst), "n"(OFF) : "memory");
}
template <int N, int STEP> DEV void glds_dev(unsigned voff, const void* sbase, unsigned lds_dst) {
    static_assert(N == 2 || N == 4, "");
    if (N == 2) asm volatile("s_mov_b32 m0, %2\n\ts_nop 0\n\tglobal_load_lds_dwordx4 %0, %1 sc1" DPOL "\n\tglobal_load_lds_dwordx4 %0, %1 offset:%3 sc1" DPOL :: "v"(voff), "s"(sbase), "s"(lds_dst), "n"(STEP) : "memory");
    else asm volatile("s_mov_b32 m0, %2\n\ts_nop 0\n\tglobal_load_lds_dwordx4 %0, %1 sc1" DPOL "\n\tglobal_load_lds_dwordx4 %0, %1 offset:%3 sc1" DPOL "\n\tglobal_load_lds_dwordx4 %0, %1 offset:%4 sc1" DPOL "\n\tglobal_load_lds_dwordx4 %0, %1 offset:%5 sc1" DPOL
                      :: "v"(voff), "s"(sbase), "s"(lds_dst), "n"(STEP), "n"(2 * STEP), "n"(3 * STEP) : "memory");
}
template <int N> DEV void vmwait() { asm volatile("s_waitcnt vmcnt(%0)" :: "n"(N) : "memory"); }
template <class T> DEV void pin_a(T& v) { asm volatile("" : "+a"(v)); }
template <class T> DEV void pin_v(T& v) { asm volatile("" : "+v"(v)); }
#define MF_ "v_mfma_f32_16x16x32_bf16 "
DEV void smf_a(f32x4& c, u32x4 a, u32x8 b, int idx) { asm volatile("v_smfmac_f32_16x16x64_bf16 %0, %1, %2, %3" : "+v"(c) : "v"(a), "a"(b), "v"(idx)); }
DEV void smf_v(f32x4& c, u32x4 a, u32x8 b, int idx) { asm volatile("v_smfmac_f32_16x16x64_bf16 %0, %1, %2, %3" : "+v"(c) : "v"(a), "v"(b), "v"(idx)); }
DEV void mf_vv(f32x4& c, u32x4 a, u32x4 b, bool first) { if (first) asm volatile(MF_ "%0, %1, %2, 0" : "=&v"(c) : "v"(a), "v"(b)); else asm volatile(MF_ "%0, %1, %2, %0" : "+v"(c) : "v"(a), "v"(b)); }
DEV void mf_av(f32x4& c, u32x4 a, u32x4 b, bool first) { if (first) asm volatile(MF_ "%0, %1, %2, 0" : "=&v"(c) : "a"(a), "v"(b)); else asm volatile(MF_ "%0, %1, %2, %0" : "+v"(c) : "a"(a), "v"(b)); }
DEV void mf_va(f32x4& c, u32x4 a, u32x4 b, bool first) { if (first) asm volatile(MF_ "%0, %1, %2, 0" : "=&v"(c) : "v"(a), "a"(b)); else asm volatile(MF_ "%0, %1, %2, %0" : "+v"(c) : "v"(a), "a"(b)); }
DEV void mf_done(f32x4& a) { asm volatile("s_nop 7" : "+v"(a)); }
DEV void mf_done(f32x4& a, f32x4& b) { asm volatile("s_nop 7" : "+v"(a), "+v"(b)); }
DEV void mf_done(f32x4& a, f32x4& b, f32x4& c) { asm volatile("s_nop 7" : "+v"(a), "+v"(b), "+v"(c)); }
DEV void mf_done(f32x4& a, f32x4& b, f32x4& c, f32x4& d) { asm volatile("s_nop 7" : "+v"(a), "+v"(b), "+v"(c), "+v"(d)); }
DEV float zero1() { float v; asm volatile("v_mov_b32 %0, 0" : "=v"(v)); return v; }
DEV void zero_acc(f32x4& a) { a = f32x4{zero1(), zero1(), zero1(), zero1()}; asm volatile("s_nop 1" : "+v"(a)); }
DEV LAS char* opq(LAS char* p) { unsigned u = (unsigned)(uintptr_t)p; asm volatile("" : "+v"(u)); return (LAS char*)(uintptr_t)u; }
template <int V> struct IC { static constexpr int value = V; constexpr operator int() const { return V; } };
template <int I, int N, class F> DEV void sfor(F&& f) { if constexpr (I < N) { f(IC<I>{}); sfor<I + 1, N>(f); } }
DEV void lgkm0() { asm volatile("s_waitcnt lgkmcnt(0)" ::: "memory"); __builtin_amdgcn_sched_barrier(0); }

#define MARK(i)
#define MARK2(i)
#define MARK3(i)
#define SETV(i, v)
#define MARKC(i)
#define FLUSH(step)
#define TA(i)
#define TA_DECL
#define TA_FLUSH(step)
struct Spin {
    gu32* tmo; bool dead, claimed; unsigned tag, slot;
    unsigned org, ev, cpf;
    DEV void entry(int wave, int lane) {
        const char* code; unsigned back, len;
        asm volatile("s_getpc_b64 %0\n.Lpc%=:\n\ts_mov_b32 %1, .Lpc%=-_Z6k_main6Params\n\ts_mov_b32 %2, .Lmain_end-_Z6k_main6Params" : "=s"(code), "=s"(back), "=s"(len));
        asm volatile("buffer_load_dword %0, %2, %3, 0 offen\n\tglobal_load_dword %1, %4, %5 sc1" : "=&v"(cpf), "=&v"(ev)
                     : "v"(((slot & 31u) * 256 + wave * 64 + lane) * 128), "s"(mk_rsrc(code - back, len & ~3u)), "v"(0), "s"(tmo - W_TMO + W_EPOCH) : "memory");
    }
    DEV void start() { asm volatile("v_readfirstlane_b32 %0, %1" : "=s"(tag) : "v"(ev), "v"(cpf)); tag += 1u; }
    u32x2 old; u32x4 ra, rb;
    DEV void early_claim(int wave, int lane) {
        start();
        old = u32x2{0u, 0u};
        if (wave == 0 && lane == 0 && !claimed)
            asm volatile("global_atomic_swap_x2 %0, %1, %2, %3 sc0 sc1" : "+v"(old) : "v"(slot * 16u), "v"(u32x2{tag, ~tag}), "s"(tmo - W_TMO + W_CLAIM) : "memory");
    }
    DEV void early_ready(int lane) {
        asm volatile("global_load_dwordx4 %0, %2, %4 sc1\n\tglobal_load_dwordx4 %1, %3, %4 sc1" : "=&v"(ra), "=&v"(rb) : "v"(lane * 128), "v"((64 + (lane & 15)) * 128), "s"(tmo - W_TMO + W_READY) : "memory");
    }
    DEV bool fail(unsigned& n, unsigned code) {
        asm volatile("" ::: "memory");
        if (dead) return true;
        ++n;
        if (n > SPINCAP) { __hip_atomic_store(tmo + 1, code, RLX_AGENT); __hip_atomic_store(tmo, tag, RLX_AGENT); dead = true; return true; }
        if ((n & 63u) == 0u && (unsigned)__builtin_amdgcn_readfirstlane(__hip_atomic_load(tmo, RLX_AGENT)) == tag) { dead = true; return true; }
        return false;
    }
};

template <int NT> DEV int img_rot(int rho) { return (NT & 1) ? 0 : (NT & 2) ? ((rho >> 2) & 1) : (NT & 4) ? ((rho >> 1) & 3) : (rho & 7); }
DEV int swap23(int r) { return (r & 3) | ((r >> 1) & 4) | ((r << 1) & 8) | (r & 16); }
template <int NT> struct WSrc {
    unsigned voff[NT]; rsrc_t r;
    DEV void init(const char* src, int lane, unsigned stride, int trot = 0) {
        r = mk_rsrc(src - 4096, 0xFFFFFFFFu);
#pragma unroll
        for (int i = 0; i < NT; ++i) {
            const int P = 64 * i + lane, rho = P / (2 * NT), cs = P % (2 * NT);
            const int tile = (((cs >> 1) ^ img_rot<NT>(rho)) + trot) % NT;
            voff[i] = (unsigned)swap23(rho) * stride + (unsigned)(2 * tile + (cs & 1)) * 16u + 4096u - (i & 3) * 1024u;
        }
    }
    DEV void issue(unsigned so, unsigned lds_dst) {
        constexpr bool nt = (NTMASK >> (NT == 6 ? 1 : NT == 10 ? 2 : NT == 3 ? 3 : 0)) & 1;
#define BL_(v, o, p) "buffer_load_dwordx4 " v ", %0, %1 offen offset:" o p " lds\n\t"
#define GRP_(p) \
            if (i + 4 <= NT) asm volatile("s_mov_b32 m0, %2\n\ts_nop 0\n\t" BL_("%3", "0", p) BL_("%4", "1024", p) BL_("%5", "2048", p) BL_("%6", "3072", p) :: "s"(r), "s"(so), "s"(d), "v"(voff[i]), "v"(voff[(i + 1) % NT]), "v"(voff[(i + 2) % NT]), "v"(voff[(i + 3) % NT]) : "memory"); \
            else if (i + 3 == NT) asm volatile("s_mov_b32 m0, %2\n\ts_nop 0\n\t" BL_("%3", "0", p) BL_("%4", "1024", p) BL_("%5", "2048", p) :: "s"(r), "s"(so), "s"(d), "v"(voff[i]), "v"(voff[(i + 1) % NT]), "v"(voff[(i + 2) % NT]) : "memory"); \
            else asm volatile("s_mov_b32 m0, %2\n\ts_nop 0\n\t" BL_("%3", "0", p) BL_("%4", "1024", p) :: "s"(r), "s"(so), "s"(d), "v"(voff[i]), "v"(voff[(i + 1) % NT]) : "memory");
#pragma unroll
        for (int i = 0; i < NT; i += 4) {
            const unsigned d = lds_dst + i * 1024;
            if (nt) { GRP_(" nt") } else { GRP_("") }
        }
#undef GRP_
#undef BL_
    }
};
template <int NT, bool PAIR> struct WRd {
    static constexpr int RR = (NT & 1) ? 1 : (NT & 2) ? 2 : (NT & 4) ? 4 : 8, NB = PAIR ? RR / 2 : RR;
    LAS char* b[NB];
    DEV void init(LAS char* base, int lane) {
        const int g = lane >> 4, q = (lane >> 2) & 3, p = lane & 3, rho = q | ((g & 1) << 2) | ((g >> 1) << 4), rot = img_rot<NT>(rho);
#pragma unroll
        for (int j = 0; j < NB; ++j) b[j] = opq(base + rho * (NT * 32) + (PAIR ? ((2 * j + (p >> 1)) ^ rot) * 32 + 16 * (p & 1) : (j ^ rot) * 32 + 8 * p));
    }
    DEV LAS char* at(int off, int tile) const { return PAIR ? b[(tile >> 1) % NB] + off + (tile / RR) * RR * 32 + 8 * (tile & 1) : b[tile % NB] + off + (tile / RR) * RR * 32; }
    DEV u32x4 get(int off, int tile) const { LAS char* a = at(off, tile); return cat2(trd(a), trd(a + 8 * NT * 32)); }
};
DEV void vmwait_n(int n) {
    switch (n) {
#define VW(n) case n: vmwait<n>(); break;
        VW(0) VW(3) VW(4) VW(6) VW(8) VW(9) VW(10) VW(12) VW(15) VW(16) VW(18) VW(20) VW(24) VW(26) VW(28) VW(29) VW(30) VW(32) VW(35) VW(36) VW(40)
#undef VW
        default: vmwait<0>(); break;
    }
}
template <int NT, int NK, int NKT, int RING, int SROT, bool PAIR, class Sink>
DEV void stream_w(const char* src, unsigned stride, LAS char* lds, unsigned base, int lane, Sink&& sink, int rot = 0, int trot = 0, Spin* sp = nullptr, int wave = 0) {
    auto krow = [&](int k) { int kk = k + 2 * rot; if (kk >= NKT) kk -= NKT; return (unsigned)kk * 32u * stride; };
    if (sp) sp->entry(wave, lane);
    WSrc<NT> ws; ws.init(src, lane, stride, trot);
    const unsigned lbase = (unsigned)(uintptr_t)lds + base;
#pragma unroll
    for (int k = 0; k < RING && k < NKT; ++k) ws.issue(krow(k), lbase + ((k + SROT) % RING) * NT * 1024);
    WRd<NT, PAIR> rd; rd.init(lds + base, lane);
#pragma unroll
    for (int k = 0; k < NK; ++k) {
        const int issued = (k + RING < NKT) ? k + RING : NKT;
        vmwait_n((issued - k - 1) * NT);
        u32x4 fr[NT];
#pragma unroll
        for (int t = 0; t < NT; ++t) fr[t] = rd.get(((k + SROT) % RING) * NT * 1024, t);
#pragma unroll
        for (int t = 0; t < NT; ++t) sink(t, k, fr[t]);
        if (k + RING < NKT) { lgkm0(); ws.issue(krow(k + RING), lbase + ((k + SROT) % RING) * NT * 1024); }
        if (sp) { if (k == 0) sp->early_claim(wave, lane); if (k == NK - 2) sp->early_ready(lane); }
    }
}

template <int NT, int NS> DEV void repack(LAS char* base, int lane) {
    WRd<NT, false> rd; rd.init(base, lane);
#pragma unroll 1
    for (int k = 0; k < NS; ++k) {
        u32x4 fr[NT];
#pragma unroll
        for (int t = 0; t < NT; ++t) fr[t] = rd.get(k * NT * 1024, t);
        lgkm0();
#pragma unroll
        for (int t = 0; t < NT; ++t) *(LAS u32x4*)(base + (k * NT + t) * 1024 + lane * 16) = fr[t];
    }
}
DEV unsigned page(unsigned par, int s, int p) { const unsigned i = (unsigned)(s & (NSLOT - 1)) * 4u + (unsigned)p; return i * PAGE_B + (((par >> i) & 1u) << 13); }
DEV unsigned rec(unsigned par, int s) { return page(par, s, 0); }
DEV void wait_slot(rsrc_t rw, int s, unsigned& seen, int rep, Spin& sp) {
    for (unsigned c = 0; (int)seen + 6 < s;) {
        seen = __builtin_amdgcn_readfirstlane(ld32(rw, 0, (unsigned)(O_CTRL + 4 * (W_PROG + 32 * rep))));
        if ((int)seen + 6 >= s || sp.fail(c, 0x280 + s)) break;
        __builtin_amdgcn_s_sleep(8);
    }
}
template <bool EARLY_> DEV void claim(Spin& sp, volatile LAS unsigned* w, int wave, int lane) {
    if (EARLY_) asm volatile("" : "+v"(sp.old), "+v"(sp.ra), "+v"(sp.rb)); else { sp.entry(wave, lane); vmwait<0>(); sp.start(); }
    if (sp.claimed) return;
    if (wave == 0 && lane == 0) {
        const unsigned long long v = sp.tag | ((unsigned long long)~sp.tag << 32);
        unsigned slot = sp.slot;
        if (!EARLY_ || (sp.old.x == sp.tag && sp.old.y == ~sp.tag))
        for (int k = 0; k < 256; ++k, slot = (slot + 1u) & 255u)
            if (__hip_atomic_exchange((GAS unsigned long long*)(sp.tmo - W_TMO + W_CLAIM) + slot * 2u, v, RLX_AGENT) != v) break;
        *w = slot;
    }
    __syncthreads();
    const unsigned got = __builtin_amdgcn_readfirstlane(*w);
    __syncthreads();
    if (__builtin_expect(got != sp.slot, 0)) {
        asm volatile("s_waitcnt vmcnt(0) lgkmcnt(0)\n\tv_mov_b32 v0, %2\n\ts_mov_b32 s2, %1\n\ts_mov_b64 s[0:1], %0\n\t"
                     "s_getpc_b64 s[4:5]\n.Lre%=:\n\ts_sub_u32 s4, s4, .Lre%=-_Z6k_main6Params\n\ts_subb_u32 s5, s5, 0\n\ts_setpc_b64 s[4:5]"
                     :: "s"(__builtin_amdgcn_kernarg_segment_ptr()), "s"(got | 0x80000000u), "v"(wave * 64 + lane) : "memory", "s0", "s1", "s2", "s4", "s5", "v0");
        __builtin_unreachable();
    }
}
template <bool EARLY_ = false> DEV unsigned load_par(rsrc_t rw, int lane, Spin& sp) {
    u32x4 a, b;
    if (EARLY_) { a = sp.ra; b = sp.rb; }
    for (unsigned c = 0;;) {
        if (!EARLY_ || c) { a = ld128(rw, lane * 128, (unsigned)(O_CTRL + 4 * W_READY)); b = ld128(rw, (64 + (lane & 15)) * 128, (unsigned)(O_CTRL + 4 * W_READY)); }
        if (!any_lane(a.x != sp.tag || a.w != ~sp.tag || b.x != sp.tag || b.w != ~sp.tag) || sp.fail(c, 0x40)) break;
        if (c > 1) __builtin_amdgcn_s_sleep(16);
    }
    unsigned m = (unsigned)__builtin_amdgcn_ballot_w64(a.y == 1u);
    const int c = __builtin_popcount(m & 0xAAAAAAAAu) - __builtin_popcount(m & 0x55555555u);
    if (c >= 5) m = 0xAAAAAAAAu; else if (c <= -5) m = 0x55555555u;
    sp.org = __builtin_amdgcn_readfirstlane(a.z);
    return m ^ (BULK_FAR * 0x11111111u);
}
DEV unsigned sent1() { unsigned v; asm volatile("v_mov_b32 %0, -1" : "=v"(v)); return v; }
#define SENT4 (u32x4{sent1(), sent1(), sent1(), sent1()})
#define SENT2 (u32x2{sent1(), sent1()})
DEV unsigned rawnow() { unsigned long long v; asm volatile("s_memrealtime %0\n\ts_waitcnt lgkmcnt(0)" : "=s"(v) :: "memory"); return (unsigned)v; }
struct Clock {
    unsigned org;
    DEV unsigned now() const { return rawnow() - org; }
    DEV unsigned wait_until(unsigned T) const {
        unsigned t = rawnow(); T += org;
        if ((int)(T - t) > 1000) T = t + 1000u;
        while ((int)(t - T) < 0) t = rawnow();
        return t - org;
    }
    DEV unsigned wait_fine(unsigned T) const {
        unsigned t = rawnow(); T += org;
        if ((int)(T - t) > 1000) T = t + 1000u;
        if ((int)(t - T) >= -2) return t + 2u - org;
        const unsigned E = T - 4u;
        while (NAP && (int)(t - E) < -16) { __builtin_amdgcn_s_sleep(2); t = rawnow(); }
        while ((int)(t - E) < 0) t = rawnow();
        const unsigned rem = T - t;
        if (rem > 2) asm volatile("s_nop 7\n\ts_nop 1");
        if (rem & 1) return T - org;
        asm volatile("s_nop 4");
        return T - org;
    }
};
DEV unsigned umax_dpp(unsigned v, int k) {
    switch (k) { case 1: return max(v, (unsigned)__builtin_amdgcn_update_dpp(0u, v, 0x121, 0xf, 0xf, true)); case 2: return max(v, (unsigned)__builtin_amdgcn_update_dpp(0u, v, 0x122, 0xf, 0xf, true));
                 case 4: return max(v, (unsigned)__builtin_amdgcn_update_dpp(0u, v, 0x124, 0xf, 0xf, true)); default: return max(v, (unsigned)__builtin_amdgcn_update_dpp(0u, v, 0x128, 0xf, 0xf, true)); }
}
DEV unsigned half_max(unsigned v) {
    v = umax_dpp(umax_dpp(umax_dpp(umax_dpp(v, 1), 2), 4), 8);
    auto r = __builtin_amdgcn_permlane16_swap(v, v, false, false);
    return max((unsigned)r[0], (unsigned)r[1]);
}
DEV unsigned wave_max(unsigned v) { const unsigned m = half_max(v); return max((unsigned)__builtin_amdgcn_readlane(m, 0), (unsigned)__builtin_amdgcn_readlane(m, 32)); }
DEV u32x2 read_notes(u32x2 nf) {
    const unsigned tp = wave_max(nf.x == SENT ? 0u : nf.x), df = wave_max(nf.y == SENT ? 0u : min(nf.y, 400u));
    return u32x2{tp, tp + df + DELTA_B};
}
DEV u32x2 first_notes(rsrc_t rw, unsigned vn, unsigned so, Spin& sp) {
    u32x2 nf;
    for (unsigned c = 0;;) { nf = ld64(rw, vn, so); if (!any_lane(max(nf.x, nf.y) == SENT) || sp.fail(c, 0x50)) break; __builtin_amdgcn_s_sleep(2); }
    return nf;
}
DEV unsigned mem_time(unsigned tp, unsigned plan, unsigned tn) { return tn + VIS > tp ? max(plan, tn + SLOW) : plan; }
struct MemStream {
    rsrc_t rw; unsigned l16, vn, lbase, ko[10];
    static DEV int rot_of(int base, int wave) { return (ROTB * base + ((wave * 5 + 1) >> 1)) % 10; }
    DEV void init(rsrc_t r, LAS char* lds, int lane, int rot, bool reader) {
        rw = r; l16 = lane * 16; vn = reader ? min(lane, 39) * 128 : (lane & 15) * 8; lbase = (unsigned)(uintptr_t)lds;
#pragma unroll
        for (int k = 0; k < 10; ++k) { int kk = k + rot; if (kk >= 10) kk -= 10; ko[k] = kk * 1024 + (kk >= 8 ? 8192 : 0); }
    }
    DEV u32x4 ld(unsigned rb, int kb) const { return ld128(rw, l16, rb + ko[kb]); }
    DEV unsigned so(unsigned rb, int kb) const { unsigned v = rb + ko[kb]; asm volatile("" : "+s"(v)); return v; }
    DEV u32x4 lds_(unsigned s) const { u32x4 v = ld128p<(RNT & 8) != 0>(rw, l16, s); __builtin_amdgcn_sched_barrier(0); return v; }
    DEV LAS char* at(int kb) const { unsigned a; asm volatile("v_add_u32 %0, %1, %2" : "=v"(a) : "s"(ko[kb] + lbase), "v"(l16)); return (LAS char*)(uintptr_t)a; }
    DEV void share(unsigned so, int kb) const {
        asm volatile("s_mov_b32 m0, %3\n\ts_nop 0\n\tbuffer_load_dwordx4 %0, %1, %2 offen sc1 lds" :: "v"(l16), "s"(rw), "s"(so), "s"(ko[kb] + lbase) : "memory");
    }
    DEV void lda(u32x4& a, unsigned so) const { asm volatile("buffer_load_dwordx4 %0, %1, %2, %3 offen sc1" : "=v"(a) : "v"(l16), "s"(rw), "s"(so)); }
    DEV u32x4 get(int kb) const { return lds128(at(kb)); }
    static DEV unsigned long long bad(u32x4 a, u32x4 b, u32x4 c, u32x4 d) {
        unsigned long long m = __builtin_amdgcn_ballot_w64(max(max(mx4(a), mx4(b)), max(mx4(c), mx4(d))) == SENT); asm volatile("" : "+s"(m)); return m;
    }
};
template <int N> DEV void vmw(u32x4& a) { asm volatile("s_waitcnt vmcnt(%1)" : "+v"(a) : "n"(N)); }
DEV unsigned long long nan2(float a, float b) { unsigned long long m; asm volatile("v_cmp_u_f32 %0, %1, %2" : "=s"(m) : "v"(a), "v"(b)); return m; }
DEV float add1(float a, float b) { float r; asm volatile("v_add_f32 %0, %1, %2" : "=v"(r) : "v"(a), "v"(b)); return r; }
DEV unsigned pk1(float a, float b) { unsigned r; asm volatile("v_cvt_pk_bf16_f32 %0, %1, %2" : "=v"(r) : "v"(a), "v"(b)); return r; }
DEV unsigned track(unsigned est, unsigned m, int s) { return m > est ? min(m, est + (s < 8 ? 8u : 3u)) : est - 1u - ((est - m) >> DECAY); }
struct AFrag {
    LAS char *p0, *p1;
    DEV void init(LAS char* img, int lane) {
        const int ga = lane >> 4, base = ga * 256 + (lane & 15) * 8, sw = (ga & 1) * 128;
        p0 = opq(img + base + sw); p1 = opq(img + base + (128 ^ sw));
    }
    DEV u32x4 get(int off) const { return cat2(trd(p0 + off), trd(p1 + off)); }
    static DEV int wofs(int lane, int odd) { const int g = lane >> 4, n = lane & 15; return (((2 * (n >> 2) + (n & 1)) * 32) ^ (odd * 128)) + g * 8 + ((n >> 1) & 1) * 2; }
};
DEV unsigned fold_pk(f32x4 c, float& f0, float& f1) {
    unsigned r;
    asm volatile("v_add_f32 %1, %3, %4\n\tv_add_f32 %2, %5, %6\n\tv_cvt_pk_bf16_f32 %0, %1, %2" : "=v"(r), "=&v"(f0), "=&v"(f1) : "v"(c[0]), "v"(c[1]), "v"(c[2]), "v"(c[3]));
    return r;
}
DEV void st2x16(LAS char* p0, LAS char* p1, int off, unsigned v) { *(LAS unsigned short*)(p0 + off) = (unsigned short)v; *(LAS unsigned short*)(p1 + off) = (unsigned short)(v >> 16); }

constexpr float SCALE_L2E = 0.07216878364870322f * 1.4426950408889634f;

DEV void role_S(KP& P, unsigned char* ws, int h, LAS char* lds, int wave, int lane, Spin& sp) {
    constexpr int L_QK = 8192, L_FL = 14336, L_W = 16384, L_WS = 36864, L_KX = L_W + L_WS, L_QX = L_W + 3 * L_WS;
    const rsrc_t rw = mk_rsrc(ws, (unsigned)WS_END);
    const int rot = MemStream::rot_of(h, wave);
    u32x8 w[6][8];
    {
        const char* src = (const char*)(wave < 2 ? P.wq : P.wk) + h * 384 + (wave & 1) * 192;
        stream_w<6, 16, 20, 6, 4, false>(src, 12288u, lds, L_W + wave * L_WS, lane,
            [&](int t, int k, u32x4 f) {
                if (k & 1) { w[t][k >> 1] = cat4(lo4(w[t][k >> 1]), f); if ((k >> 1) * 6 + t < 32) pin_a(w[t][k >> 1]); } else w[t][k >> 1] = cat4(f, f); }, rot, 0, &sp, wave);
        vmwait<0>();
    }
    MARK(2); FLUSH(128);
    claim<true>(sp, (volatile LAS unsigned*)lds, wave, lane);
    const unsigned par = load_par<true>(rw, lane, sp);
    const int idx = (lane & 1) ? 0xEEEE : 0x4444;
    const int g = lane >> 4, n = lane & 15;
    const unsigned cq = (unsigned)(O_CTRL + 4 * (W_QKPROG + 32 * (((wave < 2 ? 384 : 0) + h * 12 + (lane & 1) * 11) >> 4)));
    unsigned have = 0;
    auto wait_qk = [&](int s) {
        if (have > (unsigned)s) return;
        for (unsigned c = 0;;) {
            const unsigned v = ld32(rw, cq, 0);
            have = min((unsigned)__builtin_amdgcn_readlane(v, 0), (unsigned)__builtin_amdgcn_readlane(v, 1));
            if (have > (unsigned)s || sp.fail(c, 0x200 + s)) break;
            __builtin_amdgcn_s_sleep(2);
        }
    };
    Clock ck; ck.org = sp.org;
    MemStream ms; ms.init(rw, lds, lane, rot, wave == 1);
    volatile LAS unsigned* lfail = (volatile LAS unsigned*)(lds + L_FL);
    if (wave == 0) *lfail = ~0u;
    repack<6, 4>(lds + L_W + wave * L_WS + 12288, lane);
    LAS char* wr = opq(lds + L_W + wave * L_WS + 12288 + lane * 16);
    AFrag af; af.init(lds + L_QK, lane);
    LAS char* we = opq(lds + L_QK + wave * 1536 + AFrag::wofs(lane, 0));
    LAS char* wo = opq(lds + L_QK + wave * 1536 + AFrag::wofs(lane, 1));
    const unsigned psel = (n & 1) ? 0x07060302u : 0x01000504u, psel2 = (n & 2) ? 0x07060302u : 0x01000504u;
    unsigned so1, so2;
    if (wave == 0) {
        const int i = 2 * g + (n & 1), half = (n >> 1) & 1;
        so1 = amap(R_P) + (h >> 4) * 16384 + (h & 15) * 512 + ((n >> 3) * 16 + 2 * i + half) * 16 + ((n >> 2) & 1) * 8;
        so2 = amap(R_P + 16384) + (h >> 1) * 256 + (2 * (2 * g + half) + ((n >> 2) & 1)) * 16 + (h & 1) * 8 + (n >> 3) * 4;
    } else {
        const unsigned b = amap(R_PX) + (wave & 1) * amap(PX_B);
        so1 = (wave & 1) ? b + (h >> 3) * 16384 + (h & 7) * 1024 + lane * 16 : b + (h >> 4) * 16384 + ((h >> 1) & 7) * 1024 + lane * 16 + (h & 1) * 8;
        so2 = b + 65536 + h * 256 + n * 16 + g * 4;
    }
    unsigned xo[3];
#pragma unroll
    for (int j = 0; j < 3; ++j) { const unsigned o = (wave < 2 ? R_KX : R_QX) + h * QX_B + j * 4096; xo[j] = (((o >> 13) << 14) | (o & 8191u)) + (wave < 2 ? 0 : (wave & 1) * 1024); }
    auto stage_k = [&](int s) __attribute__((always_inline)) {
        wait_qk(s);
#pragma unroll
        for (int j = 0; j < 3; ++j) glds_dev<4, 1024>(ms.l16, ws + (page(par, s, 1) + xo[j]), (unsigned)(uintptr_t)lds + L_KX + (s & 1) * L_WS + j * 4096);
    };
    auto stage_q = [&](int s) __attribute__((always_inline)) {
        wait_qk(s);
#pragma unroll
        for (int j = 0; j < 3; ++j) glds_dev<2, 2048>(ms.l16, ws + (page(par, s, 1) + xo[j]), (unsigned)(uintptr_t)lds + L_QX + (wave & 1) * 1024 + j * 4096);
    };
    auto lwh = [&](int j, int q) { return lds128(wr + (2 * (1 - j / 6) + q) * 6144 + (j % 6) * 1024); };
    unsigned dur = 139, tn = 0;
    if (wave == 0) __builtin_amdgcn_s_setprio(3);
    if (wave == 1) stage_k(0); else if (wave >= 2) stage_q(0);
    vmwait<0>();
    bar();
    if (wave == 1) stage_k(1);
    for (int s = 0; s < NSEG; ++s) {
        const unsigned rb = rec(par, s);
        f32x4 acc[6];
        u32x2 nf; u32x4 st[12][2], a[10], kxf[12];
        LAS const char* kx = opq(lds + L_KX + (s & 1) * L_WS + lane * 16);
        unsigned long long bad = 0;
        auto prep = [&]() __attribute__((always_inline)) {
#pragma unroll
            for (int t = 0; t < 6; ++t) zero_acc(acc[t]);
#pragma unroll
            for (int i = 0; i < 6; ++i) st[i >> 1][i & 1] = lwh(i >> 1, i & 1);
        };
        float fa[6], fb[6]; unsigned fp[6];
        auto fin = [&](int t, int step) __attribute__((always_inline)) {
            if (step == 0) fa[t] = add1(acc[t][0], acc[t][1]);
            if (step == 1) fb[t] = add1(acc[t][2], acc[t][3]);
            if (step == 2) fp[t] = pk1(fa[t], fb[t]);
            if (step == 3) *(LAS unsigned short*)(((t & 1) ? wo : we) + t * 256) = (unsigned short)fp[t];
            if (step == 4) *(LAS unsigned short*)(((t & 1) ? wo : we) + t * 256 + 4) = (unsigned short)(fp[t] >> 16);
        };
        unsigned long long bad2 = 0;
        unsigned qp;
        LAS char* ad[9];
        TA_DECL;
        auto slot = [&](auto I) __attribute__((always_inline)) {
            constexpr int i = decltype(I)::value;
            if constexpr (i == 15) asm volatile("s_waitcnt lgkmcnt(4)\n\ts_barrier" ::: "memory");
            if constexpr (i == 15) { MARK3(0); }
            if constexpr (i == 36) { MARK3(5); }
            constexpr bool isl = i >= 6 && i <= 50 && (i & 3) == 2;
            constexpr int nl = i < 6 ? 0 : i > 50 ? 12 : (i - 6 + 3) / 4;
            if constexpr (isl) {
                smf_v(acc[nl % 6], a[9 - nl / 6], cat4(st[nl][0], st[nl][1]), idx);
            } else {
                constexpr int r = i - nl;
                constexpr int kb = r < 7 ? r / 6 : r == 7 ? 2 : r < 13 ? 1 : r < 24 ? r / 6 : r < 40 ? 4 + ((r - 24) % 8) / 2 : 4 + (r & 3);
                constexpr int t = r < 7 ? r % 6 : r == 7 ? 0 : r < 13 ? r - 7 : r < 24 ? r % 6 : r < 40 ? 2 * ((r - 24) / 8) + (r & 1) : 4 + (r - 40) / 4;
                if constexpr (kb * 6 + t < 32) smf_a(acc[t], a[kb], w[t][kb], idx); else smf_v(acc[t], a[kb], w[t][kb], idx);
            }
            if constexpr (i == 0) { MARK2(2); TA(1); }
            if constexpr (i == 14) { TA(2); }
            if constexpr (i == 15) { TA(3); }
            if constexpr (i == 59) { TA(4); }
            if constexpr (i == 0) *(LAS u32x4*)ad[6] = a[0];
            if constexpr (i == 2) *(LAS u32x4*)ad[7] = a[1];
            if constexpr (i == 4) *(LAS u32x4*)ad[8] = a[2];
            if constexpr (i == 13) bad = nan2(acc[0][0], acc[0][1]);
            if constexpr (i == 13) bad2 = nan2(acc[0][2], acc[0][3]);
            if constexpr (i == 16) a[3] = lds128(ad[0]);
            if constexpr (i == 18) nf = ld64(rw, ms.vn, rb + amap(R_NF));
            if constexpr (i == 30) qp = ld32(rw, cq, 0);
            if constexpr (i == 20) a[8] = lds128(ad[1]);
            if constexpr (i == 22) a[4] = lds128(ad[2]);
            if constexpr (i == 24) a[5] = lds128(ad[3]);
            if constexpr (i == 26) a[6] = lds128(ad[4]);
            if constexpr (i == 28) a[7] = lds128(ad[5]);
            if constexpr (i >= 7 && i < 43 && (i & 1)) st[(i - 7) / 4 + 3][((i - 7) >> 1) & 1] = lwh((i - 7) / 4 + 3, ((i - 7) >> 1) & 1);
            if constexpr (i >= 43 && i < 55) kxf[i - 43] = lds128(kx + (i - 43) * 1024);
            if constexpr (i >= 44 && i < 49) fin(0, i - 44);
            if constexpr (i >= 47 && i < 52) fin(1, i - 47);
            if constexpr (i >= 55) { fin(2, i - 55); fin(3, i - 55); }
        };
        prep();
        MARK(0); SETV(7, 0);
        ad[0] = ms.at(3); ad[1] = ms.at(8); ad[2] = ms.at(4); ad[3] = ms.at(5); ad[4] = ms.at(6); ad[5] = ms.at(7); ad[6] = ms.at(0); ad[7] = ms.at(1); ad[8] = ms.at(2);
        const unsigned o0 = ms.so(rb, 0), o9 = ms.so(rb, 9), o1 = ms.so(rb, 1), o2 = ms.so(rb, 2);
        unsigned tv = 0;
        if (TOUCH) tv = ld32(rw, 0, rb + amap(60 * 8192) + wave * 128);
        const unsigned tg = ck.wait_fine(tn);
        TA(0);
        a[0] = ms.lds_(o0); a[1] = ms.lds_(o1); a[2] = ms.lds_(o2); a[9] = ms.lds_(o9);
        SETV(1, tg + ck.org);
        sfor<0, 15>(slot);
        asm volatile("" :: "v"(tv));
        if (__builtin_expect((bad | bad2) != 0, 0)) {
            asm volatile("s_nop 7\n\ts_waitcnt vmcnt(0)" ::: "memory");
            *lfail = (unsigned)s; SETV(7, 1);
            for (unsigned c = 0;;) {
                a[0] = ms.ld(rb, 0); a[9] = ms.ld(rb, 9); a[1] = ms.ld(rb, 1); a[2] = ms.ld(rb, 2);
                if (MemStream::bad(a[0], a[9], a[1], a[2]) == 0 || sp.fail(c, 0x100 + s)) break;
            }
            prep();
            sfor<0, 15>(slot);
            asm volatile("s_nop 7" : "+v"(acc[0]), "+v"(acc[1]), "+v"(acc[2]), "+v"(acc[3]), "+v"(acc[4]), "+v"(acc[5]));
        }
        MARK3(6);
        sfor<15, 60>(slot);
        fin(4, 0); fin(4, 1); fin(4, 2); fin(4, 3); fin(4, 4);
        asm volatile("s_nop 0");
        fin(5, 0); fin(5, 1); fin(5, 2); fin(5, 3); fin(5, 4);
        f32x4 am, a0, a1;
        MARK2(3);
        bar();
        MARK2(4); TA(5); TA(6); TA(7);
        u32x4 qa[3], ka[3];
        if (wave >= 2) {
            const int Tq = wave & 1;
            have = min((unsigned)__builtin_amdgcn_readlane(qp, 0), (unsigned)__builtin_amdgcn_readlane(qp, 1));
            u32x4 qx[6];
            asm volatile("s_nop 7\n\ts_nop 7" ::: "memory");
#pragma unroll
            for (int kb = 0; kb < 3; ++kb) ka[kb] = af.get(3072 + kb * 1024);
#pragma unroll
            for (int ep = 0; ep < 2; ++ep) qx[ep] = lds128(lds + L_QX + lane * 16 + Tq * 1024 + ep * 2048);
            zero_acc(am);
#pragma unroll
            for (int ep = 0; ep < 6; ++ep) {
                if (ep < 4) qx[ep + 2] = lds128(lds + L_QX + lane * 16 + Tq * 1024 + (ep + 2) * 2048);
                mf_vv(a0, kxf[2 * ep], qx[ep], ep == 0); mf_vv(a1, kxf[2 * ep + 1], qx[ep], ep == 0);
                if (ep & 1) smf_v(am, ka[ep >> 1], cat4(qx[ep - 1], qx[ep]), idx);
            }
            if (QEARLY && s + 1 < NSEG) stage_q(s + 1);
            mf_done(am, a0, a1);
            float v[10];
            constexpr float SC = 0.07216878364870322f, L2E = 1.4426950408889634f;
#pragma unroll
            for (int r = 0; r < 4; ++r) {
                v[r] = (4 * g + r <= 16 * Tq + n) ? a0[r] * SC : -1e9f;
                v[4 + r] = (16 + 4 * g + r <= 16 * Tq + n) ? a1[r] * SC : -1e9f;
            }
            v[8] = (am[0] + am[1]) * SC; v[9] = (am[2] + am[3]) * SC;
            float mx = v[0];
#pragma unroll
            for (int r = 1; r < 10; ++r) mx = fmaxf(mx, v[r]);
            mx = x16(mx, true); mx = x32(mx, true);
            float sum = 0.f;
#pragma unroll
            for (int r = 0; r < 10; ++r) { v[r] = __builtin_amdgcn_exp2f((v[r] - mx) * L2E); sum += v[r]; }
            sum = x16(sum, false); sum = x32(sum, false);
            const float inv = __builtin_amdgcn_rcpf(sum);
            u32x4 ox = {pka(v[0] * inv, v[1] * inv), pka(v[2] * inv, v[3] * inv), pka(v[4] * inv, v[5] * inv), pka(v[6] * inv, v[7] * inv)};
            unsigned om = pka(v[8] * inv, v[9] * inv);
            asm volatile("s_barrier" : "+v"(ox), "+v"(om) :: "memory");
            if (Tq) st128(ox, rw, so1, rb); else st64(u32x2{ox.x, ox.y}, rw, so1, rb);
            st32(om, rw, so2, rb);
            if (!QEARLY && s + 1 < NSEG) stage_q(s + 1);
            if (s + KARM < NSEG) { const unsigned ra = rec(par, s + KARM); if (Tq) st128(SENT4, rw, so1, ra); else st64(SENT2, rw, so1, ra); st32(sent1(), rw, so2, ra); }
        } else if (wave == 1) {
            have = min((unsigned)__builtin_amdgcn_readlane(qp, 0), (unsigned)__builtin_amdgcn_readlane(qp, 1));
            if (s + 2 < NSEG) stage_k(s + 2);
            if (s == 0) nf = first_notes(rw, ms.vn, rb + amap(R_NF), sp);
            { const u32x2 pl = read_notes(nf); lfail[2] = pl.x; lfail[3] = pl.y; }
            asm volatile("s_waitcnt lgkmcnt(0)\n\ts_barrier" ::: "memory");
        } else {
            if (s < NSEG - 1) {
#pragma unroll
                for (int kb = 0; kb < 3; ++kb) { qa[kb] = af.get(kb * 1024); __builtin_amdgcn_sched_barrier(0); ka[kb] = af.get(3072 + kb * 1024); __builtin_amdgcn_sched_barrier(0); }
                a0 = f32x4{zero1(), zero1(), zero1(), zero1()}; a1 = f32x4{zero1(), zero1(), zero1(), zero1()};
                asm volatile("s_nop 0" : "+v"(a0), "+v"(a1));
                auto sx = [&](int kb, int T) __attribute__((always_inline)) { smf_v(T ? a1 : a0, qa[kb], cat4(kxf[4 * kb + T], kxf[4 * kb + 2 + T]), idx); };
                sx(0, 0); mf_vv(am, qa[0], ka[0], true); sx(0, 1); mf_vv(am, qa[1], ka[1], false); sx(1, 0); mf_vv(am, qa[2], ka[2], false);
                sx(1, 1); sx(2, 0); sx(2, 1);
                TA(6);
                float v[6];
                float ma, mb; u32x2 p01; unsigned p0, p1, p2;
#define DP " row_mask:0xf bank_mask:0xf\n\t"
#define RED(op, r) op " %6, %6, %6 row_ror:" r DP op " %7, %7, %7 row_ror:" r DP
                asm volatile("s_nop 4\n\t"
                    "v_add_f32_dpp %2, %12, %11 quad_perm:[1,1,3,3]" DP "v_add_f32_dpp %5, %14, %13 quad_perm:[1,1,3,3]" DP
                    "v_add_f32 %0, %15, %16\n\tv_add_f32 %3, %17, %18\n\t"
                    "v_mov_b32_dpp %2, %2 quad_perm:[0,0,2,2]" DP "v_mov_b32_dpp %5, %5 quad_perm:[0,0,2,2]" DP
                    "v_add_f32 %1, %19, %20\n\tv_add_f32 %4, %21, %22\n\t"
                    "v_max3_f32 %6, %0, %1, %2\n\tv_max3_f32 %7, %3, %4, %5\n\tv_mul_f32 %0, %23, %0\n\t"
                    RED("v_max_f32_dpp", "8") "v_mul_f32 %3, %23, %3\n\t" RED("v_max_f32_dpp", "4") "v_mul_f32 %1, %23, %1\n\t"
                    RED("v_max_f32_dpp", "2") "v_mul_f32 %4, %23, %4\n\t" RED("v_max_f32_dpp", "1")
                    "v_fma_f32 %0, -%6, %23, %0\n\tv_fma_f32 %3, -%7, %23, %3\n\tv_fma_f32 %1, -%6, %23, %1\n\tv_fma_f32 %4, -%7, %23, %4\n\t"
                    "v_exp_f32 %0, %0\n\tv_sub_f32 %2, %2, %6\n\tv_exp_f32 %3, %3\n\tv_sub_f32 %5, %5, %7\n\tv_exp_f32 %1, %1\n\tv_mul_f32 %2, %23, %2\n\tv_exp_f32 %4, %4\n\tv_mul_f32 %5, %23, %5\n\t"
                    "v_exp_f32 %2, %2\n\tv_add_f32 %6, %0, %1\n\tv_exp_f32 %5, %5\n\tv_add_f32 %7, %3, %4\n\t"
                    "v_fmac_f32 %6, 0.5, %2\n\tv_fmac_f32 %7, 0.5, %5\n\ts_nop 0\n\t"
                    RED("v_add_f32_dpp", "8") "s_nop 0\n\t" RED("v_add_f32_dpp", "4") "s_nop 0\n\t" RED("v_add_f32_dpp", "2") "s_nop 0\n\t" RED("v_add_f32_dpp", "1")
                    "v_rcp_f32 %6, %6\n\tv_rcp_f32 %7, %7\n\t"
                    "v_mul_f32 %0, %0, %6\n\tv_mul_f32 %3, %3, %7\n\tv_mul_f32 %1, %1, %6\n\tv_mul_f32 %4, %4, %7\n\t"
                    "v_cvt_pk_bf16_f32 %8, |%0|, |%3|\n\tv_cvt_pk_bf16_f32 %9, |%1|, |%4|\n\tv_mul_f32 %2, %2, %6\n\t"
                    "v_mov_b32_dpp %0, %8 quad_perm:[1,0,3,2]" DP "v_mov_b32_dpp %1, %9 quad_perm:[1,0,3,2]" DP "v_mul_f32 %5, %5, %7\n\t"
                    "v_cvt_pk_bf16_f32 %10, |%2|, |%5|\n\tv_perm_b32 %8, %8, %0, %24\n\tv_perm_b32 %9, %9, %1, %24"
                    : "=&v"(v[0]), "=&v"(v[1]), "=&v"(v[2]), "=&v"(v[3]), "=&v"(v[4]), "=&v"(v[5]), "=&v"(ma), "=&v"(mb), "=&v"(p0), "=&v"(p1), "=&v"(p2)
                    : "v"(am[0]), "v"(am[1]), "v"(am[2]), "v"(am[3]), "v"(a0[0]), "v"(a0[1]), "v"(a0[2]), "v"(a0[3]), "v"(a1[0]), "v"(a1[1]), "v"(a1[2]), "v"(a1[3]), "s"(SCALE_L2E), "v"(psel));
#undef RED
#undef DP
                p01 = u32x2{p0, p1};
                st64(p01, rw, so1, rb);
                st32(__builtin_amdgcn_perm(p2, __builtin_amdgcn_update_dpp(0u, p2, 0x4E, 0xf, 0xf, true), psel2), rw, so2, rb);
                TA(7);
                tn = ck.now();
                if (lane == 0) st32(dur, rw, h * 128, rb + amap(R_NS));
                lfail[1] = tn;
                asm volatile("s_waitcnt lgkmcnt(0)\n\ts_barrier" ::: "memory");
                if (s > 0 && __builtin_amdgcn_readfirstlane(*lfail) != s) dur = track(dur, tn - tg, s);
                SETV(5, tn + ck.org); SETV(6, tg + dur + ck.org);
                if (s + KARM < NSEG) { const unsigned ra = rec(par, s + KARM); st64(SENT2, rw, so1, ra); st32(sent1(), rw, so2, ra); if (lane == 0) st32(sent1(), rw, h * 128, ra + amap(R_NS)); }
            } else asm volatile("s_barrier" ::: "memory");
        }
        tn = mem_time(__builtin_amdgcn_readfirstlane(lfail[2]), __builtin_amdgcn_readfirstlane(lfail[3]), __builtin_amdgcn_readfirstlane(lfail[1]));
        FLUSH(s); TA_FLUSH(s);
    }
    vmwait<0>();
}

constexpr int L_WO = 81920;
template <class SV, class SO>
DEV void load_vo(KP& P, int h, LAS char* lds, int wave, int lane, Spin& sp, SV&& sv, SO&& so_, int rot = 0) {
    auto krow = [&](int k) { int kk = k + 2 * rot; if (kk >= 20) kk -= 20; return (unsigned)kk * 32u * 12288u; };
    sp.entry(wave, lane);
    WSrc<3> wv; wv.init((const char*)P.wv + h * 384 + wave * 96, lane, 12288u);
    const unsigned lo = (unsigned)(uintptr_t)lds + L_WO + wave * 20480, lv = (unsigned)(uintptr_t)lds + wave * 20480;
#pragma unroll
    for (int k = 0; k < 6; ++k) wv.issue(krow(k), lv + k * 3072);
    WSrc<10> wo; wo.init((const char*)P.wo + (size_t)h * E_ * 1280 + wave * 320, lane, 1280u);
    wo.issue(0, lo); wo.issue(32 * 1280, lo + 10240);
    WRd<3, false> rv; rv.init(lds + wave * 20480, lane);
    WRd<10, false> ro; ro.init(lds + L_WO + wave * 20480, lane);
#pragma unroll
    for (int k = 0; k < 20; ++k) {
        if (k == 1) { MARK(3); } if (k == 6) { MARK(4); } if (k == 12) { MARK(5); }
        if (k == 6 || k == 12) {
            const int ko = k / 3 - 2;
            vmwait<18>();
#pragma unroll
            for (int j = 0; j < 2; ++j) {
                u32x4 fo[10];
#pragma unroll
                for (int t = 0; t < 10; ++t) fo[t] = ro.get(j * 10240, t);
#pragma unroll
                for (int t = 0; t < 10; ++t) so_(t, ko + j, fo[t]);
            }
            lgkm0();
            wo.issue((ko + 2) * 32 * 1280, lo); wo.issue((ko + 3) * 32 * 1280, lo + 10240);
        }
        vmwait_n(k < 15 ? 35 : k < 18 ? 20 + (19 - k) * 3 : (19 - k) * 3);
        u32x4 fr[3];
#pragma unroll
        for (int t = 0; t < 3; ++t) fr[t] = rv.get((k % 6) * 3072, t);
#pragma unroll
        for (int t = 0; t < 3; ++t) sv(t, k, fr[t]);
        if (k + 6 < 20) { lgkm0(); wv.issue(krow(k + 6), lv + (k % 6) * 3072); }
        if (k == 0) sp.early_claim(wave, lane);
        if (k == 17) sp.early_ready(lane);
    }
    vmwait<0>();
    MARK(2); FLUSH(128);
    bar();
}

template <class SV, class SO>
DEV void load_vo_rows(KP& P, int h, LAS char* lds, int wave, int lane, Spin& sp, SV&& sv, SO&& so_) {
    sp.entry(wave, lane);
    WSrc<3> wv;
    wv.r = mk_rsrc((const char*)P.wv + h * 384 - 4096, 0xFFFFFFFFu);
#pragma unroll
    for (int i = 0; i < 3; ++i) {
        const int Pc = 64 * (3 * wave + i) + lane, rho = Pc / 24, cs = Pc % 24, tile = (cs >> 1) ^ img_rot<12>(rho);
        wv.voff[i] = (unsigned)swap23(rho) * 12288u + (unsigned)(2 * tile + (cs & 1)) * 16u + 4096u - i * 1024u;
    }
    const unsigned lo = (unsigned)(uintptr_t)lds + L_WO + wave * 20480, lv = (unsigned)(uintptr_t)lds + wave * 3072;
#pragma unroll
    for (int k = 0; k < 6; ++k) wv.issue(k * 32u * 12288u, lv + k * 12288);
    WSrc<10> wo; wo.init((const char*)P.wo + (size_t)h * E_ * 1280 + wave * 320, lane, 1280u);
    wo.issue(0, lo); wo.issue(32 * 1280, lo + 10240);
    LAS char* rv[3];
    { const int g = lane >> 4, q = (lane >> 2) & 3, p = lane & 3, rho = q | ((g & 1) << 2) | ((g >> 1) << 4);
#pragma unroll
      for (int t = 0; t < 3; ++t) rv[t] = opq(lds + rho * 384 + ((3 * wave + t) ^ img_rot<12>(rho)) * 32 + 8 * p); }
    WRd<10, false> ro; ro.init(lds + L_WO + wave * 20480, lane);
#pragma unroll
    for (int k = 0; k < 20; ++k) {
        if (k == 1) { MARK(3); } if (k == 6) { MARK(4); } if (k == 12) { MARK(5); }
        if (k == 6 || k == 12) {
            const int ko = k / 3 - 2;
            vmwait_n(k == 6 ? 15 : 18);
#pragma unroll
            for (int j = 0; j < 2; ++j) {
                u32x4 fo[10];
#pragma unroll
                for (int t = 0; t < 10; ++t) fo[t] = ro.get(j * 10240, t);
#pragma unroll
                for (int t = 0; t < 10; ++t) so_(t, ko + j, fo[t]);
            }
            lgkm0();
            wo.issue((ko + 2) * 32 * 1280, lo); wo.issue((ko + 3) * 32 * 1280, lo + 10240);
        }
        vmwait_n(k == 0 ? 35 : k == 11 ? 12 : k < 16 ? 32 : k == 16 ? 29 : (19 - k) * 3);
        bar();
        if (k >= 1 && k + 5 < 20) wv.issue((k + 5) * 32u * 12288u, lv + ((k + 5) % 6) * 12288);
        u32x4 fr[3];
#pragma unroll
        for (int t = 0; t < 3; ++t) fr[t] = cat2(trd(rv[t] + (k % 6) * 12288), trd(rv[t] + (k % 6) * 12288 + 3072));
#pragma unroll
        for (int t = 0; t < 3; ++t) sv(t, k, fr[t]);
        if (k == 0) sp.early_claim(wave, lane);
        if (k == 17) sp.early_ready(lane);
    }
    vmwait<0>();
    MARK(2); FLUSH(128);
    bar();
}

DEV void role_UV(KP& P, unsigned char* ws, int h, LAS char* lds, int wave, int lane, Spin& sp) {
    const rsrc_t rw = mk_rsrc(ws, (unsigned)WS_END);
    const int rot = MemStream::rot_of(h + 5, wave);
    u32x8 wv[3][10], wo[10][2];
    load_vo(P, h, lds, wave, lane, sp,
        [&](int t, int k, u32x4 f) { if (k & 1) { wv[t][k >> 1] = cat4(lo4(wv[t][k >> 1]), f); pin_a(wv[t][k >> 1]); } else wv[t][k >> 1] = cat4(f, f); },
        [&](int t, int k, u32x4 f) { if (k & 1) { wo[t][k >> 1] = cat4(lo4(wo[t][k >> 1]), f); if (k == 1 && t < 2) pin_a(wo[t][k >> 1]); } else wo[t][k >> 1] = cat4(f, f); }, rot);
    claim<true>(sp, (volatile LAS unsigned*)lds, wave, lane);
    const unsigned par = load_par<true>(rw, lane, sp);
    const int idx = (lane & 1) ? 0xEEEE : 0x4444;
    const int g = lane >> 4, n = lane & 15;
    MemStream ms; ms.init(rw, lds, lane, rot, wave == 0);
    volatile LAS unsigned* lfail = (volatile LAS unsigned*)(lds + 14336);
    if (wave == 0) *lfail = ~0u;
    bar();
    AFrag af; af.init(lds + 8192, lane);
    repack<10, 2>(lds + L_WO + wave * 20480, lane);
    LAS char* wr = opq(lds + L_WO + wave * 20480 + lane * 16);
    const unsigned uo = amap(R_UB) + (wave * 10 + (g & 1)) * 16384 + h * 256 + n * 16 + (g >> 1) * 8;
    LAS char* vw0 = opq(lds + 8192 + wave * 768 + AFrag::wofs(lane, wave & 1));
    LAS char* vw1 = opq(lds + 8192 + wave * 768 + AFrag::wofs(lane, ~wave & 1));
    auto lwh = [&](int t, int q) { return lds128(wr + q * 10240 + t * 1024); };
    Clock ck; ck.org = sp.org;
    unsigned dur = 126, tn = 0;
    for (int s = 0; s < NSEG; ++s) {
        const unsigned rb = rec(par, s);
        f32x4 acc[3], c[10];
        u32x4 st[10][2], a[10];
#pragma unroll
        for (int i = 0; i < 6; ++i) st[i >> 1][i & 1] = lwh(i >> 1, i & 1);
#pragma unroll
        for (int t = 0; t < 3; ++t) zero_acc(acc[t]);
#pragma unroll
        for (int t = 0; t < 4; ++t) zero_acc(c[t]);
        MARK(0); SETV(7, 0);
        const unsigned o0 = ms.so(rb, 0), o1 = ms.so(rb, 1), o2 = ms.so(rb, 2), o3 = ms.so(rb, 3);
        LAS char* ad[10];
#pragma unroll
        for (int k = 0; k < 10; ++k) if (k != 3) ad[k] = ms.at(k);
        const unsigned tg = ck.wait_fine(tn);
        a[0] = ms.lds_(o0); a[1] = ms.lds_(o1); a[2] = ms.lds_(o2); a[3] = ms.lds_(o3);
        SETV(1, tg + ck.org);
        unsigned long long bad = 0, bad2 = 0;
        auto slot = [&](auto I) __attribute__((always_inline)) {
            constexpr int i = decltype(I)::value;
            constexpr int kb = i < 4 ? i : i < 12 ? (i - 4) / 2 : i / 3, t = i < 4 ? 0 : i < 12 ? 1 + (i & 1) : i % 3;
            if constexpr (i == 9) { asm volatile("s_waitcnt lgkmcnt(0)\n\ts_barrier" ::: "memory"); a[4] = lds128(ad[4]); a[5] = lds128(ad[5]); }
            smf_a(acc[t], a[kb], wv[t][kb], idx);
            if constexpr (i == 0) { MARK2(2); }
            if constexpr (i < 3) *(LAS u32x4*)ad[i] = a[i];
            if constexpr (i == 6) { asm volatile("s_nop 4"); bad = nan2(acc[0][0], acc[0][1]); bad2 = nan2(acc[0][2], acc[0][3]); }
            if constexpr (i >= 9 && i <= 18 && i % 3 == 0) a[3 + i / 3] = lds128(ad[3 + i / 3]);
            if constexpr (i >= 14 && i < 26) { constexpr int q = i - 14; c[4 + q / 4][q & 3] = zero1(); }
        };
        sfor<0, 9>(slot);
        if (__builtin_expect((bad | bad2) != 0, 0)) {
            asm volatile("s_nop 7\n\ts_waitcnt vmcnt(0)" ::: "memory");
            *lfail = (unsigned)s; SETV(7, 1);
            for (unsigned c = 0;;) {
#pragma unroll
                for (int k = 0; k < 4; ++k) a[k] = ms.ld(rb, k);
                if (MemStream::bad(a[0], a[1], a[2], a[3]) == 0 || sp.fail(c, 0x100 + s)) break;
            }
#pragma unroll
            for (int t = 0; t < 3; ++t) zero_acc(acc[t]);
            sfor<0, 9>(slot);
            mf_done(acc[0], acc[1], acc[2]);
        }
        sfor<9, 30>(slot);
        asm volatile("s_nop 7" : "+v"(acc[0]), "+v"(acc[1]), "+v"(acc[2]), "+v"(c[4]), "+v"(c[5]), "+v"(c[6]));
#pragma unroll
        for (int t = 0; t < 3; ++t) { LAS char* p = t == 1 ? vw1 : vw0; float f0, f1; st2x16(p, p + 4, t * 256, fold_pk(acc[t], f0, f1)); }
#pragma unroll
        for (int t = 7; t < 10; ++t) { c[t] = f32x4{zero1(), zero1(), zero1(), zero1()}; pin_v(c[t]); }
        MARK2(3);
        bar();
        MARK2(4);
        u32x4 va[3];
#pragma unroll
        for (int kb = 0; kb < 3; ++kb) va[kb] = af.get(kb * 1024);
        float fa[10], fb[10]; unsigned o[10];
        sfor<0, 39>([&](auto I) __attribute__((always_inline)) {
            constexpr int i = decltype(I)::value, tp = i / 6, k = (i % 6) >> 1, t = 2 * tp + (i & 1);
            if constexpr (i < 30) {
                if constexpr (k == 0) { if constexpr (t < 2) smf_a(c[t], va[0], wo[t][0], idx); else smf_v(c[t], va[0], wo[t][0], idx); }
                else if constexpr (k == 1) smf_v(c[t], va[1], wo[t][1], idx);
                else smf_v(c[t], va[2], cat4(st[t][0], st[t][1]), idx);
                if constexpr (i >= 3 && i < 24 && i % 3 < 2) st[i / 3 + 2][i % 3] = lwh(i / 3 + 2, i % 3);
            }
            if constexpr (i >= 9) {
                constexpr int x = 2 * ((i - 9) / 6), y = x + 1, j = (i - 9) % 6;
                if constexpr (i == 33) asm volatile("s_nop 0");
                if constexpr (j == 0) { fa[x] = add1(c[x][0], c[x][1]); fb[x] = add1(c[x][2], c[x][3]); }
                if constexpr (j == 1) { fa[y] = add1(c[y][0], c[y][1]); fb[y] = add1(c[y][2], c[y][3]); }
                if constexpr (j == 2) { o[x] = pk1(fa[x], fb[x]); o[y] = pk1(fa[y], fb[y]); }
                if constexpr (j == 3) asm volatile("v_min_u32 %0, -2, %0\n\tv_min_u32 %1, -2, %1" : "+v"(o[x]), "+v"(o[y]));
                if constexpr (j == 4) asm volatile("s_nop 1\n\tv_permlane16_swap_b32 %0, %1" : "+v"(o[x]), "+v"(o[y]));
                if constexpr (j == 5) st64(u32x2{o[x], o[y]}, rw, uo, rb + x * 16384);
            }
        });
        tn = ck.now();
        if (wave == 0 && lane == 0) st32(dur, rw, (32 + h) * 128, rb + amap(R_NS));
        if (s > 0 && __builtin_amdgcn_readfirstlane(*lfail) != s) dur = track(dur, tn - tg, s);
        SETV(5, tn + ck.org);
        if (wave == 0) { const u32x2 pl = read_notes(s ? ld64(rw, ms.vn, rb + amap(R_NF)) : first_notes(rw, ms.vn, rb + amap(R_NF), sp)); lfail[1] = mem_time(pl.x, pl.y, tn); }
        bar();
        tn = __builtin_amdgcn_readfirstlane(lfail[1]);
        MARKC(6);
        if (s + KARM < NSEG) {
            const unsigned ra = rec(par, s + KARM);
#pragma unroll
            for (int tp = 0; tp < 5; ++tp) st64(SENT2, rw, uo, ra + tp * 32768);
            if (wave == 0 && lane == 0) st32(sent1(), rw, (32 + h) * 128, ra + amap(R_NS));
        }
        FLUSH(s);
    }
}

DEV void role_VC(KP& P, unsigned char* ws, int h, int odd, LAS char* lds, int wave, int lane, Spin& sp) {
    constexpr int L_V = 40960;
    const rsrc_t rw = mk_rsrc(ws, (unsigned)WS_END);
    u32x4 wv[3][20], wo[10][4];
    load_vo_rows(P, h, lds, wave, lane, sp, [&](int t, int k, u32x4 f) { wv[t][k] = f; pin_a(wv[t][k]); }, [&](int t, int k, u32x4 f) { wo[t][k] = f; if (t < 2 && k < 2) pin_a(wo[t][k]); });
    const int g = lane >> 4, n = lane & 15;
    const char* xf = (const char*)ws + O_XF;
    LAS char* wr = opq(lds + L_WO + wave * 20480 + lane * 16);
    LAS char* l16 = opq(lds + lane * 16);
    auto issue_x = [&](int s) {
#pragma unroll
        for (int i = 0; i < 10; ++i) glds<0>(lane * 16, xf + (size_t)s * 40960 + (wave * 10 + i) * 1024, (unsigned)(uintptr_t)lds + (wave * 10 + i) * 1024);
    };
    claim<true>(sp, (volatile LAS unsigned*)lds, wave, lane);
    const unsigned par = load_par<true>(rw, lane, sp);
    issue_x(odd);
    repack<10, 2>(lds + L_WO + wave * 20480, lane);
    unsigned seen = 0;
    for (int s = odd; s < NSEG; s += 2) {
        MARK(0);
        vmwait<0>();
        bar();
        if (s >= 2 && wave == 0 && lane == 0) st32((unsigned)(s - 1), rw, (unsigned)(O_CTRL + 4 * (W_VCPROG + 32 * (32 * odd + h))), 0);
        MARK(1);
        f32x4 acc[3][2];
        u32x4 xb[40];
#pragma unroll
        for (int i = 0; i < 4; ++i) xb[i] = lds128(l16 + (i & 1) * 20480 + (i >> 1) * 1024);
#pragma unroll
        for (int i = 0; i < 40; ++i) {
#pragma unroll
            for (int t = 0; t < 3; ++t) {
                mf_av(acc[t][i & 1], wv[t][i >> 1], xb[i], i < 2);
                if (t == 0 && i + 4 < 40) xb[i + 4] = lds128(l16 + (i & 1) * 20480 + ((i + 4) >> 1) * 1024);
            }
        }
        mf_done(acc[0][0], acc[1][0], acc[2][0]); mf_done(acc[0][1], acc[1][1], acc[2][1]);
#pragma unroll
        for (int t = 0; t < 3; ++t)
#pragma unroll
            for (int T = 0; T < 2; ++T) {
                const int et = wave * 3 + t;
                const int off = (et >> 1) * 2048 + T * 1024 + ((2 * (et & 1) + (g >> 1)) * 16 + n) * 16 + (g & 1) * 8;
                *(LAS u32x2*)(lds + L_V + off) = u32x2{pk(acc[t][T][0], acc[t][T][1]), pk(acc[t][T][2], acc[t][T][3])};
            }
        MARK(2);
        wait_slot(rw, s, seen, h & 7, sp);
        bar();
        MARK(3);
        u32x4 va[6][2], st[10][2];
        f32x4 c[10][2];
#pragma unroll
        for (int k = 0; k < 6; ++k)
#pragma unroll
            for (int T = 0; T < 2; ++T) va[k][T] = lds128(l16 + L_V + k * 2048 + T * 1024);
        st[0][0] = lds128(wr); st[0][1] = lds128(wr + 10240);
        const char* xn = xf + (size_t)(s + 2 < NSEG ? s + 2 : s) * 40960 + wave * 10240;
        const unsigned xd = (unsigned)(uintptr_t)lds + wave * 10240;
        auto fin = [&](int t) {
            st128(u32x4{pk(c[t][0][0], c[t][0][1]), pk(c[t][0][2], c[t][0][3]), pk(c[t][1][0], c[t][1][1]), pk(c[t][1][2], c[t][1][3])}, rw,
                  (h >> 3) * 16384 + (h & 7) * 1024 + lane * 16, (page(par, s, 2 + (wave >> 1)) ^ (CMIX ? ((h >> 3) & 1) << 13 : 0)) + ((wave & 1) * 10 + t) * 65536);
        };
#pragma unroll
        for (int t = 0; t < 10; ++t) {
#pragma unroll
            for (int k = 0; k < 6; ++k) {
                const u32x4 b = k < 4 ? wo[t][k & 3] : st[t][k & 1];
                if (t < 2 && k < 2) { mf_va(c[t][0], va[k][0], b, k == 0); mf_va(c[t][1], va[k][1], b, k == 0); }
                else { mf_vv(c[t][0], va[k][0], b, k == 0); mf_vv(c[t][1], va[k][1], b, k == 0); }
                if (k < 2 && t + 1 < 10) st[t + 1][k] = lds128(wr + k * 10240 + (t + 1) * 1024);
                if (k == 5 && t < 5) { glds<0>(lane * 16, xn + 2 * t * 1024, xd + 2 * t * 1024); }
                if (k == 2 && t < 5) { glds<1024>(lane * 16, xn + 2 * t * 1024, xd + 2 * t * 1024); }
                if (k == 3 && t >= 1) { pin_v(c[t - 1][0]); pin_v(c[t - 1][1]); fin(t - 1); }
            }
        }
        mf_done(c[9][0], c[9][1]);
        fin(9);
        MARK(4); FLUSH(s);
    }
    vmwait<0>();
    bar();
    if (wave == 0 && lane == 0) st32((unsigned)NSEG, rw, (unsigned)(O_CTRL + 4 * (W_VCPROG + 32 * (32 * odd + h))), 0);
}

DEV void role_QK(KP& P, unsigned char* ws, int cu, LAS char* lds, int wave, int lane, Spin& sp) {
    const rsrc_t rw = mk_rsrc(ws, (unsigned)WS_END);
    u32x4 w[4][20];
    const int sel = cu >= 24, c0 = (cu - 24 * sel) * 256 + wave * 64;
    stream_w<4, 20, 20, 10, 0, true>((const char*)(sel ? P.wk : P.wq) + c0 * 2, 12288u, lds, wave * 40960, lane,
                                 [&](int t, int k, u32x4 f) { w[t][k] = f; if (k < 16) pin_a(w[t][k]); }, 0, 0, &sp, wave);
    vmwait<0>();
    bar();
    const char* xf = (const char*)ws + O_XF;
    auto issue_x = [&](int s) {
#pragma unroll
        for (int i = 0; i < 10; ++i) glds<0>(lane * 16, xf + (size_t)s * 40960 + (wave * 10 + i) * 1024, (unsigned)(uintptr_t)lds + (s & 1) * 40960 + (wave * 10 + i) * 1024);
    };
    claim<true>(sp, (volatile LAS unsigned*)lds, wave, lane);
    const unsigned par = load_par<true>(rw, lane, sp);
    issue_x(0);
    const unsigned ol = (sel ? R_KX : R_QX) + (c0 >> 5) * 2048, ob = ((ol >> 13) << 14) | (ol & 8191u);
    unsigned seen = 0;
    for (int s = 0; s < NSEG; ++s) {
        MARK(0);
        const bool drain = !QLAZY || s < 6 || (int)seen + 6 < s || (int)seen + 4 > s + QLAZY;
        if (drain) vmwait<0>(); else vmwait<4>();
        if (lane == 0) ((volatile LAS unsigned*)(lds + 81920))[wave + 4 * (s & 1)] = drain;
        bar();
        MARK(1);
        if (s >= 1 && wave == 0) {
            const bool all = !any_lane(lane < 4 && ((volatile LAS unsigned*)(lds + 81920))[(lane & 3) + 4 * (s & 1)] == 0u);
            if (lane == 0) st32((unsigned)(all ? s : s - 1), rw, (unsigned)(O_CTRL + 4 * (W_QKPROG + 32 * cu)), 0);
        }
        wait_slot(rw, s, seen, cu & 7, sp);
        LAS char* l16 = opq(lds + (s & 1) * 40960 + lane * 16);
        f32x4 acc[4][2];
        u32x4 xb[40];
        unsigned pg;
#pragma unroll
        for (int i = 0; i < 4; ++i) xb[i] = lds128(l16 + (i & 1) * 20480 + (i >> 1) * 1024);
        const char* xn = xf + (size_t)(s + 1 < NSEG ? s + 1 : s) * 40960 + wave * 10240;
        const unsigned xd = (unsigned)(uintptr_t)lds + (~s & 1) * 40960 + wave * 10240;
#pragma unroll
        for (int i = 0; i < 40; ++i)
#pragma unroll
            for (int t = 0; t < 4; ++t) {
                if (t == 2 && i % 3 == 0 && i < 30) glds<0>(lane * 16, xn + (i / 3) * 1024, xd + (i / 3) * 1024);
                if (t == 2 && i == 31) pg = ld32(rw, 0, (unsigned)(O_CTRL + 4 * (W_PROG + 32 * (cu & 7))));
                if (i < 32) mf_av(acc[t][i & 1], w[t][i >> 1], xb[i], i < 2); else mf_vv(acc[t][i & 1], w[t][i >> 1], xb[i], false);
                if (t == 0 && i + 4 < 40) xb[i + 4] = lds128(l16 + (i & 1) * 20480 + ((i + 4) >> 1) * 1024);
            }
        mf_done(acc[0][0], acc[1][0], acc[2][0], acc[3][0]); mf_done(acc[0][1], acc[1][1], acc[2][1], acc[3][1]);
        seen = __builtin_amdgcn_readfirstlane(pg);
#pragma unroll
        for (int pr = 0; pr < 2; ++pr)
#pragma unroll
            for (int T = 0; T < 2; ++T) {
                const f32x4 a = acc[2 * pr][T], b = acc[2 * pr + 1][T];
                st128(u32x4{pk(a[0], a[1]), pk(a[2], a[3]), pk(b[0], b[1]), pk(b[2], b[3])}, rw, pr * 2048 + T * 1024 + lane * 16, page(par, s, 1) + ob);
            }
        MARK(2); MARKC(6); FLUSH(s);
    }
    vmwait<0>();
    bar();
    if (wave == 0 && lane == 0) st32((unsigned)NSEG, rw, (unsigned)(O_CTRL + 4 * (W_QKPROG + 32 * cu)), 0);
}

DEV void role_F(KP& P, unsigned char* ws, int f, LAS char* lds, int wave, int lane, Spin& sp) {
    constexpr int L_FL = 131072, L_U = L_FL + 1024;
    const rsrc_t rw = mk_rsrc(ws, (unsigned)WS_END);
    volatile LAS unsigned* fl = (volatile LAS unsigned*)(lds + L_FL);
    if (wave == 0 && lane < 8) fl[lane] = lane == 5 ? ~0u : 0u;
    bar();
    const unsigned par = load_par(rw, lane, sp);
    const int g = lane >> 4, n = lane & 15;
    const unsigned l16 = lane * 16;
    const unsigned xr = ((f % 10) * 3) & 15;
    auto lwait = [&](int i, int need, unsigned code) {
        for (unsigned c = 0, k = 0; (int)__builtin_amdgcn_readfirstlane(fl[i]) < need;) { if ((++k & 63u) == 0u && sp.fail(c, code)) break; __builtin_amdgcn_s_sleep(LSLEEP); }
        asm volatile("" ::: "memory");
    };
    auto lset = [&](int i, int v) { asm volatile("s_waitcnt lgkmcnt(0)" ::: "memory"); if (lane == 0) fl[i] = (unsigned)v; };
    if (wave == 3) {
        unsigned have0 = 0, have1 = 0;
        const unsigned vf = min(lane, 39) * 128;
        for (int s = 0; s < NSEG; ++s) {
            MARK(0);
            unsigned hv = (s & 1) ? have1 : have0;
            for (unsigned c = 0; hv <= (unsigned)s;) {
                const unsigned v = ld32(rw, (lane & 31) * 128, (unsigned)(O_CTRL + 4 * (W_VCPROG + 1024 * (s & 1))));
                if (!any_lane(v <= (unsigned)s)) {
                    hv = ~wave_max(~v);
                    break;
                }
                if (sp.fail(c, 0x300 + s)) break;
                __builtin_amdgcn_s_sleep(2);
            }
            if (s & 1) have1 = hv; else have0 = hv;
            lwait(2, s - 3, 0x341); lwait(3, s - 3, 0x342); lwait(1, s - 1, 0x340);
            MARK(1);
            const unsigned cpg = page(par, s, f < 20 ? 2 : 3) + (f % 20) * 65536;
            const unsigned no = rec(par, s - 1) + amap(R_NF);
            u32x2 nf;
            if (s) __builtin_amdgcn_s_sleep(4);
            asm volatile("buffer_load_dwordx2 %0, %1, %2, %3 offen sc1" : "=v"(nf) : "v"(vf), "s"(rw), "s"(no));
#pragma unroll
            for (int i = 0; i < 16; ++i) glds_dev<2, 1024>(l16, ws + ((cpg ^ (CMIX ? (((i ^ xr) >> 2) & 1) << 13 : 0)) + ((i ^ xr) >> 2) * 16384 + ((i ^ xr) & 3) * 2048), (unsigned)(uintptr_t)lds + (s & 3) * 32768 + i * 2048);
            asm volatile("s_waitcnt vmcnt(32)" : "+v"(nf));
            if (s >= 1) {
                for (unsigned c = 0; any_lane(max(nf.x, nf.y) == SENT);) { if (sp.fail(c, 0x390)) break; nf = ld64(rw, vf, no); }
                const u32x2 pl = read_notes(nf);
                if (lane == 0) { fl[6] = pl.x; fl[7] = pl.y; }
                lset(5, s - 1);
            }
            vmwait<0>();
            lset(0, s + 1);
            MARK(2); FLUSH(s);
        }
    } else if (wave == 0) {
        const int idx = (lane & 1) ? 0xEEEE : 0x4444;
        const unsigned mo = amap(R_MEM + f * 256) + (2 * (2 * g + (n & 1)) + ((n >> 1) & 1)) * 16 + (n >> 2) * 4;
        const unsigned psel = (n & 1) ? 0x07060302u : 0x01000504u;
        unsigned pv[4], ps[4];
#pragma unroll
        for (int j = 0; j < 4; ++j) { pv[j] = l16 + ((j ^ xr) & 3) * 1024; const unsigned k = j ^ (xr >> 2); ps[j] = amap(R_P) + (k & 1) * 4096 + (k >> 1) * 16384; }
        Clock ck; ck.org = sp.org;
        unsigned dur = 64, tp = 0, plan = 0, dS = 139, dU = 126;
        unsigned boost = 0, lead = 10;
        __builtin_amdgcn_s_setprio(3);
        for (int s = 0; s < NSEG - 1; ++s) {
            MARK(0);
            lwait(0, s + 1, 0x343);
            if (s == 0) { tp = ck.now() + 20u; plan = tp + dur + 30u + DELTA_B; if (lane == 0) st64(u32x2{tp, dur + 30u}, rw, f * 128, rec(par, 0) + amap(R_NF)); }
            LAS char* cb = opq(lds + (s & 3) * 32768 + l16);
            u32x8 bc[16];
#pragma unroll
            for (int t = 0; t < 16; ++t) bc[t] = cat4(lds128(cb + 2 * t * 1024), lds128(cb + (2 * t + 1) * 1024));
#pragma unroll
            for (int t = 0; t < 16; ++t) pin_a(bc[t]);
            const unsigned rb = rec(par, s), rn = rec(par, s + 1), ub = rb + amap(R_UB) + f * 16384, pb = rb + amap(R_P);
            unsigned so4[4];
#pragma unroll
            for (int j = 0; j < 4; ++j) { so4[j] = rb + ps[j]; asm volatile("" : "+s"(so4[j])); }
            if (s > 0) { lwait(5, s, 0x349); tp = __builtin_amdgcn_readfirstlane(fl[6]); plan = __builtin_amdgcn_readfirstlane(fl[7]); }
            const unsigned tu = tp - lead;
            unsigned res, rns = rn; asm volatile("" : "+s"(rns));
            TA_DECL;
            u32x4 u[8], px[16], pm[4];
            f32x4 d; zero_acc(d);
            MARK2(1);
            ck.wait_fine(tu);
#pragma unroll
            for (int i = 0; i < 8; ++i) u[i] = ld128p<(RNT & 1) != 0>(rw, l16 + (i & 3) * 1024, ub + (i >> 2) * 4096);
            const unsigned tg = ck.wait_fine(tp);
            SETV(3, tg + ck.org); SETV(4, tp + ck.org); SETV(5, plan - DELTA_B + ck.org); SETV(6, tu + ck.org);
            bool failed = false;
            TA(0); TA(6); TA(7);
            for (unsigned c = 0;;) {
#pragma unroll
                for (int i = 0; i < 16; ++i) px[i] = ld128p<(RNT & 4) != 0>(rw, pv[i & 3], so4[i >> 2]);
#pragma unroll
                for (int i = 0; i < 4; ++i) pm[i] = ld128p<(RNT & 4) != 0>(rw, l16 + i * 1024, pb + 32768);
                TA(1);
#pragma unroll
                for (int i = 0; i < 16; ++i) { smf_a(d, px[i], bc[i], idx); if (i == 0) { TA(2); } if (i == 15) { TA(3); } }
#pragma unroll
                for (int i = 0; i < 4; ++i) smf_v(d, pm[i], cat4(u[2 * i], u[2 * i + 1]), idx);
                TA(4);
                unsigned long long bad; float r0, r1;
                asm volatile("s_nop 7\n\tv_add_f32 %2, %4, %5\n\tv_add_f32 %3, %6, %7\n\tv_cvt_pk_bf16_f32 %0, %2, %3\n\tv_cmp_u_f32 %1, %2, %3\n\ts_nop 0\n\t"
                             "v_mov_b32_dpp %2, %0 quad_perm:[1,0,3,2] row_mask:0xf bank_mask:0xf\n\tv_perm_b32 %0, %0, %2, %8\n\tv_min_u32 %0, -2, %0"
                             : "=&v"(res), "=&s"(bad), "=&v"(r0), "=&v"(r1) : "v"(d[0]), "v"(d[1]), "v"(d[2]), "v"(d[3]), "v"(psel));
                if (__builtin_expect(bad == 0, 1)) { SETV(7, c); break; }
                if (c > 0) {
                    unsigned sg = 0; bool miss = false;
#pragma unroll
                    for (int i = 0; i < 8; ++i) miss |= mx4(u[i]) == SENT;
#pragma unroll
                    for (int i = 0; i < 16; ++i) sg |= px[i].x | px[i].y | px[i].z | px[i].w;
#pragma unroll
                    for (int i = 0; i < 4; ++i) sg |= pm[i].x | pm[i].y | pm[i].z | pm[i].w;
                    if (!any_lane(miss | ((sg & 0x80008000u) != 0u))) break;
                }
                if (sp.fail(c, 0x400 + s)) break;
                failed = true;
                zero_acc(d);
#pragma unroll
                for (int i = 0; i < 8; ++i) u[i] = ld128p<(RNT & 1) != 0>(rw, l16 + (i & 3) * 1024, ub + (i >> 2) * 4096);
            }
            st32(res, rw, mo, rns);
            TA(5);
            const unsigned tpub = ck.now();
            const unsigned ns = ld32(rw, lane * 128, rb + amap(R_NS));
            const unsigned prop = (tpub + VIS <= plan ? plan : tpub + LATE) + max(dS, dU + 8u) + DELTA_A + (failed ? boost : 0u) + (s < NEARLY ? EARLY : 0u);
            boost = failed ? min(boost + 40u, 400u) : 0u;
            if (lane == 0) { st64(u32x2{prop, dur + (s < NEARLY ? EARLY / 2 : 0u)}, rw, f * 128, rn + amap(R_NF)); fl[1] = (unsigned)(s + 1); }
            if (!failed && tg - tp < 15u) dur = track(dur, tpub - tp, s);
            SETV(2, tpub + ck.org);
            lwait(2, s - 1, 0x346); lwait(3, s - 1, 0x347);
#pragma unroll
            for (int i = 0; i < 8; ++i) *(LAS u32x4*)(lds + L_U + (s & 1) * 8192 + i * 1024 + l16) = u[i];
            lset(4, s + 1);
            tp = prop; plan = prop + dur + DELTA_B; lead = max(dS, dU + 8u) - dU;
            if (!any_lane(ns == SENT)) { const unsigned m = half_max(min(ns, 400u)); dS = __builtin_amdgcn_readlane(m, 0); dU = __builtin_amdgcn_readlane(m, 32); }
            if (f == 0 && lane < 8) st32((unsigned)(s + 1), rw, lane * 128, (unsigned)(O_CTRL + 4 * W_PROG));
            if (s + KARM < NSEG) { const unsigned ra = rec(par, s + KARM); st32(sent1(), rw, mo, ra); if (lane == 0) st64(SENT2, rw, f * 128, ra + amap(R_NF)); }
            FLUSH(s); TA_FLUSH(s);
        }
        {
            const unsigned ub = rec(par, NSEG - 1) + amap(R_UB) + f * 16384;
            u32x4 u[8];
            ck.wait_until(tp);
            lset(1, NSEG);
            for (unsigned c = 0;;) {
                unsigned m = 0;
#pragma unroll
                for (int i = 0; i < 8; ++i) u[i] = ld128p<(RNT & 1) != 0>(rw, l16 + (i & 3) * 1024, ub + (i >> 2) * 4096);
#pragma unroll
                for (int i = 0; i < 8; ++i) m = max(m, mx4(u[i]));
                if (!any_lane(m == SENT) || sp.fail(c, 0x4FF)) break;
            }
            lwait(2, NSEG - 2, 0x346); lwait(3, NSEG - 2, 0x347);
#pragma unroll
            for (int i = 0; i < 8; ++i) *(LAS u32x4*)(lds + L_U + ((NSEG - 1) & 1) * 8192 + i * 1024 + l16) = u[i];
            lset(4, NSEG);
        }
    } else {
        auto xrows = [&](auto TQ) __attribute__((always_inline)) {
            constexpr int Tq = decltype(TQ)::value, NX = Tq ? 32 : 16;
            const rsrc_t rx = mk_rsrc(P.x, S_ * D_ * 2);
            const unsigned ho = (f >> 1) * 1024 + ((2 * (f & 1) + (g >> 1)) * 16 + n) * 16 + (g & 1) * 8;
            for (int s = 0; s < NSEG; ++s) {
                MARK(0);
                lwait(0, s + 1, 0x345);
                LAS char* cb = opq(lds + (s & 3) * 32768 + l16);
                u32x4 ca[NX];
#pragma unroll
                for (int i = 0; i < NX; ++i) ca[i] = Tq ? lds128(cb + i * 1024) : cat2(*(LAS const u32x2*)(cb + i * 2048), *(LAS const u32x2*)(cb + i * 2048 + 1024));
#pragma unroll
                for (int i = 0; i < NX; ++i) pin_a(ca[i]);
                const unsigned pb = rec(par, s) + amap(R_PX) + Tq * amap(PX_B);
                lwait(1, s + 1, 0x344);
                MARK2(1);
                f32x4 d; u32x2 xv;
                for (unsigned c = 0;;) {
                    u32x4 u[8], px[NX + 8];
#pragma unroll
                    for (int i = 0; i < NX; ++i) {
                        if (Tq) { const unsigned k = (i >> 1) ^ xr; px[i] = ld128p<(RNT & 2) != 0>(rw, l16 + (i & 1) * 1024, pb + (k & 3) * 2048 + (k >> 2) * 16384); }
                        else { const unsigned k = i ^ xr; px[i] = ld128p<(RNT & 2) != 0>(rw, l16, pb + (k & 7) * 1024 + (k >> 3) * 16384); }
                    }
#pragma unroll
                    for (int i = 0; i < 8; ++i) px[NX + i] = ld128p<(RNT & 2) != 0>(rw, l16 + (i & 3) * 1024, pb + 65536 + (i >> 2) * 4096);
                    xv = __builtin_amdgcn_raw_buffer_load_b64(rx, ((32 * s + 16 * Tq + n) * D_ + 16 * f + 4 * g) * 2, 0, 0);
                    lwait(4, s + 1, 0x348);
#pragma unroll
                    for (int i = 0; i < 8; ++i) u[i] = lds128(lds + L_U + (s & 1) * 8192 + i * 1024 + l16);
#pragma unroll
                    for (int i = 0; i < NX; ++i) mf_av(d, ca[i], px[i], i == 0);
#pragma unroll
                    for (int i = 0; i < 8; ++i) mf_vv(d, u[i], px[NX + i], false);
                    mf_done(d);
                    if (!any_lane(__builtin_isunordered(d[0] + d[1], d[2] + d[3]))) { SETV(7, c); break; }
                    bool miss = false;
#pragma unroll
                    for (int i = 0; i < NX + 8; ++i) miss |= mx4(px[i]) == SENT;
                    if (!any_lane(miss)) break;
                    if (sp.fail(c, 0x500 + s)) break;
                }
                MARK(2);
                lset(2 + Tq, s + 1);
                const unsigned a01 = pk(d[0], d[1]), a23 = pk(d[2], d[3]);
                st64(u32x2{pks(bflo(xv.x) + bflo(a01), bfhi(xv.x) + bfhi(a01)), pks(bflo(xv.y) + bflo(a23), bfhi(xv.y) + bfhi(a23))}, rw, ho, (unsigned)(O_HF + (size_t)(2 * s + Tq) * XF_B));
                if (2 * (s + KARM) < UTAIL) st64(SENT2, rw, ho, (unsigned)(O_HF + (size_t)(2 * (s + KARM) + Tq) * XF_B));
                MARK(3); FLUSH(s);
            }
        };
        if (wave == 1) xrows(IC<0>{}); else xrows(IC<1>{});
    }
}

DEV float gelu_t(float x) {
    const float t = x * __builtin_fmaf(x * x, -0.10294324f, -2.30220820f);
    return x * __builtin_amdgcn_rcpf(1.f + __builtin_amdgcn_exp2f(t));
}

template <int NK> DEV bool any_sent(const u32x4 (&x)[NK]) {
    unsigned m = 0;
#pragma unroll
    for (int k = 0; k < NK; ++k) m = max(m, mx4(x[k]));
    return any_lane(m == SENT);
}
template <int U0, int NSET> DEV int next_unit(int u) { return u < U0 - 1 ? u + 1 : u >= U0 ? u + NSET : NSET == 4 ? U0 + 2 : NUNIT; }
template <int U0, int NSET> DEV int first_unit(int rep) { return rep == 0 ? 0 : rep < 3 ? U0 + rep - 1 : U0 + NSET - 1; }
template <int MODE>
DEV void role_W(KP& P, unsigned char* ws, int cb, int rep, LAS char* lds, int wave, int lane, Spin& sp) {
    constexpr int U0 = MODE == 1 ? U0_1 : MODE == 2 ? U0_2 : U0_3, NSET = 4;
    const rsrc_t rw = mk_rsrc(ws, (unsigned)WS_END);
    constexpr unsigned stride = MODE == 3 ? F_ * 2 : D_ * 2;
    const char* wsrc = (const char*)(MODE == 1 ? P.w1 : MODE == 2 ? P.w2 : P.w3) + (size_t)wave * 160 * stride + cb * 256;
    u32x4 w[8][5];
    stream_w<8, 5, 5, 5, 0, true>(wsrc, stride, lds, wave * 40960, lane, [&](int t, int k, u32x4 f) { w[t][k] = f; pin_a(w[t][k]); }, 0, 2 * wave + 2);
    vmwait<0>();
    bar();
    const int g = lane >> 4, n = lane & 15;
    const unsigned l16 = lane * 16;
    if (rep == 0) load_par<false>(rw, lane, sp);
    constexpr size_t O_IN = MODE == 1 ? O_HF : MODE == 2 ? O_GF : O_X1F;
    LAS char* wr[3];
#pragma unroll
    for (int j = 0; j < 3; ++j) wr[j] = opq(lds + ((wave + 1 + j) & 3) * 6144 + j * 2048 + l16);
    LAS char* rd = opq(lds + wave * 6144 + l16);
    LAS char* sw = opq(lds + 24576 + wave * 512 + lane * 8);
    LAS char* sr = opq(lds + 24576 + ((n >> 2) * 16 + n) * 8);
    u32x4 ones = {0x3F803F80u, 0x3F803F80u, 0x3F803F80u, 0x3F803F80u}; pin_v(ones);
    auto fetch1 = [&](int u, int k) __attribute__((always_inline)) { return ld128(rw, l16, (unsigned)(O_IN + (size_t)u * XF_B) + (wave * 5 + k) * 1024); };
    auto probe = [&](int u) __attribute__((always_inline)) { return ld32(rw, 1008, (unsigned)(O_IN + (size_t)u * XF_B) + (wave * 5 + 4) * 1024); };
    auto product = [&](const u32x4 (&xb)[5], auto&& side, int buf, f32x4& o0, f32x4& o1, float& S1, float& S2) __attribute__((always_inline)) {
        f32x4 acc[8], s1, s2;
        if (MODE != 2) {
#pragma unroll
            for (int k = 0; k < 5; ++k) { mf_vv(s1, ones, xb[k], k == 0); mf_vv(s2, xb[k], xb[k], k == 0); }
        }
        sfor<0, 40>([&](auto I) __attribute__((always_inline)) {
            constexpr int i = decltype(I)::value, t = 2 * (i / 10) + (i & 1), k = (i % 10) >> 1;
            mf_av(acc[t], w[t][k], xb[k], k == 0);
            if constexpr (i % 4 == 0) side(IC<i / 4>{});
            if constexpr (i >= 17 && (i % 10 == 7 || i % 10 == 8)) { constexpr int tw = 2 * (i / 10 - 1) + (i % 10 - 7); pin_v(acc[tw]); *(LAS f32x4*)(wr[tw >> 1] + buf * 28672 + (tw & 1) * 1024) = acc[tw]; }
            if constexpr (MODE != 2 && i == 20) {
                asm volatile("" : "+v"(s1), "+v"(s2));
                const float d = (n & 2) ? ((n & 1) ? s2[3] : s2[2]) : ((n & 1) ? s2[1] : s2[0]);
                *(LAS f32x2*)(sw + buf * 28672) = f32x2{s1[0], d};
            }
        });
        bar();
        f32x4 p[6]; f32x2 q[4];
#pragma unroll
        for (int j = 0; j < 6; ++j) p[j] = *(LAS const f32x4*)(rd + buf * 28672 + j * 1024);
        if (MODE != 2) {
#pragma unroll
            for (int j = 0; j < 4; ++j) q[j] = *(LAS const f32x2*)(sr + buf * 28672 + j * 512);
        }
        mf_done(acc[6], acc[7]);
        o0 = (acc[6] + p[0]) + (p[2] + p[4]); o1 = (acc[7] + p[1]) + (p[3] + p[5]);
        if (MODE != 2) { S1 = (q[0].x + q[1].x) + (q[2].x + q[3].x); S2 = (q[0].y + q[1].y) + (q[2].y + q[3].y); }
    };
    f32x4 cs0 = {}, cs1 = {};
    if (MODE != 2) {
        u32x4 o4[5] = {ones, ones, ones, ones, ones};
        asm volatile("s_nop 1" : "+v"(o4[0]), "+v"(o4[1]), "+v"(o4[2]), "+v"(o4[3]), "+v"(o4[4]));
        float a, b; product(o4, [](auto) {}, 1, cs0, cs1, a, b);
    }
    const int kso = 4 * cb + wave;
    const int grp = kso / 5, sh = grp == 0 ? 1 : grp == 1 ? -1 : grp == 2 ? 2 : -2;
    if (MODE == 1 && rep == 0) {
        const int td = sh > 0 ? n : S_ - 1 - n;
        if (n < (sh > 0 ? sh : -sh)) st128(u32x4{0u, 0u, 0u, 0u}, rw, (td >> 4) * (unsigned)XF_B + kso * 1024 + (g * 16 + (td & 15)) * 16, (unsigned)O_GF);
        vmwait<0>();
    }
    u32x4 xa[5], xc[5], hn; u32x2 sn;
    bool miss = true, ahead = false, quiet = false;
    auto extra = [&](int u) __attribute__((always_inline)) { if (MODE == 2) { hn = ld128(rw, l16, (unsigned)(O_HF + (size_t)u * XF_B) + kso * 1024); sn = ld64(rw, (16 * u + n) * 8, (unsigned)O_ST); } };
    auto unit = [&](int u, int buf, u32x4 (&xb)[5], u32x4 (&nx)[5]) __attribute__((always_inline)) {
        MARK2(0);
        const int tok = 16 * u + n, un = next_unit<U0, NSET>(u), u2 = next_unit<U0, NSET>(un);
        for (unsigned c = 0; miss;) {
            while (!quiet) {
                const unsigned pv = probe(u), gv = u >= UFREE ? ld32(rw, 0, (unsigned)(O_CTRL + 4 * (W_PROG + 32 * (cb & 7)))) : 0u;
                quiet = __builtin_amdgcn_readfirstlane(gv) >= NSEG - 1;
                if (__builtin_amdgcn_readfirstlane(pv) != SENT || sp.fail(c, 0x600 + MODE * 0x100 + u)) break;
                __builtin_amdgcn_s_sleep(PSLEEP);
            }
            __builtin_amdgcn_s_sleep(PWAIT);
#pragma unroll
            for (int k = 0; k < 5; ++k) xb[k] = fetch1(u, k);
            extra(u);
            miss = any_sent(xb);
            if (miss && sp.fail(c, 0x680 + u)) break;
        }
        MARK2(1);
        asm volatile("s_nop 1" : "+v"(xb[0]), "+v"(xb[1]), "+v"(xb[2]), "+v"(xb[3]), "+v"(xb[4]));
        const bool early = ahead;
        unsigned pr = SENT;
        if ((early ? u2 : un) < NUNIT) pr = probe(early ? u2 : un);
        u32x4 hv = hn; u32x2 stt = sn;
        if (MODE == 2) asm volatile("" : "+v"(hv), "+v"(stt));
        f32x4 o0, o1; float S1, S2;
        product(xb, [&](auto J) __attribute__((always_inline)) {
            constexpr int j = decltype(J)::value;
            if constexpr (j < 5) { if (early) nx[j] = fetch1(un, j); }
            if constexpr (j == 5) { if (early) extra(un); }
        }, buf, o0, o1, S1, S2);
        if (!early) {
            ahead = __builtin_amdgcn_readfirstlane(pr) != SENT;
            if (ahead) {
#pragma unroll
                for (int k = 0; k < 5; ++k) nx[k] = fetch1(un, k);
                extra(un);
                pr = SENT;
                if (u2 < NUNIT) pr = probe(u2);
            }
        }
        auto settle = [&]() __attribute__((always_inline)) { __builtin_amdgcn_s_waitcnt(0x0F70); miss = !ahead || any_sent(nx); ahead = ahead && __builtin_amdgcn_readfirstlane(pr) != SENT; if (MODE == 2) asm volatile("" : "+v"(hn), "+v"(sn)); };
        MARK2(2);
        if (MODE != 2) {
            const float mu = S1 * (1.f / D_), var = fmaxf(S2 * (1.f / D_) - mu * mu, 0.f);
            float al = __builtin_amdgcn_rsqf(var + 1e-5f);
            if (MODE == 1) {
                if (cb == 0 && wave == 0 && g == 0) st64(u32x2{nosent(f2u(mu)), nosent(f2u(al))}, rw, tok * 8, (unsigned)O_ST);
                al *= __builtin_amdgcn_rsqf(var * al * al + 1e-5f);
            }
            float y[8];
#pragma unroll
            for (int r = 0; r < 4; ++r) { y[r] = gelu_t(al * (o0[r] - mu * cs0[r])); y[4 + r] = gelu_t(al * (o1[r] - mu * cs1[r])); }
            u32x4 o = {pks(y[0], y[1]), pks(y[2], y[3]), pks(y[4], y[5]), pks(y[6], y[7])};
            pin_v(o);
            settle();
            if (MODE == 1) {
                const int td = tok + sh;
                const unsigned vo = (td >> 4) * (int)XF_B + kso * 1024 + (g * 16 + (td & 15)) * 16;
                if (td >= 0 && td < S_) st128(o, rw, vo, (unsigned)O_GF);
                if (td + 32 * KARM < 16 * UTAIL) st128(SENT4, rw, vo + 2 * KARM * (unsigned)XF_B, (unsigned)O_GF);
            } else {
                st128(o, rw, l16, (unsigned)(O_G3F + (size_t)u * G3_B) + kso * 1024);
                if (u + 2 * KARM < UTAIL) st128(SENT4, rw, l16, (unsigned)(O_G3F + (size_t)(u + 2 * KARM) * G3_B) + kso * 1024);
            }
        } else {
            for (unsigned c = 0; any_lane(max(mx4(hv), max(stt.x, stt.y)) == SENT);) {
                if (sp.fail(c, 0xA00 + u)) break;
                hv = ld128(rw, l16, (unsigned)(O_HF + (size_t)u * XF_B) + kso * 1024); stt = ld64(rw, tok * 8, (unsigned)O_ST);
            }
            const float mu = u2f(stt.x), al = u2f(stt.y);
            const u32x4 o = {pks((bflo(hv.x) - mu) * al + o0[0], (bfhi(hv.x) - mu) * al + o0[1]), pks((bflo(hv.y) - mu) * al + o0[2], (bfhi(hv.y) - mu) * al + o0[3]),
                             pks((bflo(hv.z) - mu) * al + o1[0], (bfhi(hv.z) - mu) * al + o1[1]), pks((bflo(hv.w) - mu) * al + o1[2], (bfhi(hv.w) - mu) * al + o1[3])};
            settle();
            st128(o, rw, l16, (unsigned)(O_X1F + (size_t)u * XF_B) + kso * 1024);
            if (u + 2 * KARM < UTAIL) st128(SENT4, rw, l16, (unsigned)(O_X1F + (size_t)(u + 2 * KARM) * XF_B) + kso * 1024);
        }
        MARK(3); FLUSH(u);
    };
    for (int u = first_unit<U0, NSET>(rep);;) {
        unit(u, 0, xa, xc); u = next_unit<U0, NSET>(u); if (u >= NUNIT) break;
        unit(u, 1, xc, xa); u = next_unit<U0, NSET>(u); if (u >= NUNIT) break;
    }
}

DEV void role_W4(KP& P, unsigned char* ws, int cb, int rep, LAS char* lds, int wave, int lane, Spin& sp) {
    constexpr int U0 = U0_4, NSET = 3;
    const rsrc_t rw = mk_rsrc(ws, (unsigned)WS_END);
    const rsrc_t ro = mk_rsrc(P.out, S_ * D_ * 2);
    u32x4 w[4][12];
    stream_w<4, 12, 12, 10, 0, false>((const char*)P.w4 + (size_t)wave * 384 * 1280 + cb * 128, 1280u, lds, wave * 40960, lane,
                                  [&](int t, int k, u32x4 f) { w[t][k] = f; pin_a(w[t][k]); }, 0, wave + 1);
    vmwait<0>();
    bar();
    const int g = lane >> 4, n = lane & 15;
    const unsigned l16 = lane * 16;
    if (rep == 0) load_par<false>(rw, lane, sp);
    const int ct = 4 * cb + wave;
    const unsigned po = (ct >> 1) * 1024 + ((2 * (ct & 1) + (g >> 1)) * 16 + n) * 16 + (g & 1) * 8;
    LAS char* wr[3];
#pragma unroll
    for (int j = 0; j < 3; ++j) wr[j] = opq(lds + ((wave + 1 + j) & 3) * 3072 + j * 1024 + l16);
    LAS char* rd = opq(lds + wave * 3072 + l16);
    auto fetch = [&](u32x4 (&x)[12], int u) __attribute__((always_inline)) {
#pragma unroll
        for (int k = 0; k < 12; ++k) x[k] = ld128(rw, l16 + (k & 3) * 1024, (unsigned)(O_G3F + (size_t)u * G3_B) + (wave * 12 + (k & ~3)) * 1024);
    };
    auto probe = [&](int u) __attribute__((always_inline)) { return ld32(rw, 1008, (unsigned)(O_G3F + (size_t)u * G3_B) + (wave * 12 + 11) * 1024); };
    u32x4 xa[12], xc[12]; u32x2 hn, xn;
    bool miss = true, ahead = false, quiet = false;
    auto extra = [&](int u) __attribute__((always_inline)) { hn = ld64(rw, po, (unsigned)(O_HF + (size_t)u * XF_B)); xn = ld64(rw, po, (unsigned)(O_X1F + (size_t)u * XF_B)); };
    auto unit = [&](int u, int buf, u32x4 (&xb)[12], u32x4 (&nx)[12]) __attribute__((always_inline)) {
        MARK2(0);
        for (unsigned c = 0; miss;) {
            while (!quiet) {
                const unsigned pv = probe(u), gv = u >= UFREE ? ld32(rw, 0, (unsigned)(O_CTRL + 4 * (W_PROG + 32 * (cb & 7)))) : 0u;
                quiet = __builtin_amdgcn_readfirstlane(gv) >= NSEG - 1;
                if (__builtin_amdgcn_readfirstlane(pv) != SENT || sp.fail(c, 0xB00 + u)) break;
                __builtin_amdgcn_s_sleep(PSLEEP);
            }
            __builtin_amdgcn_s_sleep(PWAIT);
            fetch(xb, u); extra(u);
            miss = any_sent(xb);
            if (miss && sp.fail(c, 0xB80 + u)) break;
        }
        MARK2(1);
#pragma unroll
        for (int k = 0; k < 12; ++k) pin_v(xb[k]);
        asm volatile("s_nop 1");
        u32x2 hv = hn, xv = xn;
        const int un = next_unit<U0, NSET>(u), u2 = next_unit<U0, NSET>(un);
        unsigned pr = SENT;
        const bool early = ahead;
        if ((early ? u2 : un) < NUNIT) pr = probe(early ? u2 : un);
        asm volatile("" : "+v"(hv), "+v"(xv));
        f32x4 acc[4];
        sfor<0, 48>([&](auto I) __attribute__((always_inline)) {
            constexpr int i = decltype(I)::value, t = i / 12, k = i % 12;
            mf_av(acc[t], w[t][k], xb[k], k == 0);
            if constexpr (i % 3 == 0 && i < 36) { constexpr int kk = i / 3; if (early) nx[kk] = ld128(rw, l16 + (kk & 3) * 1024, (unsigned)(O_G3F + (size_t)un * G3_B) + (wave * 12 + (kk & ~3)) * 1024); }
            if constexpr (i == 35) { if (early) extra(un); }
            if constexpr (i == 36) { if (!early) { ahead = __builtin_amdgcn_readfirstlane(pr) != SENT; if (ahead) { fetch(nx, un); extra(un); pr = SENT; if (u2 < NUNIT) pr = probe(u2); } } }
            if constexpr (t >= 1 && k == 8) { pin_v(acc[t - 1]); *(LAS f32x4*)(wr[t - 1] + buf * 12288) = acc[t - 1]; }
        });
        bar();
        f32x4 p[3];
#pragma unroll
        for (int j = 0; j < 3; ++j) p[j] = *(LAS const f32x4*)(rd + buf * 12288 + j * 1024);
        mf_done(acc[3]);
        const f32x4 o = (acc[3] + p[0]) + (p[1] + p[2]);
        MARK2(2);
        for (unsigned c = 0; any_lane(max(max(hv.x, hv.y), max(xv.x, xv.y)) == SENT);) {
            if (sp.fail(c, 0xC00 + u)) break;
            hv = ld64(rw, po, (unsigned)(O_HF + (size_t)u * XF_B)); xv = ld64(rw, po, (unsigned)(O_X1F + (size_t)u * XF_B));
        }
        const u32x2 r = {pk(bflo(hv.x) + (bflo(xv.x) + o[0]), bfhi(hv.x) + (bfhi(xv.x) + o[1])), pk(bflo(hv.y) + (bflo(xv.y) + o[2]), bfhi(hv.y) + (bfhi(xv.y) + o[3]))};
        __builtin_amdgcn_s_waitcnt(0x0F70);
        miss = !ahead || any_sent(nx); ahead = ahead && __builtin_amdgcn_readfirstlane(pr) != SENT;
        asm volatile("" : "+v"(hn), "+v"(xn));
        __builtin_amdgcn_raw_buffer_store_b64(r, ro, ((16 * u + n) * D_ + 16 * ct + 4 * g) * 2, 0, 0);
        MARK(3); FLUSH(u);
    };
    for (int u = first_unit<U0, NSET>(rep);;) {
        unit(u, 0, xa, xc); u = next_unit<U0, NSET>(u); if (u >= NUNIT) break;
        unit(u, 1, xc, xa); u = next_unit<U0, NSET>(u); if (u >= NUNIT) break;
    }
}

DEV unsigned char* ws_base(KP& P) { return P.ws + ((0u - (unsigned)(uintptr_t)P.ws) & ((2u << 20) - 1u)); }

DEV void publish_ready(rsrc_t rw, int i, unsigned near, int tid, Spin& sp) {
    vmwait<0>();
    bar();
    if (tid == 0) st128(u32x4{sp.tag, near, rawnow() - 3000u, ~sp.tag}, rw, i * 128, (unsigned)(O_CTRL + 4 * W_READY));
}
DEV void init_F(KP& P, unsigned char* ws, int f, unsigned near, LAS char* lds, int wave, int lane, Spin& sp) {
    const rsrc_t rw = mk_rsrc(ws, (unsigned)WS_END);
    const int tid = wave * 64 + lane;
    LAS float* red = (LAS float*)lds;
    const int r = tid & 15, pc = tid >> 4;
    const rsrc_t rx = mk_rsrc(P.x, S_ * D_ * 2);
    u32x4 v[4][5];
#pragma unroll
    for (int j = 0; j < 4; ++j)
#pragma unroll
        for (int i = 0; i < 5; ++i) v[j][i] = __builtin_amdgcn_raw_buffer_load_b128(rx, (r * D_ + (pc + 16 * i) * 8) * 2, min(f + 40 * j, NUNIT - 1) * (16 * D_ * 2), 0);
    if (f < 3) {
        const int p = f * 256 + tid;
        if (p < 640) {
            const unsigned* src = (const unsigned*)(P.mem0 + ((p >> 1) & 7) * D_ + (p >> 4) * 16 + (p & 1) * 2);
            const u32x4 m = {nosent(src[0]), nosent(src[2]), nosent(src[4]), nosent(src[6])};
            const unsigned o = p * 16, oa = ((o >> 13) << 14) | (o & 8191u);
            st128(m, rw, oa, 0); st128(m, rw, oa, 8192);
        }
    }
    float mu[4];
#pragma unroll
    for (int j = 0; j < 4; ++j) {
        float s1 = 0.f;
#pragma unroll
        for (int i = 0; i < 5; ++i)
#pragma unroll
            for (int k = 0; k < 4; ++k) s1 += bflo(v[j][i][k]) + bfhi(v[j][i][k]);
        s1 = x32(x16(s1, false), false);
        red[(j * 2) * 64 + wave * 16 + r] = s1;
    }
    bar();
#pragma unroll
    for (int j = 0; j < 4; ++j) {
        const LAS float* q = red + (j * 2) * 64 + r;
        mu[j] = ((q[0] + q[16]) + (q[32] + q[48])) * (1.f / D_);
        float s2 = 0.f;
#pragma unroll
        for (int i = 0; i < 5; ++i)
#pragma unroll
            for (int k = 0; k < 4; ++k) { const float a = bflo(v[j][i][k]) - mu[j], c = bfhi(v[j][i][k]) - mu[j]; s2 += a * a + c * c; }
        s2 = x32(x16(s2, false), false);
        red[(j * 2 + 1) * 64 + wave * 16 + r] = s2;
    }
    bar();
#pragma unroll
    for (int j = 0; j < 4; ++j) {
        const LAS float* q = red + (j * 2 + 1) * 64 + r;
        const float rs = __builtin_amdgcn_rsqf(((q[0] + q[16]) + (q[32] + q[48])) * (1.f / D_) + 1e-5f);
        if (f + 40 * j < NUNIT) {
#pragma unroll
            for (int i = 0; i < 5; ++i) {
                u32x4 o;
#pragma unroll
                for (int k = 0; k < 4; ++k) o[k] = pk((bflo(v[j][i][k]) - mu[j]) * rs, (bfhi(v[j][i][k]) - mu[j]) * rs);
                st128(o, rw, (pc + 16 * i) * 256 + r * 16, (unsigned)(O_XF + (size_t)(f + 40 * j) * XF_B));
            }
        }
    }
    publish_ready(rw, f, near, tid, sp);
    bar();
}
DEV void init_arm(unsigned char* ws, int i, int tid, Spin& sp) {
    const rsrc_t rw = mk_rsrc(ws, (unsigned)WS_END);
    const int q = i * 256 + tid;
    constexpr int NT = 40 * 256;
    const u32x4 sv = SENT4;
    if (q < (W_READY - W_QKPROG) / 4) st128(u32x4{0u, 0u, 0u, 0u}, rw, q * 16, (unsigned)(O_CTRL + 4 * W_QKPROG));
    if (q < S_ / 2) st128(sv, rw, q * 16, (unsigned)O_ST);
    constexpr int RU = REC_BLOCKS * 1024;
    for (int r = 0; r < KARM; ++r)
        for (int j = q; j < RU; j += NT) if (r > 0 || j >= 2048) st128(sv, rw, j * 16, (unsigned)r * 4 * PAGE_B);
    constexpr int NA = (2 * KARM + 1) * (int)(XF_B / 16), NG = 2 * KARM * (int)(G3_B / 16);
    for (int j = q; j < NA; j += NT) { st128(sv, rw, j * 16, (unsigned)O_HF); st128(sv, rw, j * 16, (unsigned)O_GF); st128(sv, rw, j * 16, (unsigned)O_X1F); }
    for (int j = q; j < NG; j += NT) st128(sv, rw, j * 16, (unsigned)O_G3F);
    constexpr int TA = (NUNIT - UTAIL) * (int)(XF_B / 16), TG = (NUNIT - UTAIL) * (int)(G3_B / 16);
    for (int j = q; j < TA; j += NT) { st128(sv, rw, j * 16, (unsigned)(O_HF + UTAIL * XF_B)); st128(sv, rw, j * 16, (unsigned)(O_GF + UTAIL * XF_B)); st128(sv, rw, j * 16, (unsigned)(O_X1F + UTAIL * XF_B)); }
    for (int j = q; j < TG; j += NT) st128(sv, rw, j * 16, (unsigned)(O_G3F + UTAIL * G3_B));
    publish_ready(rw, 40 + i, 0u, tid, sp);
}

DEV KP* kargs() { KP* p = (KP*)__builtin_amdgcn_kernarg_segment_ptr(); asm volatile("" : "+s"(p)); return p; }
__global__ void __launch_bounds__(256, 1) k_main(Params) {
    extern __shared__ __attribute__((aligned(16))) char smem[];
    LAS char* lds = (LAS char*)smem;
    int lane = threadIdx.x & 63, wave = __builtin_amdgcn_readfirstlane(threadIdx.x >> 6);
    unsigned char* ws = ws_base(*kargs());
    unsigned bid = __builtin_amdgcn_workgroup_id_x(); asm volatile("" : "+s"(bid));
    const bool again = (bid >> 31) != 0u;
    const unsigned first = again ? bid & 255u : (xcc_id() & 7u) * 32u + ((bid >> 3) & 31u);
    if (first < 128u && (first & 31u) - 8u < 10u) {
        if (wave == 0) {
            const unsigned f = (first >> 5) * 10u + (first & 31u) - 8u;
            const rsrc_t rw = mk_rsrc(ws, (unsigned)WS_END);
            const unsigned o = (f & 31u) * PAGE_B + PAGE_B - 16384u + (f >> 5) * 2048;
            st32(1u, rw, (lane & 1) * 8192, o);
            vmwait<0>();
            unsigned lat[2] = {~0u, ~0u};
#pragma unroll
            for (int i = 0; i < 4; ++i) {
                const int hf = (i ^ (i >> 1)) & 1;
                unsigned long long t0, t1;
                asm volatile("s_memtime %0\n\ts_waitcnt lgkmcnt(0)" : "=s"(t0) :: "memory");
                st32(1u, rw, 0, o + hf * 8192);
                asm volatile("s_waitcnt vmcnt(0)\n\ts_memtime %0\n\ts_waitcnt lgkmcnt(0)" : "=s"(t1) :: "memory");
                lat[hf] = min(lat[hf], (unsigned)(t1 - t0));
            }
            ((LAS unsigned*)lds)[2] = lat[0]; ((LAS unsigned*)lds)[3] = lat[1];
        }
        __builtin_amdgcn_s_barrier();
    }
    Spin sp;
    gu32* ctrl = (gu32*)(ws + O_CTRL);
    sp.tmo = ctrl + W_TMO; sp.dead = false;
    __syncthreads();
    const unsigned near1 = __builtin_amdgcn_readfirstlane(((LAS unsigned*)lds)[3] < ((LAS unsigned*)lds)[2]);
    __syncthreads();
    sp.slot = first; sp.claimed = again;
    int mlp = -1, rep = 0;
    {
        KP& P = *kargs();
        const int xq = sp.slot >> 5, t = sp.slot & 31, y = xq - 4;
        if (xq < 4) {
            if (t < 8) role_S(P, ws, xq * 8 + t, lds, wave, lane, sp);
            else if (t < 18) { claim<false>(sp, (volatile LAS unsigned*)lds, wave, lane); init_F(P, ws, xq * 10 + (t - 8), near1, lds, wave, lane, sp); role_F(P, ws, xq * 10 + (t - 8), lds, wave, lane, sp); }
            else if (t < 26) role_UV(P, ws, xq * 8 + (t - 18), lds, wave, lane, sp);
            else mlp = xq * 6 + (t - 26);
        } else {
            if (t < 16) { role_VC(P, ws, y * 8 + (t >> 1), t & 1, lds, wave, lane, sp); mlp = (t >> 1) * 4 + y; rep = 1 + (t & 1); }
            else if (t < 28) { role_QK(P, ws, y * 12 + (t - 16), lds, wave, lane, sp); if (t < 24) { mlp = (t - 16) * 4 + y; rep = 3; } }
            else mlp = t < 30 ? 24 + y * 2 + (t - 28) : 32 + y * 2 + (t - 30);
        }
        if (xq < 4 ? t >= 26 : t >= 28) claim<false>(sp, (volatile LAS unsigned*)lds, wave, lane);
    }
    lane = __builtin_amdgcn_mbcnt_hi(~0u, __builtin_amdgcn_mbcnt_lo(~0u, 0u));
    asm volatile("" : "+v"(lane));
    const int tid = wave * 64 + lane;
    if (mlp >= 0) {
        KP& P = *kargs();
        if (rep == 0) {
            const unsigned t0 = rawnow();
            init_arm(ws, mlp, tid, sp);
            while (rawnow() - t0 < DLY2) __builtin_amdgcn_s_sleep(4);
        }
        if (mlp < 5) role_W<1>(P, ws, mlp, rep, lds, wave, lane, sp);
        else if (mlp < 10) role_W<2>(P, ws, mlp - 5, rep, lds, wave, lane, sp);
        else if (mlp < 22) role_W<3>(P, ws, mlp - 10, rep, lds, wave, lane, sp);
        else if (mlp < 32) role_W4(P, ws, mlp - 22, rep, lds, wave, lane, sp);
        if (mlp == 22 && rep == 0 && tid == 0) __hip_atomic_store(ctrl + W_EPOCH, sp.tag, RLX_AGENT);
    }
    MARK(0); MARKC(1); FLUSH(129);
    asm volatile(".Lmain_end:");
}

extern "C" void kernel_launch(void* const* d_in, const int* in_sizes, int n_in, void* d_out, int out_size, void* d_ws, size_t ws_size, hipStream_t stream) {
    Params P;
    P.x = (const unsigned short*)d_in[0]; P.mem0 = (const unsigned short*)d_in[1];
    P.wq = (const unsigned short*)d_in[2]; P.wk = (const unsigned short*)d_in[3]; P.wv = (const unsigned short*)d_in[4]; P.wo = (const unsigned short*)d_in[5];
    P.w1 = (const unsigned short*)d_in[8]; P.w2 = (const unsigned short*)d_in[9]; P.w3 = (const unsigned short*)d_in[10]; P.w4 = (const unsigned short*)d_in[11];
    P.out = (unsigned short*)d_out; P.ws = (unsigned char*)d_ws;
    (void)hipFuncSetAttribute((const void*)k_main, hipFuncAttributeMaxDynamicSharedMemorySize, LDS_BYTES);
    hipLaunchKernelGGL(k_main, dim3(256), dim3(256), LDS_BYTES, stream, P);
}
```

```cpp
#include <hip/hip_runtime.h>
#include <stdint.h>

#define GAS __attribute__((address_space(1)))
#define LAS __attribute__((address_space(3)))
#define RLX_AGENT __ATOMIC_RELAXED, __HIP_MEMORY_SCOPE_AGENT
#define DEV __device__ __forceinline__
typedef __bf16 bf16x2 __attribute__((ext_vector_type(2)));
typedef short s16x4 __attribute__((ext_vector_type(4)));
typedef float f32x2 __attribute__((ext_vector_type(2)));
typedef float f32x4 __attribute__((ext_vector_type(4)));
typedef unsigned u32x2 __attribute__((ext_vector_type(2)));
typedef unsigned u32x4 __attribute__((ext_vector_type(4)));
typedef unsigned u32x8 __attribute__((ext_vector_type(8)));
typedef GAS unsigned gu32;
typedef __amdgpu_buffer_rsrc_t rsrc_t;

constexpr int S_ = 2048, D_ = 640, H_ = 32, E_ = 192, F_ = 1536, NSEG = 64, NUNIT = 128;
constexpr unsigned SENT = 0xFFFFFFFFu;
constexpr unsigned SPINCAP = 1u << 18;

constexpr unsigned PAGE_B = 2u << 20;
constexpr int NSLOT = 8;
constexpr unsigned R_MEM = 0;
constexpr unsigned R_P = 2 * 8192;
constexpr unsigned R_UB = 5 * 8192;
constexpr unsigned R_PX = 45 * 8192;
constexpr unsigned PX_B = 40960;
constexpr unsigned R_NF = 55 * 8192;
constexpr unsigned R_NS = 56 * 8192;
constexpr int REC_BLOCKS = 57;
constexpr unsigned DELTA_A = 12, DELTA_B = 13;
constexpr unsigned VIS = 2, LATE = 34, SLOW = 105;
constexpr unsigned EARLY = 16; constexpr int NEARLY = 3;
constexpr unsigned MLP_DELAY = 1500;
constexpr int U0_1 = 116, U0_2 = 108, U0_3 = 106, U0_4 = 96;
constexpr int UFREE = 118;
constexpr unsigned amap(unsigned off) { return ((off >> 13) << 14) | (off & 8191u); }
constexpr int UTAIL = 96;
constexpr int KARM = 4;
constexpr size_t O_CTRL = (size_t)NSLOT * 4 * PAGE_B, CTRL_BYTES = 65536;
constexpr int W_TMO = 0  , W_EPOCH = 64, W_READY = 12288  , W_CLAIM = 15360  , W_QKPROG = 1024  , W_VCPROG = 4096  , W_PROG = 8192  ;
constexpr size_t O_ST = O_CTRL + CTRL_BYTES + 4096;
constexpr size_t O_HF = O_ST + S_ * 8, XF_B = 20480;
constexpr size_t O_GF = O_HF + NUNIT * XF_B;
constexpr size_t O_X1F = O_GF + NUNIT * XF_B;
constexpr size_t O_G3F = O_X1F + NUNIT * XF_B, G3_B = 49152;
constexpr size_t O_XF = O_G3F + NUNIT * G3_B;
constexpr unsigned QX_B = 12288, R_QX = 0, R_KX = H_ * QX_B;
constexpr size_t WS_END = O_XF + NUNIT * XF_B;
static_assert(WS_END < (238u << 20), "workspace");
constexpr int LDS_BYTES = 160 * 1024;

struct Params;
typedef const __attribute__((address_space(4))) Params KP;
struct Params {
    const unsigned short *x, *mem0, *wq, *wk, *wv, *wo, *w1, *w2, *w3, *w4;
    unsigned short* out;
    unsigned char* ws;
};

DEV unsigned xcc_id() { return (unsigned)__builtin_amdgcn_s_getreg(20 | (3 << 11)) & 0xfu; }
DEV rsrc_t mk_rsrc(const void* p, unsigned bytes) { return __builtin_amdgcn_make_buffer_rsrc((void*)p, 0, bytes, 0x00020000); }
DEV u32x4 ld128(rsrc_t r, unsigned v, unsigned s) { return __builtin_amdgcn_raw_buffer_load_b128(r, v, s, 16); }
DEV u32x2 ld64(rsrc_t r, unsigned v, unsigned s) { return __builtin_amdgcn_raw_buffer_load_b64(r, v, s, 16); }
DEV unsigned ld32(rsrc_t r, unsigned v, unsigned s) { return __builtin_amdgcn_raw_buffer_load_b32(r, v, s, 16); }
DEV void st128(u32x4 d, rsrc_t r, unsigned v, unsigned s) {
    asm("" : "+v"(d));
    __builtin_amdgcn_raw_buffer_store_b128(d, r, v, s, 16);
    asm volatile("s_nop 1" :: "v"(d) : "memory");
}
DEV void st64(u32x2 d, rsrc_t r, unsigned v, unsigned s) { __builtin_amdgcn_raw_buffer_store_b64(d, r, v, s, 16); }
DEV void st32(unsigned d, rsrc_t r, unsigned v, unsigned s) { __builtin_amdgcn_raw_buffer_store_b32(d, r, v, s, 16); }
DEV u32x2 trd(LAS const char* p) { return __builtin_bit_cast(u32x2, __builtin_amdgcn_ds_read_tr16_b64_v4i16((LAS s16x4*)p)); }
DEV u32x4 cat2(u32x2 a, u32x2 b) { return __builtin_shufflevector(a, b, 0, 1, 2, 3); }
DEV u32x8 cat4(u32x4 a, u32x4 b) { return __builtin_shufflevector(a, b, 0, 1, 2, 3, 4, 5, 6, 7); }
DEV u32x4 lo4(u32x8 a) { return __builtin_shufflevector(a, a, 0, 1, 2, 3); }
DEV u32x4 lds128(LAS const char* p) { return *(LAS const u32x4*)p; }
DEV unsigned pk(float a, float b) { bf16x2 v = {(__bf16)a, (__bf16)b}; return __builtin_bit_cast(unsigned, v); }
DEV unsigned nosent(unsigned u) { return min(u, SENT - 1u); }
DEV unsigned pks(float a, float b) { return min(pk(a, b), SENT - 1u); }
DEV unsigned pka(float a, float b) { unsigned r; asm("v_cvt_pk_bf16_f32 %0, |%1|, |%2|" : "=v"(r) : "v"(a), "v"(b)); return r; }
DEV float bflo(unsigned u) { return __builtin_bit_cast(float, u << 16); }
DEV float bfhi(unsigned u) { return __builtin_bit_cast(float, u & 0xFFFF0000u); }
DEV unsigned f2u(float f) { return __builtin_bit_cast(unsigned, f); }
DEV float u2f(unsigned u) { return __builtin_bit_cast(float, u); }
DEV unsigned mx4(u32x4 v) { return max(max(v.x, v.y), max(v.z, v.w)); }
DEV bool any_lane(bool c) { return __builtin_amdgcn_ballot_w64(c) != 0; }
DEV void bar() { asm volatile("s_waitcnt lgkmcnt(0)" ::: "memory"); __builtin_amdgcn_s_barrier(); asm volatile("" ::: "memory"); }
DEV float x32(float v, bool mx) { auto r = __builtin_amdgcn_permlane32_swap(f2u(v), f2u(v), false, false); return mx ? fmaxf(u2f(r[0]), u2f(r[1])) : u2f(r[0]) + u2f(r[1]); }
DEV float x16(float v, bool mx) { auto r = __builtin_amdgcn_permlane16_swap(f2u(v), f2u(v), false, false); return mx ? fmaxf(u2f(r[0]), u2f(r[1])) : u2f(r[0]) + u2f(r[1]); }
template <int OFF> DEV void glds(unsigned voff, const void* sbase, unsigned lds_dst) {
    asm volatile("s_mov_b32 m0, %2\n\ts_nop 0\n\tglobal_load_lds_dwordx4 %0, %1 offset:%3 sc1" :: "v"(voff), "s"(sbase), "s"(lds_dst), "n"(OFF) : "memory");
}
template <int N, int STEP> DEV void glds_dev(unsigned voff, const void* sbase, unsigned lds_dst) {
    static_assert(N == 2 || N == 4, "");
    if (N == 2) asm volatile("s_mov_b32 m0, %2\n\ts_nop 0\n\tglobal_load_lds_dwordx4 %0, %1 sc1 nt\n\tglobal_load_lds_dwordx4 %0, %1 offset:%3 sc1 nt" :: "v"(voff), "s"(sbase), "s"(lds_dst), "n"(STEP) : "memory");
    else asm volatile("s_mov_b32 m0, %2\n\ts_nop 0\n\tglobal_load_lds_dwordx4 %0, %1 sc1 nt\n\tglobal_load_lds_dwordx4 %0, %1 offset:%3 sc1 nt\n\tglobal_load_lds_dwordx4 %0, %1 offset:%4 sc1 nt\n\tglobal_load_lds_dwordx4 %0, %1 offset:%5 sc1 nt"
                      :: "v"(voff), "s"(sbase), "s"(lds_dst), "n"(STEP), "n"(2 * STEP), "n"(3 * STEP) : "memory");
}
template <int N> DEV void vmwait() { asm volatile("s_waitcnt vmcnt(%0)" :: "n"(N) : "memory"); }
template <class T> DEV void pin_a(T& v) { asm volatile("" : "+a"(v)); }
template <class T> DEV void pin_v(T& v) { asm volatile("" : "+v"(v)); }
#define MF_ "v_mfma_f32_16x16x32_bf16 "
DEV void smf_a(f32x4& c, u32x4 a, u32x8 b, int idx) { asm volatile("v_smfmac_f32_16x16x64_bf16 %0, %1, %2, %3" : "+v"(c) : "v"(a), "a"(b), "v"(idx)); }
DEV void smf_v(f32x4& c, u32x4 a, u32x8 b, int idx) { asm volatile("v_smfmac_f32_16x16x64_bf16 %0, %1, %2, %3" : "+v"(c) : "v"(a), "v"(b), "v"(idx)); }
DEV void mf_vv(f32x4& c, u32x4 a, u32x4 b, bool first) { if (first) asm volatile(MF_ "%0, %1, %2, 0" : "=&v"(c) : "v"(a), "v"(b)); else asm volatile(MF_ "%0, %1, %2, %0" : "+v"(c) : "v"(a), "v"(b)); }
DEV void mf_av(f32x4& c, u32x4 a, u32x4 b, bool first) { if (first) asm volatile(MF_ "%0, %1, %2, 0" : "=&v"(c) : "a"(a), "v"(b)); else asm volatile(MF_ "%0, %1, %2, %0" : "+v"(c) : "a"(a), "v"(b)); }
DEV void mf_va(f32x4& c, u32x4 a, u32x4 b, bool first) { if (first) asm volatile(MF_ "%0, %1, %2, 0" : "=&v"(c) : "v"(a), "a"(b)); else asm volatile(MF_ "%0, %1, %2, %0" : "+v"(c) : "v"(a), "a"(b)); }
DEV void mf_done(f32x4& a) { asm volatile("s_nop 7" : "+v"(a)); }
DEV void mf_done(f32x4& a, f32x4& b) { asm volatile("s_nop 7" : "+v"(a), "+v"(b)); }
DEV void mf_done(f32x4& a, f32x4& b, f32x4& c) { asm volatile("s_nop 7" : "+v"(a), "+v"(b), "+v"(c)); }
DEV void mf_done(f32x4& a, f32x4& b, f32x4& c, f32x4& d) { asm volatile("s_nop 7" : "+v"(a), "+v"(b), "+v"(c), "+v"(d)); }
DEV float zero1() { float v; asm volatile("v_mov_b32 %0, 0" : "=v"(v)); return v; }
DEV void zero_acc(f32x4& a) { a = f32x4{zero1(), zero1(), zero1(), zero1()}; asm volatile("s_nop 1" : "+v"(a)); }
DEV LAS char* opq(LAS char* p) { unsigned u = (unsigned)(uintptr_t)p; asm volatile("" : "+v"(u)); return (LAS char*)(uintptr_t)u; }
template <int V> struct IC { static constexpr int value = V; constexpr operator int() const { return V; } };
template <int I, int N, class F> DEV void sfor(F&& f) { if constexpr (I < N) { f(IC<I>{}); sfor<I + 1, N>(f); } }
DEV void lgkm0() { asm volatile("s_waitcnt lgkmcnt(0)" ::: "memory"); __builtin_amdgcn_sched_barrier(0); }

struct Spin {
    gu32* tmo; bool dead, claimed; unsigned tag, slot;
    unsigned org, ev, cpf;
    DEV void entry(int wave, int lane) {
        const char* code; unsigned back, len;
        asm volatile("s_getpc_b64 %0\n.Lpc%=:\n\ts_mov_b32 %1, .Lpc%=-_Z6k_main6Params\n\ts_mov_b32 %2, .Lmain_end-_Z6k_main6Params" : "=s"(code), "=s"(back), "=s"(len));
        asm volatile("buffer_load_dword %0, %2, %3, 0 offen\n\tglobal_load_dword %1, %4, %5 sc1" : "=&v"(cpf), "=&v"(ev)
                     : "v"(((slot & 31u) * 256 + wave * 64 + lane) * 128), "s"(mk_rsrc(code - back, len & ~3u)), "v"(0), "s"(tmo - W_TMO + W_EPOCH) : "memory");
    }
    DEV void start() { asm volatile("v_readfirstlane_b32 %0, %1" : "=s"(tag) : "v"(ev), "v"(cpf)); tag += 1u; }
    u32x2 old; u32x4 ra, rb;
    DEV void early_claim(int wave, int lane) {
        start();
        old = u32x2{0u, 0u};
        if (wave == 0 && lane == 0 && !claimed)
            asm volatile("global_atomic_swap_x2 %0, %1, %2, %3 sc0 sc1" : "+v"(old) : "v"(slot * 16u), "v"(u32x2{tag, ~tag}), "s"(tmo - W_TMO + W_CLAIM) : "memory");
    }
    DEV void early_ready(int lane) {
        asm volatile("global_load_dwordx4 %0, %2, %4 sc1\n\tglobal_load_dwordx4 %1, %3, %4 sc1" : "=&v"(ra), "=&v"(rb) : "v"(lane * 128), "v"((64 + (lane & 15)) * 128), "s"(tmo - W_TMO + W_READY) : "memory");
    }
    DEV bool fail(unsigned& n, unsigned code) {
        asm volatile("" ::: "memory");
        if (dead) return true;
        ++n;
        if (n > SPINCAP) { __hip_atomic_store(tmo + 1, code, RLX_AGENT); __hip_atomic_store(tmo, tag, RLX_AGENT); dead = true; return true; }
        if ((n & 63u) == 0u && (unsigned)__builtin_amdgcn_readfirstlane(__hip_atomic_load(tmo, RLX_AGENT)) == tag) { dead = true; return true; }
        return false;
    }
};

template <int NT> DEV int img_rot(int rho) { return (NT & 1) ? 0 : (NT & 2) ? ((rho >> 2) & 1) : (NT & 4) ? ((rho >> 1) & 3) : (rho & 7); }
DEV int swap23(int r) { return (r & 3) | ((r >> 1) & 4) | ((r << 1) & 8) | (r & 16); }
template <int NT> struct WSrc {
    unsigned voff[NT]; rsrc_t r;
    DEV void init(const char* src, int lane, unsigned stride, int trot = 0) {
        r = mk_rsrc(src - 4096, 0xFFFFFFFFu);
#pragma unroll
        for (int i = 0; i < NT; ++i) {
            const int P = 64 * i + lane, rho = P / (2 * NT), cs = P % (2 * NT);
            const int tile = (((cs >> 1) ^ img_rot<NT>(rho)) + trot) % NT;
            voff[i] = (unsigned)swap23(rho) * stride + (unsigned)(2 * tile + (cs & 1)) * 16u + 4096u - (i & 3) * 1024u;
        }
    }
    DEV void issue(unsigned so, unsigned lds_dst) {
#define BL_(v, o, p) "buffer_load_dwordx4 " v ", %0, %1 offen offset:" o p " lds\n\t"
#define GRP_(p) \
            if (i + 4 <= NT) asm volatile("s_mov_b32 m0, %2\n\ts_nop 0\n\t" BL_("%3", "0", p) BL_("%4", "1024", p) BL_("%5", "2048", p) BL_("%6", "3072", p) :: "s"(r), "s"(so), "s"(d), "v"(voff[i]), "v"(voff[(i + 1) % NT]), "v"(voff[(i + 2) % NT]), "v"(voff[(i + 3) % NT]) : "memory"); \
            else if (i + 3 == NT) asm volatile("s_mov_b32 m0, %2\n\ts_nop 0\n\t" BL_("%3", "0", p) BL_("%4", "1024", p) BL_("%5", "2048", p) :: "s"(r), "s"(so), "s"(d), "v"(voff[i]), "v"(voff[(i + 1) % NT]), "v"(voff[(i + 2) % NT]) : "memory"); \
            else asm volatile("s_mov_b32 m0, %2\n\ts_nop 0\n\t" BL_("%3", "0", p) BL_("%4", "1024", p) :: "s"(r), "s"(so), "s"(d), "v"(voff[i]), "v"(voff[(i + 1) % NT]) : "memory");
#pragma unroll
        for (int i = 0; i < NT; i += 4) {
            const unsigned d = lds_dst + i * 1024;
            GRP_("")
        }
#undef GRP_
#undef BL_
    }
};
template <int NT, bool PAIR> struct WRd {
    static constexpr int RR = (NT & 1) ? 1 : (NT & 2) ? 2 : (NT & 4) ? 4 : 8, NB = PAIR ? RR / 2 : RR;
    LAS char* b[NB];
    DEV void init(LAS char* base, int lane) {
        const int g = lane >> 4, q = (lane >> 2) & 3, p = lane & 3, rho = q | ((g & 1) << 2) | ((g >> 1) << 4), rot = img_rot<NT>(rho);
#pragma unroll
        for (int j = 0; j < NB; ++j) b[j] = opq(base + rho * (NT * 32) + (PAIR ? ((2 * j + (p >> 1)) ^ rot) * 32 + 16 * (p & 1) : (j ^ rot) * 32 + 8 * p));
    }
    DEV LAS char* at(int off, int tile) const { return PAIR ? b[(tile >> 1) % NB] + off + (tile / RR) * RR * 32 + 8 * (tile & 1) : b[tile % NB] + off + (tile / RR) * RR * 32; }
    DEV u32x4 get(int off, int tile) const { LAS char* a = at(off, tile); return cat2(trd(a), trd(a + 8 * NT * 32)); }
};
DEV void vmwait_n(int n) {
    switch (n) {
#define VW(n) case n: vmwait<n>(); break;
        VW(0) VW(3) VW(4) VW(6) VW(8) VW(9) VW(10) VW(12) VW(15) VW(16) VW(18) VW(20) VW(24) VW(26) VW(28) VW(29) VW(30) VW(32) VW(35) VW(36) VW(40)
#undef VW
        default: vmwait<0>(); break;
    }
}
template <int NT, int NK, int NKT, int RING, int SROT, bool PAIR, class Sink>
DEV void stream_w(const char* src, unsigned stride, LAS char* lds, unsigned base, int lane, Sink&& sink, int rot = 0, int trot = 0, Spin* sp = nullptr, int wave = 0) {
    auto krow = [&](int k) { int kk = k + 2 * rot; if (kk >= NKT) kk -= NKT; return (unsigned)kk * 32u * stride; };
    if (sp) sp->entry(wave, lane);
    WSrc<NT> ws; ws.init(src, lane, stride, trot);
    const unsigned lbase = (unsigned)(uintptr_t)lds + base;
#pragma unroll
    for (int k = 0; k < RING && k < NKT; ++k) ws.issue(krow(k), lbase + ((k + SROT) % RING) * NT * 1024);
    WRd<NT, PAIR> rd; rd.init(lds + base, lane);
#pragma unroll
    for (int k = 0; k < NK; ++k) {
        const int issued = (k + RING < NKT) ? k + RING : NKT;
        vmwait_n((issued - k - 1) * NT);
        u32x4 fr[NT];
#pragma unroll
        for (int t = 0; t < NT; ++t) fr[t] = rd.get(((k + SROT) % RING) * NT * 1024, t);
#pragma unroll
        for (int t = 0; t < NT; ++t) sink(t, k, fr[t]);
        if (k + RING < NKT) { lgkm0(); ws.issue(krow(k + RING), lbase + ((k + SROT) % RING) * NT * 1024); }
        if (sp) { if (k == 0) sp->early_claim(wave, lane); if (k == NK - 2) sp->early_ready(lane); }
    }
}

template <int NT, int NS> DEV void repack(LAS char* base, int lane) {
    WRd<NT, false> rd; rd.init(base, lane);
#pragma unroll 1
    for (int k = 0; k < NS; ++k) {
        u32x4 fr[NT];
#pragma unroll
        for (int t = 0; t < NT; ++t) fr[t] = rd.get(k * NT * 1024, t);
        lgkm0();
#pragma unroll
        for (int t = 0; t < NT; ++t) *(LAS u32x4*)(base + (k * NT + t) * 1024 + lane * 16) = fr[t];
    }
}
DEV unsigned page(unsigned par, int s, int p) { const unsigned i = (unsigned)(s & (NSLOT - 1)) * 4u + (unsigned)p; return i * PAGE_B + (((par >> i) & 1u) << 13); }
DEV unsigned rec(unsigned par, int s) { return page(par, s, 0); }
DEV void wait_slot(rsrc_t rw, int s, unsigned& seen, int rep, Spin& sp) {
    for (unsigned c = 0; (int)seen + 6 < s;) {
        seen = __builtin_amdgcn_readfirstlane(ld32(rw, 0, (unsigned)(O_CTRL + 4 * (W_PROG + 32 * rep))));
        if ((int)seen + 6 >= s || sp.fail(c, 0x280 + s)) break;
        __builtin_amdgcn_s_sleep(8);
    }
}
template <bool EARLY_> DEV void claim(Spin& sp, volatile LAS unsigned* w, int wave, int lane) {
    if (EARLY_) asm volatile("" : "+v"(sp.old), "+v"(sp.ra), "+v"(sp.rb)); else { sp.entry(wave, lane); vmwait<0>(); sp.start(); }
    if (sp.claimed) return;
    if (wave == 0 && lane == 0) {
        const unsigned long long v = sp.tag | ((unsigned long long)~sp.tag << 32);
        unsigned slot = sp.slot;
        if (!EARLY_ || (sp.old.x == sp.tag && sp.old.y == ~sp.tag))
        for (int k = 0; k < 256; ++k, slot = (slot + 1u) & 255u)
            if (__hip_atomic_exchange((GAS unsigned long long*)(sp.tmo - W_TMO + W_CLAIM) + slot * 2u, v, RLX_AGENT) != v) break;
        *w = slot;
    }
    __syncthreads();
    const unsigned got = __builtin_amdgcn_readfirstlane(*w);
    __syncthreads();
    if (__builtin_expect(got != sp.slot, 0)) {
        asm volatile("s_waitcnt vmcnt(0) lgkmcnt(0)\n\tv_mov_b32 v0, %2\n\ts_mov_b32 s2, %1\n\ts_mov_b64 s[0:1], %0\n\t"
                     "s_getpc_b64 s[4:5]\n.Lre%=:\n\ts_sub_u32 s4, s4, .Lre%=-_Z6k_main6Params\n\ts_subb_u32 s5, s5, 0\n\ts_setpc_b64 s[4:5]"
                     :: "s"(__builtin_amdgcn_kernarg_segment_ptr()), "s"(got | 0x80000000u), "v"(wave * 64 + lane) : "memory", "s0", "s1", "s2", "s4", "s5", "v0");
        __builtin_unreachable();
    }
}
template <bool EARLY_ = false> DEV unsigned load_par(rsrc_t rw, int lane, Spin& sp) {
    u32x4 a, b;
    if (EARLY_) { a = sp.ra; b = sp.rb; }
    for (unsigned c = 0;;) {
        if (!EARLY_ || c) { a = ld128(rw, lane * 128, (unsigned)(O_CTRL + 4 * W_READY)); b = ld128(rw, (64 + (lane & 15)) * 128, (unsigned)(O_CTRL + 4 * W_READY)); }
        if (!any_lane(a.x != sp.tag || a.w != ~sp.tag || (a.y ^ (sp.tag << 1)) > 1u || b.x != sp.tag || b.w != ~sp.tag) || sp.fail(c, 0x40)) break;
        if (c > 1) __builtin_amdgcn_s_sleep(16);
    }
    unsigned m = (unsigned)__builtin_amdgcn_ballot_w64((a.y & 1u) != 0u);
    const int c = __builtin_popcount(m & 0xAAAAAAAAu) - __builtin_popcount(m & 0x55555555u);
    if (c >= 5) m = 0xAAAAAAAAu; else if (c <= -5) m = 0x55555555u;
    sp.org = __builtin_amdgcn_readfirstlane(a.z);
    return m ^ 0xEEEEEEEEu;
}
DEV unsigned sent1() { unsigned v; asm volatile("v_mov_b32 %0, -1" : "=v"(v)); return v; }
#define SENT4 (u32x4{sent1(), sent1(), sent1(), sent1()})
#define SENT2 (u32x2{sent1(), sent1()})
DEV unsigned rawnow() { unsigned long long v; asm volatile("s_memrealtime %0\n\ts_waitcnt lgkmcnt(0)" : "=s"(v) :: "memory"); return (unsigned)v; }
struct Clock {
    unsigned org;
    DEV unsigned now() const { return rawnow() - org; }
    DEV unsigned wait_until(unsigned T) const {
        unsigned t = rawnow(); T += org;
        if ((int)(T - t) > 1000) T = t + 1000u;
        while ((int)(t - T) < 0) t = rawnow();
        return t - org;
    }
    DEV unsigned wait_fine(unsigned T) const {
        unsigned t = rawnow(); T += org;
        if ((int)(T - t) > 1000) T = t + 1000u;
        if ((int)(t - T) >= -2) return t + 2u - org;
        const unsigned E = T - 4u;
        while ((int)(t - E) < -16) { __builtin_amdgcn_s_sleep(2); t = rawnow(); }
        while ((int)(t - E) < 0) t = rawnow();
        const unsigned rem = T - t;
        if (rem > 2) asm volatile("s_nop 7\n\ts_nop 1");
        if (rem & 1) return T - org;
        asm volatile("s_nop 4");
        return T - org;
    }
};
DEV unsigned umax_dpp(unsigned v, int k) {
    switch (k) { case 1: return max(v, (unsigned)__builtin_amdgcn_update_dpp(0u, v, 0x121, 0xf, 0xf, true)); case 2: return max(v, (unsigned)__builtin_amdgcn_update_dpp(0u, v, 0x122, 0xf, 0xf, true));
                 case 4: return max(v, (unsigned)__builtin_amdgcn_update_dpp(0u, v, 0x124, 0xf, 0xf, true)); default: return max(v, (unsigned)__builtin_amdgcn_update_dpp(0u, v, 0x128, 0xf, 0xf, true)); }
}
DEV unsigned half_max(unsigned v) {
    v = umax_dpp(umax_dpp(umax_dpp(umax_dpp(v, 1), 2), 4), 8);
    auto r = __builtin_amdgcn_permlane16_swap(v, v, false, false);
    return max((unsigned)r[0], (unsigned)r[1]);
}
DEV unsigned wave_max(unsigned v) { const unsigned m = half_max(v); return max((unsigned)__builtin_amdgcn_readlane(m, 0), (unsigned)__builtin_amdgcn_readlane(m, 32)); }
DEV u32x2 read_notes(u32x2 nf) {
    const unsigned tp = wave_max(nf.x == SENT ? 0u : nf.x), df = wave_max(nf.y == SENT ? 0u : min(nf.y, 400u));
    return u32x2{tp, tp + df + DELTA_B};
}
DEV u32x2 first_notes(rsrc_t rw, unsigned vn, unsigned so, Spin& sp) {
    u32x2 nf;
    for (unsigned c = 0;;) { nf = ld64(rw, vn, so); if (!any_lane(max(nf.x, nf.y) == SENT) || sp.fail(c, 0x50)) break; __builtin_amdgcn_s_sleep(2); }
    return nf;
}
DEV unsigned mem_time(unsigned tp, unsigned plan, unsigned tn) { return tn + VIS > tp ? max(plan, tn + SLOW) : plan; }
struct MemStream {
    rsrc_t rw; unsigned l16, vn, lbase, ko[10];
    static DEV int rot_of(int wave) { return (wave * 5 + 1) >> 1; }
    DEV void init(rsrc_t r, LAS char* lds, int lane, int rot, bool reader) {
        rw = r; l16 = lane * 16; vn = reader ? min(lane, 39) * 128 : (lane & 15) * 8; lbase = (unsigned)(uintptr_t)lds;
#pragma unroll
        for (int k = 0; k < 10; ++k) { int kk = k + rot; if (kk >= 10) kk -= 10; ko[k] = kk * 1024 + (kk >= 8 ? 8192 : 0); }
    }
    DEV u32x4 ld(unsigned rb, int kb) const { return ld128(rw, l16, rb + ko[kb]); }
    DEV unsigned so(unsigned rb, int kb) const { unsigned v = rb + ko[kb]; asm volatile("" : "+s"(v)); return v; }
    DEV u32x4 lds_(unsigned s) const { u32x4 v = ld128(rw, l16, s); __builtin_amdgcn_sched_barrier(0); return v; }
    DEV LAS char* at(int kb) const { unsigned a; asm volatile("v_add_u32 %0, %1, %2" : "=v"(a) : "s"(ko[kb] + lbase), "v"(l16)); return (LAS char*)(uintptr_t)a; }
    static DEV unsigned long long bad(u32x4 a, u32x4 b, u32x4 c, u32x4 d) {
        unsigned long long m = __builtin_amdgcn_ballot_w64(max(max(mx4(a), mx4(b)), max(mx4(c), mx4(d))) == SENT); asm volatile("" : "+s"(m)); return m;
    }
};
DEV unsigned long long nan2(float a, float b) { unsigned long long m; asm volatile("v_cmp_u_f32 %0, %1, %2" : "=s"(m) : "v"(a), "v"(b)); return m; }
DEV float add1(float a, float b) { float r; asm volatile("v_add_f32 %0, %1, %2" : "=v"(r) : "v"(a), "v"(b)); return r; }
DEV unsigned pk1(float a, float b) { unsigned r; asm volatile("v_cvt_pk_bf16_f32 %0, %1, %2" : "=v"(r) : "v"(a), "v"(b)); return r; }
DEV unsigned track(unsigned est, unsigned m, int s) { return m > est ? min(m, est + (s < 8 ? 8u : 3u)) : est - 1u - ((est - m) >> 2); }
struct AFrag {
    LAS char *p0, *p1;
    DEV void init(LAS char* img, int lane) {
        const int ga = lane >> 4, base = ga * 256 + (lane & 15) * 8, sw = (ga & 1) * 128;
        p0 = opq(img + base + sw); p1 = opq(img + base + (128 ^ sw));
    }
    DEV u32x4 get(int off) const { return cat2(trd(p0 + off), trd(p1 + off)); }
    static DEV int wofs(int lane, int odd) { const int g = lane >> 4, n = lane & 15; return (((2 * (n >> 2) + (n & 1)) * 32) ^ (odd * 128)) + g * 8 + ((n >> 1) & 1) * 2; }
};
DEV unsigned fold_pk(f32x4 c, float& f0, float& f1) {
    unsigned r;
    asm volatile("v_add_f32 %1, %3, %4\n\tv_add_f32 %2, %5, %6\n\tv_cvt_pk_bf16_f32 %0, %1, %2" : "=v"(r), "=&v"(f0), "=&v"(f1) : "v"(c[0]), "v"(c[1]), "v"(c[2]), "v"(c[3]));
    return r;
}
DEV void st2x16(LAS char* p0, LAS char* p1, int off, unsigned v) { *(LAS unsigned short*)(p0 + off) = (unsigned short)v; *(LAS unsigned short*)(p1 + off) = (unsigned short)(v >> 16); }

constexpr float SCALE_L2E = 0.07216878364870322f * 1.4426950408889634f;

DEV void role_S(KP& P, unsigned char* ws, int h, LAS char* lds, int wave, int lane, Spin& sp) {
    constexpr int L_QK = 8192, L_FL = 14336, L_W = 16384, L_WS = 36864, L_KX = L_W + L_WS, L_QX = L_W + 3 * L_WS;
    const rsrc_t rw = mk_rsrc(ws, (unsigned)WS_END);
    const int rot = MemStream::rot_of(wave);
    u32x8 w[6][8];
    {
        const char* src = (const char*)(wave < 2 ? P.wq : P.wk) + h * 384 + (wave & 1) * 192;
        stream_w<6, 16, 20, 6, 4, false>(src, 12288u, lds, L_W + wave * L_WS, lane,
            [&](int t, int k, u32x4 f) {
                if (k & 1) { w[t][k >> 1] = cat4(lo4(w[t][k >> 1]), f); if ((k >> 1) * 6 + t < 32) pin_a(w[t][k >> 1]); } else w[t][k >> 1] = cat4(f, f); }, rot, 0, &sp, wave);
        vmwait<0>();
    }
    bar();
    claim<true>(sp, (volatile LAS unsigned*)lds, wave, lane);
    const unsigned par = load_par<true>(rw, lane, sp);
    const int idx = (lane & 1) ? 0xEEEE : 0x4444;
    const int g = lane >> 4, n = lane & 15;
    const unsigned cq = (unsigned)(O_CTRL + 4 * (W_QKPROG + 32 * (((wave < 2 ? 384 : 0) + h * 12 + (lane & 1) * 11) >> 4)));
    unsigned have = 0;
    auto wait_qk = [&](int s) {
        if (have > (unsigned)s) return;
        for (unsigned c = 0;;) {
            const unsigned v = ld32(rw, cq, 0);
            have = min((unsigned)__builtin_amdgcn_readlane(v, 0), (unsigned)__builtin_amdgcn_readlane(v, 1));
            if (have > (unsigned)s || sp.fail(c, 0x200 + s)) break;
            __builtin_amdgcn_s_sleep(2);
        }
    };
    Clock ck; ck.org = sp.org;
    MemStream ms; ms.init(rw, lds, lane, rot, wave == 1);
    volatile LAS unsigned* lfail = (volatile LAS unsigned*)(lds + L_FL);
    if (wave == 0) *lfail = ~0u;
    repack<6, 4>(lds + L_W + wave * L_WS + 12288, lane);
    LAS char* wr = opq(lds + L_W + wave * L_WS + 12288 + lane * 16);
    AFrag af; af.init(lds + L_QK, lane);
    LAS char* we = opq(lds + L_QK + wave * 1536 + AFrag::wofs(lane, 0));
    LAS char* wo = opq(lds + L_QK + wave * 1536 + AFrag::wofs(lane, 1));
    const unsigned psel = (n & 1) ? 0x07060302u : 0x01000504u, psel2 = (n & 2) ? 0x07060302u : 0x01000504u;
    unsigned so1, so2;
    if (wave == 0) {
        const int i = 2 * g + (n & 1), half = (n >> 1) & 1;
        so1 = amap(R_P) + (h >> 4) * 16384 + (h & 15) * 512 + ((n >> 3) * 16 + 2 * i + half) * 16 + ((n >> 2) & 1) * 8;
        so2 = amap(R_P + 16384) + (h >> 1) * 256 + (2 * (2 * g + half) + ((n >> 2) & 1)) * 16 + (h & 1) * 8 + (n >> 3) * 4;
    } else {
        const unsigned b = amap(R_PX) + (wave & 1) * amap(PX_B);
        so1 = (wave & 1) ? b + (h >> 3) * 16384 + (h & 7) * 1024 + lane * 16 : b + (h >> 4) * 16384 + ((h >> 1) & 7) * 1024 + lane * 16 + (h & 1) * 8;
        so2 = b + 65536 + h * 256 + n * 16 + g * 4;
    }
    unsigned xo[3];
#pragma unroll
    for (int j = 0; j < 3; ++j) { const unsigned o = (wave < 2 ? R_KX : R_QX) + h * QX_B + j * 4096; xo[j] = (((o >> 13) << 14) | (o & 8191u)) + (wave < 2 ? 0 : (wave & 1) * 1024); }
    auto stage_k = [&](int s) __attribute__((always_inline)) {
        wait_qk(s);
#pragma unroll
        for (int j = 0; j < 3; ++j) glds_dev<4, 1024>(ms.l16, ws + (page(par, s, 1) + xo[j]), (unsigned)(uintptr_t)lds + L_KX + (s & 1) * L_WS + j * 4096);
    };
    auto stage_q = [&](int s) __attribute__((always_inline)) {
        wait_qk(s);
#pragma unroll
        for (int j = 0; j < 3; ++j) glds_dev<2, 2048>(ms.l16, ws + (page(par, s, 1) + xo[j]), (unsigned)(uintptr_t)lds + L_QX + (wave & 1) * 1024 + j * 4096);
    };
    auto lwh = [&](int j, int q) { return lds128(wr + (2 * (1 - j / 6) + q) * 6144 + (j % 6) * 1024); };
    unsigned dur = 139, tn = 0;
    if (wave == 0) __builtin_amdgcn_s_setprio(3);
    if (wave == 1) stage_k(0); else if (wave >= 2) stage_q(0);
    vmwait<0>();
    bar();
    if (wave == 1) stage_k(1);
    for (int s = 0; s < NSEG; ++s) {
        const unsigned rb = rec(par, s);
        f32x4 acc[6];
        u32x2 nf; u32x4 st[12][2], a[10], kxf[12];
        LAS const char* kx = opq(lds + L_KX + (s & 1) * L_WS + lane * 16);
        unsigned long long bad = 0;
        auto prep = [&]() __attribute__((always_inline)) {
#pragma unroll
            for (int t = 0; t < 6; ++t) zero_acc(acc[t]);
#pragma unroll
            for (int i = 0; i < 6; ++i) st[i >> 1][i & 1] = lwh(i >> 1, i & 1);
        };
        float fa[6], fb[6]; unsigned fp[6];
        auto fin = [&](int t, int step) __attribute__((always_inline)) {
            if (step == 0) fa[t] = add1(acc[t][0], acc[t][1]);
            if (step == 1) fb[t] = add1(acc[t][2], acc[t][3]);
            if (step == 2) fp[t] = pk1(fa[t], fb[t]);
            if (step == 3) *(LAS unsigned short*)(((t & 1) ? wo : we) + t * 256) = (unsigned short)fp[t];
            if (step == 4) *(LAS unsigned short*)(((t & 1) ? wo : we) + t * 256 + 4) = (unsigned short)(fp[t] >> 16);
        };
        unsigned long long bad2 = 0;
        unsigned qp;
        LAS char* ad[9];
        auto slot = [&](auto I) __attribute__((always_inline)) {
            constexpr int i = decltype(I)::value;
            if constexpr (i == 15) asm volatile("s_waitcnt lgkmcnt(4)\n\ts_barrier" ::: "memory");
            constexpr bool isl = i >= 6 && i <= 50 && (i & 3) == 2;
            constexpr int nl = i < 6 ? 0 : i > 50 ? 12 : (i - 6 + 3) / 4;
            if constexpr (isl) {
                smf_v(acc[nl % 6], a[9 - nl / 6], cat4(st[nl][0], st[nl][1]), idx);
            } else {
                constexpr int r = i - nl;
                constexpr int kb = r < 7 ? r / 6 : r == 7 ? 2 : r < 13 ? 1 : r < 24 ? r / 6 : r < 40 ? 4 + ((r - 24) % 8) / 2 : 4 + (r & 3);
                constexpr int t = r < 7 ? r % 6 : r == 7 ? 0 : r < 13 ? r - 7 : r < 24 ? r % 6 : r < 40 ? 2 * ((r - 24) / 8) + (r & 1) : 4 + (r - 40) / 4;
                if constexpr (kb * 6 + t < 32) smf_a(acc[t], a[kb], w[t][kb], idx); else smf_v(acc[t], a[kb], w[t][kb], idx);
            }

            if constexpr (i == 0) *(LAS u32x4*)ad[6] = a[0];
            if constexpr (i == 2) *(LAS u32x4*)ad[7] = a[1];
            if constexpr (i == 4) *(LAS u32x4*)ad[8] = a[2];
            if constexpr (i == 13) bad = nan2(acc[0][0], acc[0][1]);
            if constexpr (i == 13) bad2 = nan2(acc[0][2], acc[0][3]);
            if constexpr (i == 16) a[3] = lds128(ad[0]);
            if constexpr (i == 18) nf = ld64(rw, ms.vn, rb + amap(R_NF));
            if constexpr (i == 30) qp = ld32(rw, cq, 0);
            if constexpr (i == 20) a[8] = lds128(ad[1]);
            if constexpr (i == 22) a[4] = lds128(ad[2]);
            if constexpr (i == 24) a[5] = lds128(ad[3]);
            if constexpr (i == 26) a[6] = lds128(ad[4]);
            if constexpr (i == 28) a[7] = lds128(ad[5]);
            if constexpr (i >= 7 && i < 43 && (i & 1)) st[(i - 7) / 4 + 3][((i - 7) >> 1) & 1] = lwh((i - 7) / 4 + 3, ((i - 7) >> 1) & 1);
            if constexpr (i >= 43 && i < 55) kxf[i - 43] = lds128(kx + (i - 43) * 1024);
            if constexpr (i >= 44 && i < 49) fin(0, i - 44);
            if constexpr (i >= 47 && i < 52) fin(1, i - 47);
            if constexpr (i >= 55) { fin(2, i - 55); fin(3, i - 55); }
        };
        prep();
        ad[0] = ms.at(3); ad[1] = ms.at(8); ad[2] = ms.at(4); ad[3] = ms.at(5); ad[4] = ms.at(6); ad[5] = ms.at(7); ad[6] = ms.at(0); ad[7] = ms.at(1); ad[8] = ms.at(2);
        const unsigned o0 = ms.so(rb, 0), o9 = ms.so(rb, 9), o1 = ms.so(rb, 1), o2 = ms.so(rb, 2);
        const unsigned tg = ck.wait_fine(tn);
        a[0] = ms.lds_(o0); a[1] = ms.lds_(o1); a[2] = ms.lds_(o2); a[9] = ms.lds_(o9);
        sfor<0, 15>(slot);
        if (__builtin_expect((bad | bad2) != 0, 0)) {
            asm volatile("s_nop 7\n\ts_waitcnt vmcnt(0)" ::: "memory");
            *lfail = (unsigned)s;
            for (unsigned c = 0;;) {
                a[0] = ms.ld(rb, 0); a[9] = ms.ld(rb, 9); a[1] = ms.ld(rb, 1); a[2] = ms.ld(rb, 2);
                if (MemStream::bad(a[0], a[9], a[1], a[2]) == 0 || sp.fail(c, 0x100 + s)) break;
            }
            prep();
            sfor<0, 15>(slot);
            asm volatile("s_nop 7" : "+v"(acc[0]), "+v"(acc[1]), "+v"(acc[2]), "+v"(acc[3]), "+v"(acc[4]), "+v"(acc[5]));
        }
        sfor<15, 60>(slot);
        fin(4, 0); fin(4, 1); fin(4, 2); fin(4, 3); fin(4, 4);
        asm volatile("s_nop 0");
        fin(5, 0); fin(5, 1); fin(5, 2); fin(5, 3); fin(5, 4);
        f32x4 am, a0, a1;
        bar();
        u32x4 qa[3], ka[3];
        if (wave >= 2) {
            const int Tq = wave & 1;
            have = min((unsigned)__builtin_amdgcn_readlane(qp, 0), (unsigned)__builtin_amdgcn_readlane(qp, 1));
            u32x4 qx[6];
            asm volatile("s_nop 7\n\ts_nop 7" ::: "memory");
#pragma unroll
            for (int kb = 0; kb < 3; ++kb) ka[kb] = af.get(3072 + kb * 1024);
#pragma unroll
            for (int ep = 0; ep < 2; ++ep) qx[ep] = lds128(lds + L_QX + lane * 16 + Tq * 1024 + ep * 2048);
            zero_acc(am);
#pragma unroll
            for (int ep = 0; ep < 6; ++ep) {
                if (ep < 4) qx[ep + 2] = lds128(lds + L_QX + lane * 16 + Tq * 1024 + (ep + 2) * 2048);
                mf_vv(a0, kxf[2 * ep], qx[ep], ep == 0); mf_vv(a1, kxf[2 * ep + 1], qx[ep], ep == 0);
                if (ep & 1) smf_v(am, ka[ep >> 1], cat4(qx[ep - 1], qx[ep]), idx);
            }
            mf_done(am, a0, a1);
            float v[10];
            constexpr float SC = 0.07216878364870322f, L2E = 1.4426950408889634f;
#pragma unroll
            for (int r = 0; r < 4; ++r) {
                v[r] = (4 * g + r <= 16 * Tq + n) ? a0[r] * SC : -1e9f;
                v[4 + r] = (16 + 4 * g + r <= 16 * Tq + n) ? a1[r] * SC : -1e9f;
            }
            v[8] = (am[0] + am[1]) * SC; v[9] = (am[2] + am[3]) * SC;
            float mx = v[0];
#pragma unroll
            for (int r = 1; r < 10; ++r) mx = fmaxf(mx, v[r]);
            mx = x16(mx, true); mx = x32(mx, true);
            float sum = 0.f;
#pragma unroll
            for (int r = 0; r < 10; ++r) { v[r] = __builtin_amdgcn_exp2f((v[r] - mx) * L2E); sum += v[r]; }
            sum = x16(sum, false); sum = x32(sum, false);
            const float inv = __builtin_amdgcn_rcpf(sum);
            u32x4 ox = {pka(v[0] * inv, v[1] * inv), pka(v[2] * inv, v[3] * inv), pka(v[4] * inv, v[5] * inv), pka(v[6] * inv, v[7] * inv)};
            unsigned om = pka(v[8] * inv, v[9] * inv);
            asm volatile("s_barrier" : "+v"(ox), "+v"(om) :: "memory");
            if (Tq) st128(ox, rw, so1, rb); else st64(u32x2{ox.x, ox.y}, rw, so1, rb);
            st32(om, rw, so2, rb);
            if (s + 1 < NSEG) stage_q(s + 1);
            if (s + KARM < NSEG) { const unsigned ra = rec(par, s + KARM); if (Tq) st128(SENT4, rw, so1, ra); else st64(SENT2, rw, so1, ra); st32(sent1(), rw, so2, ra); }
        } else if (wave == 1) {
            have = min((unsigned)__builtin_amdgcn_readlane(qp, 0), (unsigned)__builtin_amdgcn_readlane(qp, 1));
            if (s + 2 < NSEG) stage_k(s + 2);
            if (s == 0) nf = first_notes(rw, ms.vn, rb + amap(R_NF), sp);
            { const u32x2 pl = read_notes(nf); lfail[2] = pl.x; lfail[3] = pl.y; }
            asm volatile("s_waitcnt lgkmcnt(0)\n\ts_barrier" ::: "memory");
        } else {
            if (s < NSEG - 1) {
#pragma unroll
                for (int kb = 0; kb < 3; ++kb) { qa[kb] = af.get(kb * 1024); __builtin_amdgcn_sched_barrier(0); ka[kb] = af.get(3072 + kb * 1024); __builtin_amdgcn_sched_barrier(0); }
                a0 = f32x4{zero1(), zero1(), zero1(), zero1()}; a1 = f32x4{zero1(), zero1(), zero1(), zero1()};
                asm volatile("s_nop 0" : "+v"(a0), "+v"(a1));
                auto sx = [&](int kb, int T) __attribute__((always_inline)) { smf_v(T ? a1 : a0, qa[kb], cat4(kxf[4 * kb + T], kxf[4 * kb + 2 + T]), idx); };
                sx(0, 0); mf_vv(am, qa[0], ka[0], true); sx(0, 1); mf_vv(am, qa[1], ka[1], false); sx(1, 0); mf_vv(am, qa[2], ka[2], false);
                sx(1, 1); sx(2, 0); sx(2, 1);
                float v[6];
                float ma, mb; u32x2 p01; unsigned p0, p1, p2;
#define DP " row_mask:0xf bank_mask:0xf\n\t"
#define RED(op, r) op " %6, %6, %6 row_ror:" r DP op " %7, %7, %7 row_ror:" r DP
                asm volatile("s_nop 4\n\t"
                    "v_add_f32_dpp %2, %12, %11 quad_perm:[1,1,3,3]" DP "v_add_f32_dpp %5, %14, %13 quad_perm:[1,1,3,3]" DP
                    "v_add_f32 %0, %15, %16\n\tv_add_f32 %3, %17, %18\n\t"
                    "v_mov_b32_dpp %2, %2 quad_perm:[0,0,2,2]" DP "v_mov_b32_dpp %5, %5 quad_perm:[0,0,2,2]" DP
                    "v_add_f32 %1, %19, %20\n\tv_add_f32 %4, %21, %22\n\t"
                    "v_max3_f32 %6, %0, %1, %2\n\tv_max3_f32 %7, %3, %4, %5\n\tv_mul_f32 %0, %23, %0\n\t"
                    RED("v_max_f32_dpp", "8") "v_mul_f32 %3, %23, %3\n\t" RED("v_max_f32_dpp", "4") "v_mul_f32 %1, %23, %1\n\t"
                    RED("v_max_f32_dpp", "2") "v_mul_f32 %4, %23, %4\n\t" RED("v_max_f32_dpp", "1")
                    "v_fma_f32 %0, -%6, %23, %0\n\tv_fma_f32 %3, -%7, %23, %3\n\tv_fma_f32 %1, -%6, %23, %1\n\tv_fma_f32 %4, -%7, %23, %4\n\t"
                    "v_exp_f32 %0, %0\n\tv_sub_f32 %2, %2, %6\n\tv_exp_f32 %3, %3\n\tv_sub_f32 %5, %5, %7\n\tv_exp_f32 %1, %1\n\tv_mul_f32 %2, %23, %2\n\tv_exp_f32 %4, %4\n\tv_mul_f32 %5, %23, %5\n\t"
                    "v_exp_f32 %2, %2\n\tv_add_f32 %6, %0, %1\n\tv_exp_f32 %5, %5\n\tv_add_f32 %7, %3, %4\n\t"
                    "v_fmac_f32 %6, 0.5, %2\n\tv_fmac_f32 %7, 0.5, %5\n\ts_nop 0\n\t"
                    RED("v_add_f32_dpp", "8") "s_nop 0\n\t" RED("v_add_f32_dpp", "4") "s_nop 0\n\t" RED("v_add_f32_dpp", "2") "s_nop 0\n\t" RED("v_add_f32_dpp", "1")
                    "v_rcp_f32 %6, %6\n\tv_rcp_f32 %7, %7\n\t"
                    "v_mul_f32 %0, %0, %6\n\tv_mul_f32 %3, %3, %7\n\tv_mul_f32 %1, %1, %6\n\tv_mul_f32 %4, %4, %7\n\t"
                    "v_cvt_pk_bf16_f32 %8, |%0|, |%3|\n\tv_cvt_pk_bf16_f32 %9, |%1|, |%4|\n\tv_mul_f32 %2, %2, %6\n\t"
                    "v_mov_b32_dpp %0, %8 quad_perm:[1,0,3,2]" DP "v_mov_b32_dpp %1, %9 quad_perm:[1,0,3,2]" DP "v_mul_f32 %5, %5, %7\n\t"
                    "v_cvt_pk_bf16_f32 %10, |%2|, |%5|\n\tv_perm_b32 %8, %8, %0, %24\n\tv_perm_b32 %9, %9, %1, %24"
                    : "=&v"(v[0]), "=&v"(v[1]), "=&v"(v[2]), "=&v"(v[3]), "=&v"(v[4]), "=&v"(v[5]), "=&v"(ma), "=&v"(mb), "=&v"(p0), "=&v"(p1), "=&v"(p2)
                    : "v"(am[0]), "v"(am[1]), "v"(am[2]), "v"(am[3]), "v"(a0[0]), "v"(a0[1]), "v"(a0[2]), "v"(a0[3]), "v"(a1[0]), "v"(a1[1]), "v"(a1[2]), "v"(a1[3]), "s"(SCALE_L2E), "v"(psel));
#undef RED
#undef DP
                p01 = u32x2{p0, p1};
                st64(p01, rw, so1, rb);
                st32(__builtin_amdgcn_perm(p2, __builtin_amdgcn_update_dpp(0u, p2, 0x4E, 0xf, 0xf, true), psel2), rw, so2, rb);
                tn = ck.now();
                if (lane == 0) st32(dur, rw, h * 128, rb + amap(R_NS));
                lfail[1] = tn;
                asm volatile("s_waitcnt lgkmcnt(0)\n\ts_barrier" ::: "memory");
                if (s > 0 && __builtin_amdgcn_readfirstlane(*lfail) != s) dur = track(dur, tn - tg, s);
                if (s + KARM < NSEG) { const unsigned ra = rec(par, s + KARM); st64(SENT2, rw, so1, ra); st32(sent1(), rw, so2, ra); if (lane == 0) st32(sent1(), rw, h * 128, ra + amap(R_NS)); }
            } else asm volatile("s_barrier" ::: "memory");
        }
        tn = mem_time(__builtin_amdgcn_readfirstlane(lfail[2]), __builtin_amdgcn_readfirstlane(lfail[3]), __builtin_amdgcn_readfirstlane(lfail[1]));
    }
    vmwait<0>();
}

constexpr int L_WO = 81920;
template <class SV, class SO>
DEV void load_vo(KP& P, int h, LAS char* lds, int wave, int lane, Spin& sp, SV&& sv, SO&& so_, int rot = 0) {
    auto krow = [&](int k) { int kk = k + 2 * rot; if (kk >= 20) kk -= 20; return (unsigned)kk * 32u * 12288u; };
    sp.entry(wave, lane);
    WSrc<3> wv; wv.init((const char*)P.wv + h * 384 + wave * 96, lane, 12288u);
    const unsigned lo = (unsigned)(uintptr_t)lds + L_WO + wave * 20480, lv = (unsigned)(uintptr_t)lds + wave * 20480;
#pragma unroll
    for (int k = 0; k < 6; ++k) wv.issue(krow(k), lv + k * 3072);
    WSrc<10> wo; wo.init((const char*)P.wo + (size_t)h * E_ * 1280 + wave * 320, lane, 1280u);
    wo.issue(0, lo); wo.issue(32 * 1280, lo + 10240);
    WRd<3, false> rv; rv.init(lds + wave * 20480, lane);
    WRd<10, false> ro; ro.init(lds + L_WO + wave * 20480, lane);
#pragma unroll
    for (int k = 0; k < 20; ++k) {
        if (k == 6 || k == 12) {
            const int ko = k / 3 - 2;
            vmwait<18>();
#pragma unroll
            for (int j = 0; j < 2; ++j) {
                u32x4 fo[10];
#pragma unroll
                for (int t = 0; t < 10; ++t) fo[t] = ro.get(j * 10240, t);
#pragma unroll
                for (int t = 0; t < 10; ++t) so_(t, ko + j, fo[t]);
            }
            lgkm0();
            wo.issue((ko + 2) * 32 * 1280, lo); wo.issue((ko + 3) * 32 * 1280, lo + 10240);
        }
        vmwait_n(k < 15 ? 35 : k < 18 ? 20 + (19 - k) * 3 : (19 - k) * 3);
        u32x4 fr[3];
#pragma unroll
        for (int t = 0; t < 3; ++t) fr[t] = rv.get((k % 6) * 3072, t);
#pragma unroll
        for (int t = 0; t < 3; ++t) sv(t, k, fr[t]);
        if (k + 6 < 20) { lgkm0(); wv.issue(krow(k + 6), lv + (k % 6) * 3072); }
        if (k == 0) sp.early_claim(wave, lane);
        if (k == 17) sp.early_ready(lane);
    }
    vmwait<0>();
    bar();
}

template <class SV, class SO>
DEV void load_vo_rows(KP& P, int h, LAS char* lds, int wave, int lane, Spin& sp, SV&& sv, SO&& so_) {
    sp.entry(wave, lane);
    WSrc<3> wv;
    wv.r = mk_rsrc((const char*)P.wv + h * 384 - 4096, 0xFFFFFFFFu);
#pragma unroll
    for (int i = 0; i < 3; ++i) {
        const int Pc = 64 * (3 * wave + i) + lane, rho = Pc / 24, cs = Pc % 24, tile = (cs >> 1) ^ img_rot<12>(rho);
        wv.voff[i] = (unsigned)swap23(rho) * 12288u + (unsigned)(2 * tile + (cs & 1)) * 16u + 4096u - i * 1024u;
    }
    const unsigned lo = (unsigned)(uintptr_t)lds + L_WO + wave * 20480, lv = (unsigned)(uintptr_t)lds + wave * 3072;
#pragma unroll
    for (int k = 0; k < 6; ++k) wv.issue(k * 32u * 12288u, lv + k * 12288);
    WSrc<10> wo; wo.init((const char*)P.wo + (size_t)h * E_ * 1280 + wave * 320, lane, 1280u);
    wo.issue(0, lo); wo.issue(32 * 1280, lo + 10240);
    LAS char* rv[3];
    { const int g = lane >> 4, q = (lane >> 2) & 3, p = lane & 3, rho = q | ((g & 1) << 2) | ((g >> 1) << 4);
#pragma unroll
      for (int t = 0; t < 3; ++t) rv[t] = opq(lds + rho * 384 + ((3 * wave + t) ^ img_rot<12>(rho)) * 32 + 8 * p); }
    WRd<10, false> ro; ro.init(lds + L_WO + wave * 20480, lane);
#pragma unroll
    for (int k = 0; k < 20; ++k) {
        if (k == 6 || k == 12) {
            const int ko = k / 3 - 2;
            vmwait_n(k == 6 ? 15 : 18);
#pragma unroll
            for (int j = 0; j < 2; ++j) {
                u32x4 fo[10];
#pragma unroll
                for (int t = 0; t < 10; ++t) fo[t] = ro.get(j * 10240, t);
#pragma unroll
                for (int t = 0; t < 10; ++t) so_(t, ko + j, fo[t]);
            }
            lgkm0();
            wo.issue((ko + 2) * 32 * 1280, lo); wo.issue((ko + 3) * 32 * 1280, lo + 10240);
        }
        vmwait_n(k == 0 ? 35 : k == 11 ? 12 : k < 16 ? 32 : k == 16 ? 29 : (19 - k) * 3);
        bar();
        if (k >= 1 && k + 5 < 20) wv.issue((k + 5) * 32u * 12288u, lv + ((k + 5) % 6) * 12288);
        u32x4 fr[3];
#pragma unroll
        for (int t = 0; t < 3; ++t) fr[t] = cat2(trd(rv[t] + (k % 6) * 12288), trd(rv[t] + (k % 6) * 12288 + 3072));
#pragma unroll
        for (int t = 0; t < 3; ++t) sv(t, k, fr[t]);
        if (k == 0) sp.early_claim(wave, lane);
        if (k == 17) sp.early_ready(lane);
    }
    vmwait<0>();
    bar();
}

DEV void role_UV(KP& P, unsigned char* ws, int h, LAS char* lds, int wave, int lane, Spin& sp) {
    const rsrc_t rw = mk_rsrc(ws, (unsigned)WS_END);
    const int rot = MemStream::rot_of(wave);
    u32x8 wv[3][10], wo[10][2];
    load_vo(P, h, lds, wave, lane, sp,
        [&](int t, int k, u32x4 f) { if (k & 1) { wv[t][k >> 1] = cat4(lo4(wv[t][k >> 1]), f); pin_a(wv[t][k >> 1]); } else wv[t][k >> 1] = cat4(f, f); },
        [&](int t, int k, u32x4 f) { if (k & 1) { wo[t][k >> 1] = cat4(lo4(wo[t][k >> 1]), f); if (k == 1 && t < 2) pin_a(wo[t][k >> 1]); } else wo[t][k >> 1] = cat4(f, f); }, rot);
    claim<true>(sp, (volatile LAS unsigned*)lds, wave, lane);
    const unsigned par = load_par<true>(rw, lane, sp);
    const int idx = (lane & 1) ? 0xEEEE : 0x4444;
    const int g = lane >> 4, n = lane & 15;
    MemStream ms; ms.init(rw, lds, lane, rot, wave == 0);
    volatile LAS unsigned* lfail = (volatile LAS unsigned*)(lds + 14336);
    if (wave == 0) *lfail = ~0u;
    bar();
    AFrag af; af.init(lds + 8192, lane);
    repack<10, 2>(lds + L_WO + wave * 20480, lane);
    LAS char* wr = opq(lds + L_WO + wave * 20480 + lane * 16);
    const unsigned uo = amap(R_UB) + (wave * 10 + (g & 1)) * 16384 + h * 256 + n * 16 + (g >> 1) * 8;
    LAS char* vw0 = opq(lds + 8192 + wave * 768 + AFrag::wofs(lane, wave & 1));
    LAS char* vw1 = opq(lds + 8192 + wave * 768 + AFrag::wofs(lane, ~wave & 1));
    auto lwh = [&](int t, int q) { return lds128(wr + q * 10240 + t * 1024); };
    Clock ck; ck.org = sp.org;
    unsigned dur = 126, tn = 0;
    for (int s = 0; s < NSEG; ++s) {
        const unsigned rb = rec(par, s);
        f32x4 acc[3], c[10];
        u32x4 st[10][2], a[10];
#pragma unroll
        for (int i = 0; i < 6; ++i) st[i >> 1][i & 1] = lwh(i >> 1, i & 1);
#pragma unroll
        for (int t = 0; t < 3; ++t) zero_acc(acc[t]);
#pragma unroll
        for (int t = 0; t < 4; ++t) zero_acc(c[t]);
        const unsigned o0 = ms.so(rb, 0), o1 = ms.so(rb, 1), o2 = ms.so(rb, 2), o3 = ms.so(rb, 3);
        LAS char* ad[10];
#pragma unroll
        for (int k = 0; k < 10; ++k) if (k != 3) ad[k] = ms.at(k);
        const unsigned tg = ck.wait_fine(tn);
        a[0] = ms.lds_(o0); a[1] = ms.lds_(o1); a[2] = ms.lds_(o2); a[3] = ms.lds_(o3);
        unsigned long long bad = 0, bad2 = 0;
        auto slot = [&](auto I) __attribute__((always_inline)) {
            constexpr int i = decltype(I)::value;
            constexpr int kb = i < 4 ? i : i < 12 ? (i - 4) / 2 : i / 3, t = i < 4 ? 0 : i < 12 ? 1 + (i & 1) : i % 3;
            if constexpr (i == 9) { asm volatile("s_waitcnt lgkmcnt(0)\n\ts_barrier" ::: "memory"); a[4] = lds128(ad[4]); a[5] = lds128(ad[5]); }
            smf_a(acc[t], a[kb], wv[t][kb], idx);
            if constexpr (i < 3) *(LAS u32x4*)ad[i] = a[i];
            if constexpr (i == 6) { asm volatile("s_nop 4"); bad = nan2(acc[0][0], acc[0][1]); bad2 = nan2(acc[0][2], acc[0][3]); }
            if constexpr (i >= 9 && i <= 18 && i % 3 == 0) a[3 + i / 3] = lds128(ad[3 + i / 3]);
            if constexpr (i >= 14 && i < 26) { constexpr int q = i - 14; c[4 + q / 4][q & 3] = zero1(); }
        };
        sfor<0, 9>(slot);
        if (__builtin_expect((bad | bad2) != 0, 0)) {
            asm volatile("s_nop 7\n\ts_waitcnt vmcnt(0)" ::: "memory");
            *lfail = (unsigned)s;
            for (unsigned c = 0;;) {
#pragma unroll
                for (int k = 0; k < 4; ++k) a[k] = ms.ld(rb, k);
                if (MemStream::bad(a[0], a[1], a[2], a[3]) == 0 || sp.fail(c, 0x100 + s)) break;
            }
#pragma unroll
            for (int t = 0; t < 3; ++t) zero_acc(acc[t]);
            sfor<0, 9>(slot);
            mf_done(acc[0], acc[1], acc[2]);
        }
        sfor<9, 30>(slot);
        asm volatile("s_nop 7" : "+v"(acc[0]), "+v"(acc[1]), "+v"(acc[2]), "+v"(c[4]), "+v"(c[5]), "+v"(c[6]));
#pragma unroll
        for (int t = 0; t < 3; ++t) { LAS char* p = t == 1 ? vw1 : vw0; float f0, f1; st2x16(p, p + 4, t * 256, fold_pk(acc[t], f0, f1)); }
#pragma unroll
        for (int t = 7; t < 10; ++t) { c[t] = f32x4{zero1(), zero1(), zero1(), zero1()}; pin_v(c[t]); }
        bar();
        u32x4 va[3];
#pragma unroll
        for (int kb = 0; kb < 3; ++kb) va[kb] = af.get(kb * 1024);
        float fa[10], fb[10]; unsigned o[10];
        sfor<0, 39>([&](auto I) __attribute__((always_inline)) {
            constexpr int i = decltype(I)::value, tp = i / 6, k = (i % 6) >> 1, t = 2 * tp + (i & 1);
            if constexpr (i < 30) {
                if constexpr (k == 0) { if constexpr (t < 2) smf_a(c[t], va[0], wo[t][0], idx); else smf_v(c[t], va[0], wo[t][0], idx); }
                else if constexpr (k == 1) smf_v(c[t], va[1], wo[t][1], idx);
                else smf_v(c[t], va[2], cat4(st[t][0], st[t][1]), idx);
                if constexpr (i >= 3 && i < 24 && i % 3 < 2) st[i / 3 + 2][i % 3] = lwh(i / 3 + 2, i % 3);
            }
            if constexpr (i >= 9) {
                constexpr int x = 2 * ((i - 9) / 6), y = x + 1, j = (i - 9) % 6;
                if constexpr (i == 33) asm volatile("s_nop 0");
                if constexpr (j == 0) { fa[x] = add1(c[x][0], c[x][1]); fb[x] = add1(c[x][2], c[x][3]); }
                if constexpr (j == 1) { fa[y] = add1(c[y][0], c[y][1]); fb[y] = add1(c[y][2], c[y][3]); }
                if constexpr (j == 2) { o[x] = pk1(fa[x], fb[x]); o[y] = pk1(fa[y], fb[y]); }
                if constexpr (j == 3) asm volatile("v_min_u32 %0, -2, %0\n\tv_min_u32 %1, -2, %1" : "+v"(o[x]), "+v"(o[y]));
                if constexpr (j == 4) asm volatile("s_nop 1\n\tv_permlane16_swap_b32 %0, %1" : "+v"(o[x]), "+v"(o[y]));
                if constexpr (j == 5) st64(u32x2{o[x], o[y]}, rw, uo, rb + x * 16384);
            }
        });
        tn = ck.now();
        if (wave == 0 && lane == 0) st32(dur, rw, (32 + h) * 128, rb + amap(R_NS));
        if (s > 0 && __builtin_amdgcn_readfirstlane(*lfail) != s) dur = track(dur, tn - tg, s);
        if (wave == 0) { const u32x2 pl = read_notes(s ? ld64(rw, ms.vn, rb + amap(R_NF)) : first_notes(rw, ms.vn, rb + amap(R_NF), sp)); lfail[1] = mem_time(pl.x, pl.y, tn); }
        bar();
        tn = __builtin_amdgcn_readfirstlane(lfail[1]);
        if (s + KARM < NSEG) {
            const unsigned ra = rec(par, s + KARM);
#pragma unroll
            for (int tp = 0; tp < 5; ++tp) st64(SENT2, rw, uo, ra + tp * 32768);
            if (wave == 0 && lane == 0) st32(sent1(), rw, (32 + h) * 128, ra + amap(R_NS));
        }
    }
}

DEV void role_VC(KP& P, unsigned char* ws, int h, int odd, LAS char* lds, int wave, int lane, Spin& sp) {
    constexpr int L_V = 40960;
    const rsrc_t rw = mk_rsrc(ws, (unsigned)WS_END);
    u32x4 wv[3][20], wo[10][4];
    load_vo_rows(P, h, lds, wave, lane, sp, [&](int t, int k, u32x4 f) { wv[t][k] = f; pin_a(wv[t][k]); }, [&](int t, int k, u32x4 f) { wo[t][k] = f; if (t < 2 && k < 2) pin_a(wo[t][k]); });
    const int g = lane >> 4, n = lane & 15;
    const char* xf = (const char*)ws + O_XF;
    LAS char* wr = opq(lds + L_WO + wave * 20480 + lane * 16);
    LAS char* l16 = opq(lds + lane * 16);
    auto issue_x = [&](int s) {
#pragma unroll
        for (int i = 0; i < 10; ++i) glds<0>(lane * 16, xf + (size_t)s * 40960 + (wave * 10 + i) * 1024, (unsigned)(uintptr_t)lds + (wave * 10 + i) * 1024);
    };
    claim<true>(sp, (volatile LAS unsigned*)lds, wave, lane);
    const unsigned par = load_par<true>(rw, lane, sp);
    issue_x(odd);
    repack<10, 2>(lds + L_WO + wave * 20480, lane);
    unsigned seen = 0;
    for (int s = odd; s < NSEG; s += 2) {
        vmwait<0>();
        bar();
        if (s >= 2 && wave == 0 && lane == 0) st32((unsigned)(s - 1), rw, (unsigned)(O_CTRL + 4 * (W_VCPROG + 32 * (32 * odd + h))), 0);
        f32x4 acc[3][2];
        u32x4 xb[40];
#pragma unroll
        for (int i = 0; i < 4; ++i) xb[i] = lds128(l16 + (i & 1) * 20480 + (i >> 1) * 1024);
#pragma unroll
        for (int i = 0; i < 40; ++i) {
#pragma unroll
            for (int t = 0; t < 3; ++t) {
                mf_av(acc[t][i & 1], wv[t][i >> 1], xb[i], i < 2);
                if (t == 0 && i + 4 < 40) xb[i + 4] = lds128(l16 + (i & 1) * 20480 + ((i + 4) >> 1) * 1024);
            }
        }
        mf_done(acc[0][0], acc[1][0], acc[2][0]); mf_done(acc[0][1], acc[1][1], acc[2][1]);
#pragma unroll
        for (int t = 0; t < 3; ++t)
#pragma unroll
            for (int T = 0; T < 2; ++T) {
                const int et = wave * 3 + t;
                const int off = (et >> 1) * 2048 + T * 1024 + ((2 * (et & 1) + (g >> 1)) * 16 + n) * 16 + (g & 1) * 8;
                *(LAS u32x2*)(lds + L_V + off) = u32x2{pk(acc[t][T][0], acc[t][T][1]), pk(acc[t][T][2], acc[t][T][3])};
            }
        wait_slot(rw, s, seen, h & 7, sp);
        bar();
        u32x4 va[6][2], st[10][2];
        f32x4 c[10][2];
#pragma unroll
        for (int k = 0; k < 6; ++k)
#pragma unroll
            for (int T = 0; T < 2; ++T) va[k][T] = lds128(l16 + L_V + k * 2048 + T * 1024);
        st[0][0] = lds128(wr); st[0][1] = lds128(wr + 10240);
        const char* xn = xf + (size_t)(s + 2 < NSEG ? s + 2 : s) * 40960 + wave * 10240;
        const unsigned xd = (unsigned)(uintptr_t)lds + wave * 10240;
        auto fin = [&](int t) {
            st128(u32x4{pk(c[t][0][0], c[t][0][1]), pk(c[t][0][2], c[t][0][3]), pk(c[t][1][0], c[t][1][1]), pk(c[t][1][2], c[t][1][3])}, rw,
                  (h >> 3) * 16384 + (h & 7) * 1024 + lane * 16, (page(par, s, 2 + (wave >> 1)) ^ (((h >> 3) & 1) << 13)) + ((wave & 1) * 10 + t) * 65536);
        };
#pragma unroll
        for (int t = 0; t < 10; ++t) {
#pragma unroll
            for (int k = 0; k < 6; ++k) {
                const u32x4 b = k < 4 ? wo[t][k & 3] : st[t][k & 1];
                if (t < 2 && k < 2) { mf_va(c[t][0], va[k][0], b, k == 0); mf_va(c[t][1], va[k][1], b, k == 0); }
                else { mf_vv(c[t][0], va[k][0], b, k == 0); mf_vv(c[t][1], va[k][1], b, k == 0); }
                if (k < 2 && t + 1 < 10) st[t + 1][k] = lds128(wr + k * 10240 + (t + 1) * 1024);
                if (k == 5 && t < 5) { glds<0>(lane * 16, xn + 2 * t * 1024, xd + 2 * t * 1024); }
                if (k == 2 && t < 5) { glds<1024>(lane * 16, xn + 2 * t * 1024, xd + 2 * t * 1024); }
                if (k == 3 && t >= 1) { pin_v(c[t - 1][0]); pin_v(c[t - 1][1]); fin(t - 1); }
            }
        }
        mf_done(c[9][0], c[9][1]);
        fin(9);
    }
    vmwait<0>();
    bar();
    if (wave == 0 && lane == 0) st32((unsigned)NSEG, rw, (unsigned)(O_CTRL + 4 * (W_VCPROG + 32 * (32 * odd + h))), 0);
}

DEV void role_QK(KP& P, unsigned char* ws, int cu, LAS char* lds, int wave, int lane, Spin& sp) {
    const rsrc_t rw = mk_rsrc(ws, (unsigned)WS_END);
    u32x4 w[4][20];
    const int sel = cu >= 24, c0 = (cu - 24 * sel) * 256 + wave * 64;
    stream_w<4, 20, 20, 10, 0, true>((const char*)(sel ? P.wk : P.wq) + c0 * 2, 12288u, lds, wave * 40960, lane,
                                 [&](int t, int k, u32x4 f) { w[t][k] = f; if (k < 16) pin_a(w[t][k]); }, 0, 0, &sp, wave);
    vmwait<0>();
    bar();
    const char* xf = (const char*)ws + O_XF;
    auto issue_x = [&](int s) {
#pragma unroll
        for (int i = 0; i < 10; ++i) glds<0>(lane * 16, xf + (size_t)s * 40960 + (wave * 10 + i) * 1024, (unsigned)(uintptr_t)lds + (s & 1) * 40960 + (wave * 10 + i) * 1024);
    };
    claim<true>(sp, (volatile LAS unsigned*)lds, wave, lane);
    const unsigned par = load_par<true>(rw, lane, sp);
    issue_x(0);
    const unsigned ol = (sel ? R_KX : R_QX) + (c0 >> 5) * 2048, ob = ((ol >> 13) << 14) | (ol & 8191u);
    unsigned seen = 0;
    for (int s = 0; s < NSEG; ++s) {
        vmwait<0>();
        bar();
        if (s >= 1 && wave == 0 && lane == 0) st32((unsigned)s, rw, (unsigned)(O_CTRL + 4 * (W_QKPROG + 32 * cu)), 0);
        wait_slot(rw, s, seen, cu & 7, sp);
        LAS char* l16 = opq(lds + (s & 1) * 40960 + lane * 16);
        f32x4 acc[4][2];
        u32x4 xb[40];
        unsigned pg;
#pragma unroll
        for (int i = 0; i < 4; ++i) xb[i] = lds128(l16 + (i & 1) * 20480 + (i >> 1) * 1024);
        const char* xn = xf + (size_t)(s + 1 < NSEG ? s + 1 : s) * 40960 + wave * 10240;
        const unsigned xd = (unsigned)(uintptr_t)lds + (~s & 1) * 40960 + wave * 10240;
#pragma unroll
        for (int i = 0; i < 40; ++i)
#pragma unroll
            for (int t = 0; t < 4; ++t) {
                if (t == 2 && i % 3 == 0 && i < 30) glds<0>(lane * 16, xn + (i / 3) * 1024, xd + (i / 3) * 1024);
                if (t == 2 && i == 31) pg = ld32(rw, 0, (unsigned)(O_CTRL + 4 * (W_PROG + 32 * (cu & 7))));
                if (i < 32) mf_av(acc[t][i & 1], w[t][i >> 1], xb[i], i < 2); else mf_vv(acc[t][i & 1], w[t][i >> 1], xb[i], false);
                if (t == 0 && i + 4 < 40) xb[i + 4] = lds128(l16 + (i & 1) * 20480 + ((i + 4) >> 1) * 1024);
            }
        mf_done(acc[0][0], acc[1][0], acc[2][0], acc[3][0]); mf_done(acc[0][1], acc[1][1], acc[2][1], acc[3][1]);
        seen = __builtin_amdgcn_readfirstlane(pg);
#pragma unroll
        for (int pr = 0; pr < 2; ++pr)
#pragma unroll
            for (int T = 0; T < 2; ++T) {
                const f32x4 a = acc[2 * pr][T], b = acc[2 * pr + 1][T];
                st128(u32x4{pk(a[0], a[1]), pk(a[2], a[3]), pk(b[0], b[1]), pk(b[2], b[3])}, rw, pr * 2048 + T * 1024 + lane * 16, page(par, s, 1) + ob);
            }
    }
    vmwait<0>();
    bar();
    if (wave == 0 && lane == 0) st32((unsigned)NSEG, rw, (unsigned)(O_CTRL + 4 * (W_QKPROG + 32 * cu)), 0);
}

DEV void role_F(KP& P, unsigned char* ws, int f, LAS char* lds, int wave, int lane, Spin& sp) {
    constexpr int L_FL = 131072, L_U = L_FL + 1024;
    const rsrc_t rw = mk_rsrc(ws, (unsigned)WS_END);
    volatile LAS unsigned* fl = (volatile LAS unsigned*)(lds + L_FL);
    if (wave == 0 && lane < 8) fl[lane] = lane == 5 ? ~0u : 0u;
    bar();
    const unsigned par = load_par(rw, lane, sp);
    const int g = lane >> 4, n = lane & 15;
    const unsigned l16 = lane * 16;
    const unsigned xr = ((f % 10) * 3) & 15;
    auto lwait = [&](int i, int need, unsigned code) {
        for (unsigned c = 0, k = 0; (int)__builtin_amdgcn_readfirstlane(fl[i]) < need;) { if ((++k & 63u) == 0u && sp.fail(c, code)) break; __builtin_amdgcn_s_sleep(1); }
        asm volatile("" ::: "memory");
    };
    auto lset = [&](int i, int v) { asm volatile("s_waitcnt lgkmcnt(0)" ::: "memory"); if (lane == 0) fl[i] = (unsigned)v; };
    if (wave == 3) {
        unsigned have0 = 0, have1 = 0;
        const unsigned vf = min(lane, 39) * 128;
        for (int s = 0; s < NSEG; ++s) {
            unsigned hv = (s & 1) ? have1 : have0;
            for (unsigned c = 0; hv <= (unsigned)s;) {
                const unsigned v = ld32(rw, (lane & 31) * 128, (unsigned)(O_CTRL + 4 * (W_VCPROG + 1024 * (s & 1))));
                if (!any_lane(v <= (unsigned)s)) {
                    hv = ~wave_max(~v);
                    break;
                }
                if (sp.fail(c, 0x300 + s)) break;
                __builtin_amdgcn_s_sleep(2);
            }
            if (s & 1) have1 = hv; else have0 = hv;
            lwait(2, s - 3, 0x341); lwait(3, s - 3, 0x342); lwait(1, s - 1, 0x340);
            const unsigned cpg = page(par, s, f < 20 ? 2 : 3) + (f % 20) * 65536;
            const unsigned no = rec(par, s - 1) + amap(R_NF);
            u32x2 nf;
            if (s) __builtin_amdgcn_s_sleep(4);
            asm volatile("buffer_load_dwordx2 %0, %1, %2, %3 offen sc1" : "=v"(nf) : "v"(vf), "s"(rw), "s"(no));
#pragma unroll
            for (int i = 0; i < 16; ++i) glds_dev<2, 1024>(l16, ws + ((cpg ^ ((((i ^ xr) >> 2) & 1) << 13)) + ((i ^ xr) >> 2) * 16384 + ((i ^ xr) & 3) * 2048), (unsigned)(uintptr_t)lds + (s & 3) * 32768 + i * 2048);
            asm volatile("s_waitcnt vmcnt(32)" : "+v"(nf));
            if (s >= 1) {
                for (unsigned c = 0; any_lane(max(nf.x, nf.y) == SENT);) { if (sp.fail(c, 0x390)) break; nf = ld64(rw, vf, no); }
                const u32x2 pl = read_notes(nf);
                if (lane == 0) { fl[6] = pl.x; fl[7] = pl.y; }
                lset(5, s - 1);
            }
            vmwait<0>();
            lset(0, s + 1);
        }
    } else if (wave == 0) {
        const int idx = (lane & 1) ? 0xEEEE : 0x4444;
        const unsigned mo = amap(R_MEM + f * 256) + (2 * (2 * g + (n & 1)) + ((n >> 1) & 1)) * 16 + (n >> 2) * 4;
        const unsigned psel = (n & 1) ? 0x07060302u : 0x01000504u;
        unsigned pv[4], ps[4];
#pragma unroll
        for (int j = 0; j < 4; ++j) { pv[j] = l16 + ((j ^ xr) & 3) * 1024; const unsigned k = j ^ (xr >> 2); ps[j] = amap(R_P) + (k & 1) * 4096 + (k >> 1) * 16384; }
        Clock ck; ck.org = sp.org;
        unsigned dur = 64, tp = 0, plan = 0, dS = 139, dU = 126;
        unsigned boost = 0, lead = 10;
        __builtin_amdgcn_s_setprio(3);
        for (int s = 0; s < NSEG - 1; ++s) {
            lwait(0, s + 1, 0x343);
            if (s == 0) { tp = ck.now() + 20u; plan = tp + dur + 30u + DELTA_B; if (lane == 0) st64(u32x2{tp, dur + 30u}, rw, f * 128, rec(par, 0) + amap(R_NF)); }
            LAS char* cb = opq(lds + (s & 3) * 32768 + l16);
            u32x8 bc[16];
#pragma unroll
            for (int t = 0; t < 16; ++t) bc[t] = cat4(lds128(cb + 2 * t * 1024), lds128(cb + (2 * t + 1) * 1024));
#pragma unroll
            for (int t = 0; t < 16; ++t) pin_a(bc[t]);
            const unsigned rb = rec(par, s), rn = rec(par, s + 1), ub = rb + amap(R_UB) + f * 16384, pb = rb + amap(R_P);
            unsigned so4[4];
#pragma unroll
            for (int j = 0; j < 4; ++j) { so4[j] = rb + ps[j]; asm volatile("" : "+s"(so4[j])); }
            if (s > 0) { lwait(5, s, 0x349); tp = __builtin_amdgcn_readfirstlane(fl[6]); plan = __builtin_amdgcn_readfirstlane(fl[7]); }
            const unsigned tu = tp - lead;
            unsigned res, rns = rn; asm volatile("" : "+s"(rns));
            u32x4 u[8], px[16], pm[4];
            f32x4 d; zero_acc(d);
            ck.wait_fine(tu);
#pragma unroll
            for (int i = 0; i < 8; ++i) u[i] = ld128(rw, l16 + (i & 3) * 1024, ub + (i >> 2) * 4096);
            const unsigned tg = ck.wait_fine(tp);
            bool failed = false;
            for (unsigned c = 0;;) {
#pragma unroll
                for (int i = 0; i < 16; ++i) px[i] = ld128(rw, pv[i & 3], so4[i >> 2]);
#pragma unroll
                for (int i = 0; i < 4; ++i) pm[i] = ld128(rw, l16 + i * 1024, pb + 32768);
#pragma unroll
                for (int i = 0; i < 16; ++i) { smf_a(d, px[i], bc[i], idx); }
#pragma unroll
                for (int i = 0; i < 4; ++i) smf_v(d, pm[i], cat4(u[2 * i], u[2 * i + 1]), idx);
                unsigned long long bad; float r0, r1;
                asm volatile("s_nop 7\n\tv_add_f32 %2, %4, %5\n\tv_add_f32 %3, %6, %7\n\tv_cvt_pk_bf16_f32 %0, %2, %3\n\tv_cmp_u_f32 %1, %2, %3\n\ts_nop 0\n\t"
                             "v_mov_b32_dpp %2, %0 quad_perm:[1,0,3,2] row_mask:0xf bank_mask:0xf\n\tv_perm_b32 %0, %0, %2, %8\n\tv_min_u32 %0, -2, %0"
                             : "=&v"(res), "=&s"(bad), "=&v"(r0), "=&v"(r1) : "v"(d[0]), "v"(d[1]), "v"(d[2]), "v"(d[3]), "v"(psel));
                if (__builtin_expect(bad == 0, 1)) { break; }
                if (c > 0) {
                    unsigned sg = 0; bool miss = false;
#pragma unroll
                    for (int i = 0; i < 8; ++i) miss |= mx4(u[i]) == SENT;
#pragma unroll
                    for (int i = 0; i < 16; ++i) sg |= px[i].x | px[i].y | px[i].z | px[i].w;
#pragma unroll
                    for (int i = 0; i < 4; ++i) sg |= pm[i].x | pm[i].y | pm[i].z | pm[i].w;
                    if (!any_lane(miss | ((sg & 0x80008000u) != 0u))) break;
                }
                if (sp.fail(c, 0x400 + s)) break;
                failed = true;
                zero_acc(d);
#pragma unroll
                for (int i = 0; i < 8; ++i) u[i] = ld128(rw, l16 + (i & 3) * 1024, ub + (i >> 2) * 4096);
            }
            st32(res, rw, mo, rns);
            const unsigned tpub = ck.now();
            const unsigned ns = ld32(rw, lane * 128, rb + amap(R_NS));
            const unsigned prop = (tpub + VIS <= plan ? plan : tpub + LATE) + max(dS, dU + 8u) + DELTA_A + (failed ? boost : 0u) + (s < NEARLY ? EARLY : 0u);
            boost = failed ? min(boost + 40u, 400u) : 0u;
            if (lane == 0) { st64(u32x2{prop, dur + (s < NEARLY ? EARLY / 2 : 0u)}, rw, f * 128, rn + amap(R_NF)); fl[1] = (unsigned)(s + 1); }
            if (!failed && tg - tp < 15u) dur = track(dur, tpub - tp, s);
            lwait(2, s - 1, 0x346); lwait(3, s - 1, 0x347);
#pragma unroll
            for (int i = 0; i < 8; ++i) *(LAS u32x4*)(lds + L_U + (s & 1) * 8192 + i * 1024 + l16) = u[i];
            lset(4, s + 1);
            tp = prop; plan = prop + dur + DELTA_B; lead = max(dS, dU + 8u) - dU;
            if (!any_lane(ns == SENT)) { const unsigned m = half_max(min(ns, 400u)); dS = __builtin_amdgcn_readlane(m, 0); dU = __builtin_amdgcn_readlane(m, 32); }
            if (f == 0 && lane < 8) st32((unsigned)(s + 1), rw, lane * 128, (unsigned)(O_CTRL + 4 * W_PROG));
            if (s + KARM < NSEG) { const unsigned ra = rec(par, s + KARM); st32(sent1(), rw, mo, ra); if (lane == 0) st64(SENT2, rw, f * 128, ra + amap(R_NF)); }
        }
        {
            const unsigned ub = rec(par, NSEG - 1) + amap(R_UB) + f * 16384;
            u32x4 u[8];
            ck.wait_until(tp);
            lset(1, NSEG);
            for (unsigned c = 0;;) {
                unsigned m = 0;
#pragma unroll
                for (int i = 0; i < 8; ++i) u[i] = ld128(rw, l16 + (i & 3) * 1024, ub + (i >> 2) * 4096);
#pragma unroll
                for (int i = 0; i < 8; ++i) m = max(m, mx4(u[i]));
                if (!any_lane(m == SENT) || sp.fail(c, 0x4FF)) break;
            }
            lwait(2, NSEG - 2, 0x346); lwait(3, NSEG - 2, 0x347);
#pragma unroll
            for (int i = 0; i < 8; ++i) *(LAS u32x4*)(lds + L_U + ((NSEG - 1) & 1) * 8192 + i * 1024 + l16) = u[i];
            lset(4, NSEG);
        }
    } else {
        auto xrows = [&](auto TQ) __attribute__((always_inline)) {
            constexpr int Tq = decltype(TQ)::value, NX = Tq ? 32 : 16;
            const rsrc_t rx = mk_rsrc(P.x, S_ * D_ * 2);
            const unsigned ho = (f >> 1) * 1024 + ((2 * (f & 1) + (g >> 1)) * 16 + n) * 16 + (g & 1) * 8;
            for (int s = 0; s < NSEG; ++s) {
                lwait(0, s + 1, 0x345);
                LAS char* cb = opq(lds + (s & 3) * 32768 + l16);
                u32x4 ca[NX];
#pragma unroll
                for (int i = 0; i < NX; ++i) ca[i] = Tq ? lds128(cb + i * 1024) : cat2(*(LAS const u32x2*)(cb + i * 2048), *(LAS const u32x2*)(cb + i * 2048 + 1024));
#pragma unroll
                for (int i = 0; i < NX; ++i) pin_a(ca[i]);
                const unsigned pb = rec(par, s) + amap(R_PX) + Tq * amap(PX_B);
                lwait(1, s + 1, 0x344);
                f32x4 d; u32x2 xv;
                for (unsigned c = 0;;) {
                    u32x4 u[8], px[NX + 8];
#pragma unroll
                    for (int i = 0; i < NX; ++i) {
                        if (Tq) { const unsigned k = (i >> 1) ^ xr; px[i] = ld128(rw, l16 + (i & 1) * 1024, pb + (k & 3) * 2048 + (k >> 2) * 16384); }
                        else { const unsigned k = i ^ xr; px[i] = ld128(rw, l16, pb + (k & 7) * 1024 + (k >> 3) * 16384); }
                    }
#pragma unroll
                    for (int i = 0; i < 8; ++i) px[NX + i] = ld128(rw, l16 + (i & 3) * 1024, pb + 65536 + (i >> 2) * 4096);
                    xv = __builtin_amdgcn_raw_buffer_load_b64(rx, ((32 * s + 16 * Tq + n) * D_ + 16 * f + 4 * g) * 2, 0, 0);
                    lwait(4, s + 1, 0x348);
#pragma unroll
                    for (int i = 0; i < 8; ++i) u[i] = lds128(lds + L_U + (s & 1) * 8192 + i * 1024 + l16);
#pragma unroll
                    for (int i = 0; i < NX; ++i) mf_av(d, ca[i], px[i], i == 0);
#pragma unroll
                    for (int i = 0; i < 8; ++i) mf_vv(d, u[i], px[NX + i], false);
                    mf_done(d);
                    if (!any_lane(__builtin_isunordered(d[0] + d[1], d[2] + d[3]))) { break; }
                    bool miss = false;
#pragma unroll
                    for (int i = 0; i < NX + 8; ++i) miss |= mx4(px[i]) == SENT;
                    if (!any_lane(miss)) break;
                    if (sp.fail(c, 0x500 + s)) break;
                }
                lset(2 + Tq, s + 1);
                const unsigned a01 = pk(d[0], d[1]), a23 = pk(d[2], d[3]);
                st64(u32x2{pks(bflo(xv.x) + bflo(a01), bfhi(xv.x) + bfhi(a01)), pks(bflo(xv.y) + bflo(a23), bfhi(xv.y) + bfhi(a23))}, rw, ho, (unsigned)(O_HF + (size_t)(2 * s + Tq) * XF_B));
                if (2 * (s + KARM) < UTAIL) st64(SENT2, rw, ho, (unsigned)(O_HF + (size_t)(2 * (s + KARM) + Tq) * XF_B));
            }
        };
        if (wave == 1) xrows(IC<0>{}); else xrows(IC<1>{});
    }
}

DEV float gelu_t(float x) {
    const float t = x * __builtin_fmaf(x * x, -0.10294324f, -2.30220820f);
    return x * __builtin_amdgcn_rcpf(1.f + __builtin_amdgcn_exp2f(t));
}

template <int NK> DEV bool any_sent(const u32x4 (&x)[NK]) {
    unsigned m = 0;
#pragma unroll
    for (int k = 0; k < NK; ++k) m = max(m, mx4(x[k]));
    return any_lane(m == SENT);
}
template <int U0, int NSET> DEV int next_unit(int u) { return u < U0 - 1 ? u + 1 : u >= U0 ? u + NSET : NSET == 4 ? U0 + 2 : NUNIT; }
template <int U0, int NSET> DEV int first_unit(int rep) { return rep == 0 ? 0 : rep < 3 ? U0 + rep - 1 : U0 + NSET - 1; }
template <int MODE>
DEV void role_W(KP& P, unsigned char* ws, int cb, int rep, LAS char* lds, int wave, int lane, Spin& sp) {
    constexpr int U0 = MODE == 1 ? U0_1 : MODE == 2 ? U0_2 : U0_3, NSET = 4;
    const rsrc_t rw = mk_rsrc(ws, (unsigned)WS_END);
    constexpr unsigned stride = MODE == 3 ? F_ * 2 : D_ * 2;
    const char* wsrc = (const char*)(MODE == 1 ? P.w1 : MODE == 2 ? P.w2 : P.w3) + (size_t)wave * 160 * stride + cb * 256;
    u32x4 w[8][5];
    stream_w<8, 5, 5, 5, 0, true>(wsrc, stride, lds, wave * 40960, lane, [&](int t, int k, u32x4 f) { w[t][k] = f; pin_a(w[t][k]); }, 0, 2 * wave + 2);
    vmwait<0>();
    bar();
    const int g = lane >> 4, n = lane & 15;
    const unsigned l16 = lane * 16;
    if (rep == 0) load_par<false>(rw, lane, sp);
    constexpr size_t O_IN = MODE == 1 ? O_HF : MODE == 2 ? O_GF : O_X1F;
    LAS char* wr[3];
#pragma unroll
    for (int j = 0; j < 3; ++j) wr[j] = opq(lds + ((wave + 1 + j) & 3) * 6144 + j * 2048 + l16);
    LAS char* rd = opq(lds + wave * 6144 + l16);
    LAS char* sw = opq(lds + 24576 + wave * 512 + lane * 8);
    LAS char* sr = opq(lds + 24576 + ((n >> 2) * 16 + n) * 8);
    u32x4 ones = {0x3F803F80u, 0x3F803F80u, 0x3F803F80u, 0x3F803F80u}; pin_v(ones);
    auto fetch1 = [&](int u, int k) __attribute__((always_inline)) { return ld128(rw, l16, (unsigned)(O_IN + (size_t)u * XF_B) + (wave * 5 + k) * 1024); };
    auto probe = [&](int u) __attribute__((always_inline)) { return ld32(rw, 1008, (unsigned)(O_IN + (size_t)u * XF_B) + (wave * 5 + 4) * 1024); };
    auto product = [&](const u32x4 (&xb)[5], auto&& side, int buf, f32x4& o0, f32x4& o1, float& S1, float& S2) __attribute__((always_inline)) {
        f32x4 acc[8], s1, s2;
        if (MODE != 2) {
#pragma unroll
            for (int k = 0; k < 5; ++k) { mf_vv(s1, ones, xb[k], k == 0); mf_vv(s2, xb[k], xb[k], k == 0); }
        }
        sfor<0, 40>([&](auto I) __attribute__((always_inline)) {
            constexpr int i = decltype(I)::value, t = 2 * (i / 10) + (i & 1), k = (i % 10) >> 1;
            mf_av(acc[t], w[t][k], xb[k], k == 0);
            if constexpr (i % 4 == 0) side(IC<i / 4>{});
            if constexpr (i >= 17 && (i % 10 == 7 || i % 10 == 8)) { constexpr int tw = 2 * (i / 10 - 1) + (i % 10 - 7); pin_v(acc[tw]); *(LAS f32x4*)(wr[tw >> 1] + buf * 28672 + (tw & 1) * 1024) = acc[tw]; }
            if constexpr (MODE != 2 && i == 20) {
                asm volatile("" : "+v"(s1), "+v"(s2));
                const float d = (n & 2) ? ((n & 1) ? s2[3] : s2[2]) : ((n & 1) ? s2[1] : s2[0]);
                *(LAS f32x2*)(sw + buf * 28672) = f32x2{s1[0], d};
            }
        });
        bar();
        f32x4 p[6]; f32x2 q[4];
#pragma unroll
        for (int j = 0; j < 6; ++j) p[j] = *(LAS const f32x4*)(rd + buf * 28672 + j * 1024);
        if (MODE != 2) {
#pragma unroll
            for (int j = 0; j < 4; ++j) q[j] = *(LAS const f32x2*)(sr + buf * 28672 + j * 512);
        }
        mf_done(acc[6], acc[7]);
        o0 = (acc[6] + p[0]) + (p[2] + p[4]); o1 = (acc[7] + p[1]) + (p[3] + p[5]);
        if (MODE != 2) { S1 = (q[0].x + q[1].x) + (q[2].x + q[3].x); S2 = (q[0].y + q[1].y) + (q[2].y + q[3].y); }
    };
    f32x4 cs0 = {}, cs1 = {};
    if (MODE != 2) {
        u32x4 o4[5] = {ones, ones, ones, ones, ones};
        asm volatile("s_nop 1" : "+v"(o4[0]), "+v"(o4[1]), "+v"(o4[2]), "+v"(o4[3]), "+v"(o4[4]));
        float a, b; product(o4, [](auto) {}, 1, cs0, cs1, a, b);
    }
    const int kso = 4 * cb + wave;
    const int grp = kso / 5, sh = grp == 0 ? 1 : grp == 1 ? -1 : grp == 2 ? 2 : -2;
    if (MODE == 1 && rep == 0) {
        const int td = sh > 0 ? n : S_ - 1 - n;
        if (n < (sh > 0 ? sh : -sh)) st128(u32x4{0u, 0u, 0u, 0u}, rw, (td >> 4) * (unsigned)XF_B + kso * 1024 + (g * 16 + (td & 15)) * 16, (unsigned)O_GF);
        vmwait<0>();
    }
    u32x4 xa[5], xc[5], hn; u32x2 sn;
    bool miss = true, ahead = false, quiet = false;
    auto extra = [&](int u) __attribute__((always_inline)) { if (MODE == 2) { hn = ld128(rw, l16, (unsigned)(O_HF + (size_t)u * XF_B) + kso * 1024); sn = ld64(rw, (16 * u + n) * 8, (unsigned)O_ST); } };
    auto unit = [&](int u, int buf, u32x4 (&xb)[5], u32x4 (&nx)[5]) __attribute__((always_inline)) {
        const int tok = 16 * u + n, un = next_unit<U0, NSET>(u), u2 = next_unit<U0, NSET>(un);
        for (unsigned c = 0; miss;) {
            while (!quiet) {
                const unsigned pv = probe(u), gv = u >= UFREE ? ld32(rw, 0, (unsigned)(O_CTRL + 4 * (W_PROG + 32 * (cb & 7)))) : 0u;
                quiet = __builtin_amdgcn_readfirstlane(gv) >= NSEG - 1;
                if (__builtin_amdgcn_readfirstlane(pv) != SENT || sp.fail(c, 0x600 + MODE * 0x100 + u)) break;
                __builtin_amdgcn_s_sleep(2);
            }
            __builtin_amdgcn_s_sleep(1);
#pragma unroll
            for (int k = 0; k < 5; ++k) xb[k] = fetch1(u, k);
            extra(u);
            miss = any_sent(xb);
            if (miss && sp.fail(c, 0x680 + u)) break;
        }
        asm volatile("s_nop 1" : "+v"(xb[0]), "+v"(xb[1]), "+v"(xb[2]), "+v"(xb[3]), "+v"(xb[4]));
        const bool early = ahead;
        unsigned pr = SENT;
        if ((early ? u2 : un) < NUNIT) pr = probe(early ? u2 : un);
        u32x4 hv = hn; u32x2 stt = sn;
        if (MODE == 2) asm volatile("" : "+v"(hv), "+v"(stt));
        f32x4 o0, o1; float S1, S2;
        product(xb, [&](auto J) __attribute__((always_inline)) {
            constexpr int j = decltype(J)::value;
            if constexpr (j < 5) { if (early) nx[j] = fetch1(un, j); }
            if constexpr (j == 5) { if (early) extra(un); }
        }, buf, o0, o1, S1, S2);
        if (!early) {
            ahead = __builtin_amdgcn_readfirstlane(pr) != SENT;
            if (ahead) {
#pragma unroll
                for (int k = 0; k < 5; ++k) nx[k] = fetch1(un, k);
                extra(un);
                pr = SENT;
                if (u2 < NUNIT) pr = probe(u2);
            }
        }
        auto settle = [&]() __attribute__((always_inline)) { __builtin_amdgcn_s_waitcnt(0x0F70); miss = !ahead || any_sent(nx); ahead = ahead && __builtin_amdgcn_readfirstlane(pr) != SENT; if (MODE == 2) asm volatile("" : "+v"(hn), "+v"(sn)); };
        if (MODE != 2) {
            const float mu = S1 * (1.f / D_), var = fmaxf(S2 * (1.f / D_) - mu * mu, 0.f);
            float al = __builtin_amdgcn_rsqf(var + 1e-5f);
            if (MODE == 1) {
                if (cb == 0 && wave == 0 && g == 0) st64(u32x2{nosent(f2u(mu)), nosent(f2u(al))}, rw, tok * 8, (unsigned)O_ST);
                al *= __builtin_amdgcn_rsqf(var * al * al + 1e-5f);
            }
            float y[8];
#pragma unroll
            for (int r = 0; r < 4; ++r) { y[r] = gelu_t(al * (o0[r] - mu * cs0[r])); y[4 + r] = gelu_t(al * (o1[r] - mu * cs1[r])); }
            u32x4 o = {pks(y[0], y[1]), pks(y[2], y[3]), pks(y[4], y[5]), pks(y[6], y[7])};
            pin_v(o);
            settle();
            if (MODE == 1) {
                const int td = tok + sh;
                const unsigned vo = (td >> 4) * (int)XF_B + kso * 1024 + (g * 16 + (td & 15)) * 16;
                if (td >= 0 && td < S_) st128(o, rw, vo, (unsigned)O_GF);
                if (td + 32 * KARM < 16 * UTAIL) st128(SENT4, rw, vo + 2 * KARM * (unsigned)XF_B, (unsigned)O_GF);
            } else {
                st128(o, rw, l16, (unsigned)(O_G3F + (size_t)u * G3_B) + kso * 1024);
                if (u + 2 * KARM < UTAIL) st128(SENT4, rw, l16, (unsigned)(O_G3F + (size_t)(u + 2 * KARM) * G3_B) + kso * 1024);
            }
        } else {
            for (unsigned c = 0; any_lane(max(mx4(hv), max(stt.x, stt.y)) == SENT);) {
                if (sp.fail(c, 0xA00 + u)) break;
                hv = ld128(rw, l16, (unsigned)(O_HF + (size_t)u * XF_B) + kso * 1024); stt = ld64(rw, tok * 8, (unsigned)O_ST);
            }
            const float mu = u2f(stt.x), al = u2f(stt.y);
            const u32x4 o = {pks((bflo(hv.x) - mu) * al + o0[0], (bfhi(hv.x) - mu) * al + o0[1]), pks((bflo(hv.y) - mu) * al + o0[2], (bfhi(hv.y) - mu) * al + o0[3]),
                             pks((bflo(hv.z) - mu) * al + o1[0], (bfhi(hv.z) - mu) * al + o1[1]), pks((bflo(hv.w) - mu) * al + o1[2], (bfhi(hv.w) - mu) * al + o1[3])};
            settle();
            st128(o, rw, l16, (unsigned)(O_X1F + (size_t)u * XF_B) + kso * 1024);
            if (u + 2 * KARM < UTAIL) st128(SENT4, rw, l16, (unsigned)(O_X1F + (size_t)(u + 2 * KARM) * XF_B) + kso * 1024);
        }
    };
    for (int u = first_unit<U0, NSET>(rep);;) {
        unit(u, 0, xa, xc); u = next_unit<U0, NSET>(u); if (u >= NUNIT) break;
        unit(u, 1, xc, xa); u = next_unit<U0, NSET>(u); if (u >= NUNIT) break;
    }
}

DEV void role_W4(KP& P, unsigned char* ws, int cb, int rep, LAS char* lds, int wave, int lane, Spin& sp) {
    constexpr int U0 = U0_4, NSET = 3;
    const rsrc_t rw = mk_rsrc(ws, (unsigned)WS_END);
    const rsrc_t ro = mk_rsrc(P.out, S_ * D_ * 2);
    u32x4 w[4][12];
    stream_w<4, 12, 12, 10, 0, false>((const char*)P.w4 + (size_t)wave * 384 * 1280 + cb * 128, 1280u, lds, wave * 40960, lane,
                                  [&](int t, int k, u32x4 f) { w[t][k] = f; pin_a(w[t][k]); }, 0, wave + 1);
    vmwait<0>();
    bar();
    const int g = lane >> 4, n = lane & 15;
    const unsigned l16 = lane * 16;
    if (rep == 0) load_par<false>(rw, lane, sp);
    const int ct = 4 * cb + wave;
    const unsigned po = (ct >> 1) * 1024 + ((2 * (ct & 1) + (g >> 1)) * 16 + n) * 16 + (g & 1) * 8;
    LAS char* wr[3];
#pragma unroll
    for (int j = 0; j < 3; ++j) wr[j] = opq(lds + ((wave + 1 + j) & 3) * 3072 + j * 1024 + l16);
    LAS char* rd = opq(lds + wave * 3072 + l16);
    auto fetch = [&](u32x4 (&x)[12], int u) __attribute__((always_inline)) {
#pragma unroll
        for (int k = 0; k < 12; ++k) x[k] = ld128(rw, l16 + (k & 3) * 1024, (unsigned)(O_G3F + (size_t)u * G3_B) + (wave * 12 + (k & ~3)) * 1024);
    };
    auto probe = [&](int u) __attribute__((always_inline)) { return ld32(rw, 1008, (unsigned)(O_G3F + (size_t)u * G3_B) + (wave * 12 + 11) * 1024); };
    u32x4 xa[12], xc[12]; u32x2 hn, xn;
    bool miss = true, ahead = false, quiet = false;
    auto extra = [&](int u) __attribute__((always_inline)) { hn = ld64(rw, po, (unsigned)(O_HF + (size_t)u * XF_B)); xn = ld64(rw, po, (unsigned)(O_X1F + (size_t)u * XF_B)); };
    auto unit = [&](int u, int buf, u32x4 (&xb)[12], u32x4 (&nx)[12]) __attribute__((always_inline)) {
        for (unsigned c = 0; miss;) {
            while (!quiet) {
                const unsigned pv = probe(u), gv = u >= UFREE ? ld32(rw, 0, (unsigned)(O_CTRL + 4 * (W_PROG + 32 * (cb & 7)))) : 0u;
                quiet = __builtin_amdgcn_readfirstlane(gv) >= NSEG - 1;
                if (__builtin_amdgcn_readfirstlane(pv) != SENT || sp.fail(c, 0xB00 + u)) break;
                __builtin_amdgcn_s_sleep(2);
            }
            __builtin_amdgcn_s_sleep(1);
            fetch(xb, u); extra(u);
            miss = any_sent(xb);
            if (miss && sp.fail(c, 0xB80 + u)) break;
        }
#pragma unroll
        for (int k = 0; k < 12; ++k) pin_v(xb[k]);
        asm volatile("s_nop 1");
        u32x2 hv = hn, xv = xn;
        const int un = next_unit<U0, NSET>(u), u2 = next_unit<U0, NSET>(un);
        unsigned pr = SENT;
        const bool early = ahead;
        if ((early ? u2 : un) < NUNIT) pr = probe(early ? u2 : un);
        asm volatile("" : "+v"(hv), "+v"(xv));
        f32x4 acc[4];
        sfor<0, 48>([&](auto I) __attribute__((always_inline)) {
            constexpr int i = decltype(I)::value, t = i / 12, k = i % 12;
            mf_av(acc[t], w[t][k], xb[k], k == 0);
            if constexpr (i % 3 == 0 && i < 36) { constexpr int kk = i / 3; if (early) nx[kk] = ld128(rw, l16 + (kk & 3) * 1024, (unsigned)(O_G3F + (size_t)un * G3_B) + (wave * 12 + (kk & ~3)) * 1024); }
            if constexpr (i == 35) { if (early) extra(un); }
            if constexpr (i == 36) { if (!early) { ahead = __builtin_amdgcn_readfirstlane(pr) != SENT; if (ahead) { fetch(nx, un); extra(un); pr = SENT; if (u2 < NUNIT) pr = probe(u2); } } }
            if constexpr (t >= 1 && k == 8) { pin_v(acc[t - 1]); *(LAS f32x4*)(wr[t - 1] + buf * 12288) = acc[t - 1]; }
        });
        bar();
        f32x4 p[3];
#pragma unroll
        for (int j = 0; j < 3; ++j) p[j] = *(LAS const f32x4*)(rd + buf * 12288 + j * 1024);
        mf_done(acc[3]);
        const f32x4 o = (acc[3] + p[0]) + (p[1] + p[2]);
        for (unsigned c = 0; any_lane(max(max(hv.x, hv.y), max(xv.x, xv.y)) == SENT);) {
            if (sp.fail(c, 0xC00 + u)) break;
            hv = ld64(rw, po, (unsigned)(O_HF + (size_t)u * XF_B)); xv = ld64(rw, po, (unsigned)(O_X1F + (size_t)u * XF_B));
        }
        const u32x2 r = {pk(bflo(hv.x) + (bflo(xv.x) + o[0]), bfhi(hv.x) + (bfhi(xv.x) + o[1])), pk(bflo(hv.y) + (bflo(xv.y) + o[2]), bfhi(hv.y) + (bfhi(xv.y) + o[3]))};
        __builtin_amdgcn_s_waitcnt(0x0F70);
        miss = !ahead || any_sent(nx); ahead = ahead && __builtin_amdgcn_readfirstlane(pr) != SENT;
        asm volatile("" : "+v"(hn), "+v"(xn));
        __builtin_amdgcn_raw_buffer_store_b64(r, ro, ((16 * u + n) * D_ + 16 * ct + 4 * g) * 2, 0, 0);
    };
    for (int u = first_unit<U0, NSET>(rep);;) {
        unit(u, 0, xa, xc); u = next_unit<U0, NSET>(u); if (u >= NUNIT) break;
        unit(u, 1, xc, xa); u = next_unit<U0, NSET>(u); if (u >= NUNIT) break;
    }
}

DEV unsigned char* ws_base(KP& P) { return P.ws + ((0u - (unsigned)(uintptr_t)P.ws) & ((2u << 20) - 1u)); }

DEV void publish_ready(rsrc_t rw, int i, unsigned near, int tid, Spin& sp) {
    vmwait<0>();
    bar();
    if (tid == 0) st128(u32x4{sp.tag, (sp.tag << 1) | near, rawnow() - 3000u, ~sp.tag}, rw, i * 128, (unsigned)(O_CTRL + 4 * W_READY));
}
DEV void init_F(KP& P, unsigned char* ws, int f, unsigned near, LAS char* lds, int wave, int lane, Spin& sp) {
    const rsrc_t rw = mk_rsrc(ws, (unsigned)WS_END);
    const int tid = wave * 64 + lane;
    LAS float* red = (LAS float*)lds;
    const int r = tid & 15, pc = tid >> 4;
    const rsrc_t rx = mk_rsrc(P.x, S_ * D_ * 2);
    u32x4 v[4][5];
#pragma unroll
    for (int j = 0; j < 4; ++j)
#pragma unroll
        for (int i = 0; i < 5; ++i) v[j][i] = __builtin_amdgcn_raw_buffer_load_b128(rx, (r * D_ + (pc + 16 * i) * 8) * 2, min(f + 40 * j, NUNIT - 1) * (16 * D_ * 2), 0);
    if (f < 3) {
        const int p = f * 256 + tid;
        if (p < 640) {
            const unsigned* src = (const unsigned*)(P.mem0 + ((p >> 1) & 7) * D_ + (p >> 4) * 16 + (p & 1) * 2);
            const u32x4 m = {nosent(src[0]), nosent(src[2]), nosent(src[4]), nosent(src[6])};
            const unsigned o = p * 16, oa = ((o >> 13) << 14) | (o & 8191u);
            st128(m, rw, oa, 0); st128(m, rw, oa, 8192);
        }
    }
    float mu[4];
#pragma unroll
    for (int j = 0; j < 4; ++j) {
        float s1 = 0.f;
#pragma unroll
        for (int i = 0; i < 5; ++i)
#pragma unroll
            for (int k = 0; k < 4; ++k) s1 += bflo(v[j][i][k]) + bfhi(v[j][i][k]);
        s1 = x32(x16(s1, false), false);
        red[(j * 2) * 64 + wave * 16 + r] = s1;
    }
    bar();
#pragma unroll
    for (int j = 0; j < 4; ++j) {
        const LAS float* q = red + (j * 2) * 64 + r;
        mu[j] = ((q[0] + q[16]) + (q[32] + q[48])) * (1.f / D_);
        float s2 = 0.f;
#pragma unroll
        for (int i = 0; i < 5; ++i)
#pragma unroll
            for (int k = 0; k < 4; ++k) { const float a = bflo(v[j][i][k]) - mu[j], c = bfhi(v[j][i][k]) - mu[j]; s2 += a * a + c * c; }
        s2 = x32(x16(s2, false), false);
        red[(j * 2 + 1) * 64 + wave * 16 + r] = s2;
    }
    bar();
#pragma unroll
    for (int j = 0; j < 4; ++j) {
        const LAS float* q = red + (j * 2 + 1) * 64 + r;
        const float rs = __builtin_amdgcn_rsqf(((q[0] + q[16]) + (q[32] + q[48])) * (1.f / D_) + 1e-5f);
        if (f + 40 * j < NUNIT) {
#pragma unroll
            for (int i = 0; i < 5; ++i) {
                u32x4 o;
#pragma unroll
                for (int k = 0; k < 4; ++k) o[k] = pk((bflo(v[j][i][k]) - mu[j]) * rs, (bfhi(v[j][i][k]) - mu[j]) * rs);
                st128(o, rw, (pc + 16 * i) * 256 + r * 16, (unsigned)(O_XF + (size_t)(f + 40 * j) * XF_B));
            }
        }
    }
    publish_ready(rw, f, near, tid, sp);
    bar();
}
DEV void init_arm(unsigned char* ws, int i, int tid, Spin& sp) {
    const rsrc_t rw = mk_rsrc(ws, (unsigned)WS_END);
    const int q = i * 256 + tid;
    constexpr int NT = 40 * 256;
    const u32x4 sv = SENT4;
    if (q < (W_READY - W_QKPROG) / 4) st128(u32x4{0u, 0u, 0u, 0u}, rw, q * 16, (unsigned)(O_CTRL + 4 * W_QKPROG));
    if (q < S_ / 2) st128(sv, rw, q * 16, (unsigned)O_ST);
    constexpr int RU = REC_BLOCKS * 1024;
    for (int r = 0; r < KARM; ++r)
        for (int j = q; j < RU; j += NT) if (r > 0 || j >= 2048) st128(sv, rw, j * 16, (unsigned)r * 4 * PAGE_B);
    constexpr int NA = (2 * KARM + 1) * (int)(XF_B / 16), NG = 2 * KARM * (int)(G3_B / 16);
    for (int j = q; j < NA; j += NT) { st128(sv, rw, j * 16, (unsigned)O_HF); st128(sv, rw, j * 16, (unsigned)O_GF); st128(sv, rw, j * 16, (unsigned)O_X1F); }
    for (int j = q; j < NG; j += NT) st128(sv, rw, j * 16, (unsigned)O_G3F);
    constexpr int TA = (NUNIT - UTAIL) * (int)(XF_B / 16), TG = (NUNIT - UTAIL) * (int)(G3_B / 16);
    for (int j = q; j < TA; j += NT) { st128(sv, rw, j * 16, (unsigned)(O_HF + UTAIL * XF_B)); st128(sv, rw, j * 16, (unsigned)(O_GF + UTAIL * XF_B)); st128(sv, rw, j * 16, (unsigned)(O_X1F + UTAIL * XF_B)); }
    for (int j = q; j < TG; j += NT) st128(sv, rw, j * 16, (unsigned)(O_G3F + UTAIL * G3_B));
    publish_ready(rw, 40 + i, 0u, tid, sp);
}

DEV KP* kargs() { KP* p = (KP*)__builtin_amdgcn_kernarg_segment_ptr(); asm volatile("" : "+s"(p)); return p; }
__global__ void __launch_bounds__(256, 1) k_main(Params) {
    extern __shared__ __attribute__((aligned(16))) char smem[];
    LAS char* lds = (LAS char*)smem;
    int lane = threadIdx.x & 63, wave = __builtin_amdgcn_readfirstlane(threadIdx.x >> 6);
    unsigned char* ws = ws_base(*kargs());
    unsigned bid = __builtin_amdgcn_workgroup_id_x(); asm volatile("" : "+s"(bid));
    const bool again = (bid >> 31) != 0u;
    const unsigned first = again ? bid & 255u : (xcc_id() & 7u) * 32u + ((bid >> 3) & 31u);
    if (first < 128u && (first & 31u) - 8u < 10u) {
        if (wave == 0) {
            const unsigned f = (first >> 5) * 10u + (first & 31u) - 8u;
            const rsrc_t rw = mk_rsrc(ws, (unsigned)WS_END);
            const unsigned o = (f & 31u) * PAGE_B + PAGE_B - 16384u + (f >> 5) * 2048;
            st32(1u, rw, (lane & 1) * 8192, o);
            vmwait<0>();
            unsigned lat[2] = {~0u, ~0u};
#pragma unroll
            for (int i = 0; i < 4; ++i) {
                const int hf = (i ^ (i >> 1)) & 1;
                unsigned long long t0, t1;
                asm volatile("s_memtime %0\n\ts_waitcnt lgkmcnt(0)" : "=s"(t0) :: "memory");
                st32(1u, rw, 0, o + hf * 8192);
                asm volatile("s_waitcnt vmcnt(0)\n\ts_memtime %0\n\ts_waitcnt lgkmcnt(0)" : "=s"(t1) :: "memory");
                lat[hf] = min(lat[hf], (unsigned)(t1 - t0));
            }
            ((LAS unsigned*)lds)[2] = lat[0]; ((LAS unsigned*)lds)[3] = lat[1];
        }
        __builtin_amdgcn_s_barrier();
    }
    Spin sp;
    gu32* ctrl = (gu32*)(ws + O_CTRL);
    sp.tmo = ctrl + W_TMO; sp.dead = false;
    __syncthreads();
    const unsigned near1 = __builtin_amdgcn_readfirstlane(((LAS unsigned*)lds)[3] < ((LAS unsigned*)lds)[2]);
    __syncthreads();
    sp.slot = first; sp.claimed = again;
    int mlp = -1, rep = 0;
    {
        KP& P = *kargs();
        const int xq = sp.slot >> 5, t = sp.slot & 31, y = xq - 4;
        if (xq < 4) {
            if (t < 8) role_S(P, ws, xq * 8 + t, lds, wave, lane, sp);
            else if (t < 18) { claim<false>(sp, (volatile LAS unsigned*)lds, wave, lane); init_F(P, ws, xq * 10 + (t - 8), near1, lds, wave, lane, sp); role_F(P, ws, xq * 10 + (t - 8), lds, wave, lane, sp); }
            else if (t < 26) role_UV(P, ws, xq * 8 + (t - 18), lds, wave, lane, sp);
            else mlp = xq * 6 + (t - 26);
        } else {
            if (t < 16) { role_VC(P, ws, y * 8 + (t >> 1), t & 1, lds, wave, lane, sp); mlp = (t >> 1) * 4 + y; rep = 1 + (t & 1); }
            else if (t < 28) { role_QK(P, ws, y * 12 + (t - 16), lds, wave, lane, sp); if (t < 24) { mlp = (t - 16) * 4 + y; rep = 3; } }
            else mlp = t < 30 ? 24 + y * 2 + (t - 28) : 32 + y * 2 + (t - 30);
        }
        if (xq < 4 ? t >= 26 : t >= 28) claim<false>(sp, (volatile LAS unsigned*)lds, wave, lane);
    }
    lane = __builtin_amdgcn_mbcnt_hi(~0u, __builtin_amdgcn_mbcnt_lo(~0u, 0u));
    asm volatile("" : "+v"(lane));
    const int tid = wave * 64 + lane;
    if (mlp >= 0) {
        KP& P = *kargs();
        if (rep == 0) {
            const unsigned t0 = rawnow();
            init_arm(ws, mlp, tid, sp);
            while (rawnow() - t0 < MLP_DELAY) __builtin_amdgcn_s_sleep(4);
        }
        if (mlp < 5) role_W<1>(P, ws, mlp, rep, lds, wave, lane, sp);
        else if (mlp < 10) role_W<2>(P, ws, mlp - 5, rep, lds, wave, lane, sp);
        else if (mlp < 22) role_W<3>(P, ws, mlp - 10, rep, lds, wave, lane, sp);
        else if (mlp < 32) role_W4(P, ws, mlp - 22, rep, lds, wave, lane, sp);
        if (mlp == 22 && rep == 0 && tid == 0) __hip_atomic_store(ctrl + W_EPOCH, sp.tag, RLX_AGENT);
    }
    asm volatile(".Lmain_end:");
}

extern "C" void kernel_launch(void* const* d_in, const int* in_sizes, int n_in, void* d_out, int out_size, void* d_ws, size_t ws_size, hipStream_t stream) {
    Params P;
    P.x = (const unsigned short*)d_in[0]; P.mem0 = (const unsigned short*)d_in[1];
    P.wq = (const unsigned short*)d_in[2]; P.wk = (const unsigned short*)d_in[3]; P.wv = (const unsigned short*)d_in[4]; P.wo = (const unsigned short*)d_in[5];
    P.w1 = (const unsigned short*)d_in[8]; P.w2 = (const unsigned short*)d_in[9]; P.w3 = (const unsigned short*)d_in[10]; P.w4 = (const unsigned short*)d_in[11];
    P.out = (unsigned short*)d_out; P.ws = (unsigned char*)d_ws;
    (void)hipFuncSetAttribute((const void*)k_main, hipFuncAttributeMaxDynamicSharedMemorySize, LDS_BYTES);
    hipLaunchKernelGGL(k_main, dim3(256), dim3(256), LDS_BYTES, stream, P);
}
```
